```python
import torch

def _softmax(x, axis=-1):
    return torch.softmax(x, dim=axis)
def _gelu(x):
    return torch.nn.functional.gelu(x, approximate="tanh")
def _silu(x):
    return torch.nn.functional.silu(x)
def _sigmoid(x):
    return torch.sigmoid(x)
def _layernorm(x, axis=-1, eps=1e-5):
    m = x.mean(dim=axis, keepdim=True)
    v = x.var(dim=axis, keepdim=True, unbiased=False)
    return (x - m) / torch.sqrt(v + eps)
def _scatter_add(tgt, idx, src, axis=0):
    return tgt.clone().index_add_(axis, idx, src)
def _segment_sum(src, seg, n):
    out = torch.zeros((n,) + src.shape[1:], dtype=src.dtype)
    return out.index_add_(0, seg, src)
def _pad(x, pw):
    flat = tuple(v for pair in reversed(pw) for v in pair)
    return torch.nn.functional.pad(x, flat)

kernel_name = 'gated_mix__ssm_conv_h3__mixer_channel_mlp__prompt'

def setup_inputs():

    _w = (lambda a: (lambda t: t.to(torch.bfloat16) if t.is_floating_point() else t)(torch.as_tensor(a)))
    def _A_setup():
        B, S, D = 1, 2048, 640
        ks = 4
        return {
            "q": _w((lambda *s: torch.randn(*s, dtype=torch.bfloat16) * 0.05)(B, S, D)),
            "k": _w((lambda *s: torch.randn(*s, dtype=torch.bfloat16) * 0.4)(B, S, D)),
            "v": _w((lambda *s: torch.randn(*s, dtype=torch.bfloat16) * 0.4)(B, S, D)),
            "shift_k": _w((lambda *s: torch.randn(*s, dtype=torch.bfloat16) * 0.4)(D, ks) * 0.5),
            "ssm_k": _w((lambda *s: torch.randn(*s, dtype=torch.bfloat16) * 1.6)(D, S) * (1.0 / S ** 0.5)),
            "ssm_D": _w((lambda *s: torch.randn(*s, dtype=torch.bfloat16) * 0.4)(D)),
        }
    _A_in = _A_setup()

    def _B_setup():
        B, S, D, F = 1, 2048, 640, 1536
        return {
            "x": _w((lambda *s: torch.randn(*s, dtype=torch.bfloat16) * 0.05)(B, S, D)),
            "w1": _w((lambda *s: torch.randn(*s, dtype=torch.bfloat16) * 0.8)(D, F) * (D ** -0.5)),
            "b1": _w((lambda *s: torch.randn(*s, dtype=torch.bfloat16) * 0.4)(F)),
            "w2": _w((lambda *s: torch.randn(*s, dtype=torch.bfloat16) * 0.05)(F, D) * (F ** -0.5)),
            "b2": _w((lambda *s: torch.randn(*s, dtype=torch.bfloat16) * 0.05)(D)),
        }
    _B_in = _B_setup()

    return {
        "A_q": _A_in["q"],
        "A_k": _A_in["k"],
        "A_v": _A_in["v"],
        "A_shift_k": _A_in["shift_k"],
        "A_ssm_k": _A_in["ssm_k"],
        "A_ssm_D": _A_in["ssm_D"],
        "B_x": _B_in["x"],
        "B_w1": _B_in["w1"],
        "B_b1": _B_in["b1"],
        "B_w2": _B_in["w2"],
        "B_b2": _B_in["b2"],
        "gate_w": _w((lambda *s: torch.randn(*s, dtype=torch.bfloat16) * 0.05)(640) * 16.0),
    }

def reference(A_q, A_k, A_v, A_shift_k, A_ssm_k, A_ssm_D, B_x, B_w1, B_b1, B_w2, B_b2, gate_w):
    def _A_ref(q, k, v, shift_k, ssm_k, ssm_D):
        B, S, D = q.shape
        ks = shift_k.shape[-1]
        k_pad = _pad(k, ((0, 0), (ks - 1, 0), (0, 0)))
        k_shift = torch.zeros_like(k)
        for t in range(ks):
            k_shift = k_shift + k_pad[:, t:t + S, :] * shift_k[:, ks - 1 - t][None, None, :]
        kv = k_shift * v
        L = 2 * S
        kv_pad = _pad(kv, ((0, 0), (0, S), (0, 0)))
        kf_pad = _pad(ssm_k, ((0, 0), (0, S)))
        kv_f = (lambda x, axes=(-2,-1): torch.fft.rfft2(x.float(), dim=axes))(kv_pad[:, None, :, :], axes=(1, 2))
        kf_f = (lambda x, axes=(-2,-1): torch.fft.rfft2(x.float(), dim=axes))(kf_pad[:, None, :], axes=(1, 2))
        kf_f = (lambda x, *d: x.permute(*d))(kf_f, 1, 2, 0)
        y_f = kv_f * kf_f[None, :, :, :]
        y = (lambda x, s, axes=(-2,-1): torch.fft.irfft2(x, s=s, dim=axes).to(torch.bfloat16))(y_f, (1, L), axes=(1, 2))[:, 0, :S, :]
        y = y + kv * ssm_D[None, None, :]
        return q * y

    def _B_ref(x, w1, b1, w2, b2):
        y = _layernorm(x, axis=-1)
        h = _gelu(torch.einsum("bsd,df->bsf", y, w1) + b1)
        return torch.einsum("bsf,fd->bsd", h, w2) + b2

    x = A_q
    g = _sigmoid((x * gate_w).sum(-1, keepdims=True))
    a = _A_ref(x, A_k, A_v, A_shift_k, A_ssm_k, A_ssm_D)
    b = _B_ref(x, B_w1, B_b1, B_w2, B_b2)
    return g * a + (1 - g) * b
```

```cpp
#include <hip/hip_runtime.h>
#include <hip/hip_bf16.h>
#include <stdint.h>

typedef unsigned short u16;
static constexpr int S = 2048, D = 640, F = 1536;

__device__ __forceinline__ float bf2f(u16 v) { return __uint_as_float(((unsigned)v) << 16); }
__device__ __forceinline__ u16 f2bf(float f) { __hip_bfloat16 b = __float2bfloat16(f); return *reinterpret_cast<u16*>(&b); }

__global__ void k_stats(const u16* q, const u16* gw, float* stats) {
    int t = blockIdx.x * 4 + (threadIdx.x >> 6), lane = threadIdx.x & 63;
    float v[10]; float s = 0, gs = 0;
    for (int i = 0; i < 10; ++i) { int c = lane + 64 * i; v[i] = bf2f(q[t * D + c]); s += v[i]; gs += v[i] * bf2f(gw[c]); }
    for (int m = 32; m; m >>= 1) { s += __shfl_xor(s, m); gs += __shfl_xor(gs, m); }
    float mean = s / D, vs = 0;
    for (int i = 0; i < 10; ++i) { float d = v[i] - mean; vs += d * d; }
    for (int m = 32; m; m >>= 1) vs += __shfl_xor(vs, m);
    float rstd = 1.f / sqrtf(vs / D + 1e-5f);
    if (lane == 0) { stats[t * 4] = mean; stats[t * 4 + 1] = rstd; stats[t * 4 + 2] = 1.f / (1.f + __expf(-gs)); }
}
__global__ void k_kv(const u16* k, const u16* v, const u16* sk, u16* kvT) {
    int idx = blockIdx.x * 256 + threadIdx.x; int t = idx / D, d = idx % D;
    float a = 0;
    for (int j = 0; j < 4; ++j) if (t - j >= 0) a += bf2f(k[(t - j) * D + d]) * bf2f(sk[d * 4 + j]);
    kvT[d * S + t] = f2bf(a * bf2f(v[t * D + d]));
}
__global__ void k_conv(const u16* kvT, const u16* h, const u16* Dv, u16* yT) {
    __shared__ float xs[S], hs[S];
    int d = blockIdx.x;
    for (int i = threadIdx.x; i < S; i += 256) { xs[i] = bf2f(kvT[d * S + i]); hs[i] = bf2f(h[d * S + i]); }
    __syncthreads();
    float dd = bf2f(Dv[d]);
    for (int t = threadIdx.x; t < S; t += 256) {
        float a = 0;
        for (int j = 0; j <= t; ++j) a += hs[j] * xs[t - j];
        yT[d * S + t] = f2bf(a + dd * xs[t]);
    }
}
__global__ void k_g1(const u16* q, const u16* w1, const u16* b1, const float* stats, u16* hh) {
    int idx = blockIdx.x * 256 + threadIdx.x; int t = idx / F, f = idx % F;
    float mean = stats[t * 4], rstd = stats[t * 4 + 1];
    float a = 0;
    for (int k = 0; k < D; ++k) a += (bf2f(q[t * D + k]) - mean) * rstd * bf2f(w1[k * F + f]);
    float u = a + bf2f(b1[f]);
    float z = u * (1.f + 0.044715f * u * u) * 1.5957691216057308f;
    hh[t * F + f] = f2bf(u / (1.f + __expf(-z)));
}
__global__ void k_g2(const u16* hh, const u16* w2, const u16* b2, const u16* q, const u16* yT, const float* stats, u16* out) {
    int idx = blockIdx.x * 256 + threadIdx.x; int t = idx / D, d = idx % D;
    float a = 0;
    for (int f = 0; f < F; ++f) a += bf2f(hh[t * F + f]) * bf2f(w2[f * D + d]);
    float b = a + bf2f(b2[d]);
    float g = stats[t * 4 + 2];
    float av = bf2f(q[t * D + d]) * bf2f(yT[d * S + t]);
    out[t * D + d] = f2bf(g * av + (1.f - g) * b);
}

extern "C" void kernel_launch(void* const* d_in, const int* in_sizes, int n_in,
                              void* d_out, int out_size, void* d_ws, size_t ws_size,
                              hipStream_t stream) {
    const u16* q = (const u16*)d_in[0]; const u16* k = (const u16*)d_in[1]; const u16* v = (const u16*)d_in[2];
    const u16* sk = (const u16*)d_in[3]; const u16* ssmk = (const u16*)d_in[4]; const u16* ssmD = (const u16*)d_in[5];
    const u16* w1 = (const u16*)d_in[7]; const u16* b1 = (const u16*)d_in[8]; const u16* w2 = (const u16*)d_in[9]; const u16* b2 = (const u16*)d_in[10];
    const u16* gw = (const u16*)d_in[11];
    char* ws = (char*)d_ws;
    float* stats = (float*)ws; u16* kvT = (u16*)(ws + 65536); u16* yT = kvT + S * D; u16* hh = yT + S * D;
    k_stats<<<S / 4, 256, 0, stream>>>(q, gw, stats);
    k_kv<<<S * D / 256, 256, 0, stream>>>(k, v, sk, kvT);
    k_conv<<<D, 256, 0, stream>>>(kvT, ssmk, ssmD, yT);
    k_g1<<<S * F / 256, 256, 0, stream>>>(q, w1, b1, stats, hh);
    k_g2<<<S * D / 256, 256, 0, stream>>>(hh, w2, b2, q, yT, stats, (u16*)d_out);
}
```

```cpp
#define CODE_BYTES 38040
#define PFA_T0 1
#define PFA_X 16
#define ROTM 3u
#define ROT2 0
#define RSC 1
#define TCHL 60
#define CSW 1
#define BND 1
#define G16 1
#define QL 1
#define LB 1
#define W2T 1
#include <hip/hip_runtime.h>
#include <stdint.h>
#include <utility>

#define DI __device__ __forceinline__
#define LAS __attribute__((address_space(3)))
typedef unsigned u32; typedef unsigned long long u64; typedef unsigned short u16;
typedef __bf16 bf16x2 __attribute__((ext_vector_type(2)));
typedef short s16x4 __attribute__((ext_vector_type(4)));
typedef float f32x4 __attribute__((ext_vector_type(4)));
typedef u32 u32x4 __attribute__((ext_vector_type(4)));
typedef u32 u32x2 __attribute__((ext_vector_type(2)));
typedef u32x4 rsrc_t;

static constexpr int S = 2048, D = 640, F = 1536;
static constexpr u32 WS_E = 0, WS_TMO = 64, WS_FP = 4096, WS_FW = 6144, WS_FH = 8192, WS_FY = 12288, WS_STATS = 16384,
                     WS_KVT = 65536, WS_YT = WS_KVT + D * S * 2, WS_H = WS_YT + D * S * 2, WS_W2T = WS_H + S * F * 2,
                     WS_END = WS_W2T + F * D * 2;
static constexpr u32 OOB = 0x80000000u;
static constexpr u32 WR = 0, WSL = 12288, XR = 61440, XSL = 16384;
static constexpr u32 P_K = XR + 4 * XSL, P_V = P_K + 11584, P_W2 = P_V + 11584, P_KVO = P_K;
static constexpr u32 SM_GW = 158976, SM_TAP = SM_GW + 2048, SM_B1 = SM_TAP + 1024, B1F = SM_B1 + 1024, P_CS = B1F + 384;
static constexpr u32 CPY = 4160, CXS = 4608, CXP = P_K, CF0 = CXP + 3 * CXS, CF1 = 0, CF2 = XR + 2 * XSL;
static constexpr u32 CX = XR, GX = 0, YT = XR + 20480;
static constexpr u32 B2L = P_W2 + 7744;
static_assert(B2L + 1024 <= SM_GW && CF0 + 4 * CPY <= B2L && P_CS + 384 == 163840 && P_W2 + 7744 <= SM_GW && CF0 + 4 * CPY <= SM_GW && YT + 11264 <= CF2, "lds map");
typedef float f32x2 __attribute__((ext_vector_type(2)));
typedef float f32x16 __attribute__((ext_vector_type(16)));
typedef u32 u32x16 __attribute__((ext_vector_type(16)));
typedef u32 u32x8 __attribute__((ext_vector_type(8)));
typedef u32 u32x32 __attribute__((ext_vector_type(32)));
struct Args { u32x4 dq, dk, dv, dw1, dw2, dsk, dws; const u16 *tap, *gw, *b1, *b2, *ssmD; u16* out; };
static_assert(sizeof(Args) == 160, "args");

template <int... I, class Fn> DI void sfor_impl(std::integer_sequence<int, I...>, Fn&& f) { (f(std::integral_constant<int, I>{}), ...); }
template <int N, class Fn> DI void sfor(Fn&& f) { sfor_impl(std::make_integer_sequence<int, N>{}, f); }

DI float bflo(u32 x) { return __uint_as_float(x << 16); }
DI float bfhi(u32 x) { return __uint_as_float(x & 0xffff0000u); }
DI u32 pk2(float lo, float hi) { bf16x2 v = {(__bf16)lo, (__bf16)hi}; return __builtin_bit_cast(u32, v); }
DI float dot2(u32 a, u32 b, float c) { return __builtin_amdgcn_fdot2_f32_bf16(__builtin_bit_cast(bf16x2, a), __builtin_bit_cast(bf16x2, b), c, false); }
DI u32x2 trd(LAS char* p) { s16x4 v = __builtin_amdgcn_ds_read_tr16_b64_v4i16((LAS s16x4*)p); return __builtin_bit_cast(u32x2, v); }
DI u32x4 MK(u32x2 a, u32x2 b) { return __builtin_shufflevector(a, b, 0, 1, 2, 3); }
DI int rfl(int x) { return __builtin_amdgcn_readfirstlane(x); }
template <int CTRL> DI float dpp(float x) { return __builtin_bit_cast(float, __builtin_amdgcn_update_dpp(0, __builtin_bit_cast(int, x), CTRL, 0xf, 0xf, true)); }
DI float red32(float x) {
    x += dpp<0x128>(x); x += dpp<0x124>(x); x += dpp<0x122>(x); x += dpp<0x121>(x);
    auto s = __builtin_amdgcn_permlane16_swap(__float_as_uint(x), __float_as_uint(x), false, false);
    return __uint_as_float(s[0]) + __uint_as_float(s[1]);
}
DI void spin_flags(rsrc_t rws, u32 voff, u32 elo, u32 ehi, u32 code) {
    for (u32 it = 0;; ++it) {
        u32x2 v;
        asm volatile("buffer_load_dwordx2 %0, %1, %2, 0 offen sc1\n\ts_waitcnt vmcnt(0)" : "=v"(v) : "v"(voff), "s"(rws) : "memory");
        const u32 a = v[0], b = v[1];
        if (__all(a == elo && b == ehi)) break;
        if (it > (1u << 18)) { asm volatile("buffer_store_dword %0, %1, %2, 0 offen sc1" ::"v"(code), "v"(WS_TMO), "s"(rws) : "memory"); break; }
        __builtin_amdgcn_s_sleep(2);
    }
    asm volatile("s_waitcnt vmcnt(0)" ::: "memory");
}

#define ACLOB "a0", "a1", "a2", "a3", "a4", "a5", "a6", "a7", "a8", "a9", "a10", "a11", "a12", "a13", "a14", "a15", "a16", "a17", "a18", "a19", "a20", "a21", "a22", "a23", "a24", "a25", "a26", "a27", "a28", "a29", "a30", "a31", "a32", "a33", "a34", "a35", "a36", "a37", "a38", "a39", "a40", "a41", "a42", "a43", "a44", "a45", "a46", "a47", "a48", "a49", "a50", "a51", "a52", "a53", "a54", "a55", "a56", "a57", "a58", "a59", "a60", "a61", "a62", "a63", "a64", "a65", "a66", "a67", "a68", "a69", "a70", "a71", "a72", "a73", "a74", "a75", "a76", "a77", "a78", "a79", "a80", "a81", "a82", "a83", "a84", "a85", "a86", "a87", "a88", "a89", "a90", "a91", "a92", "a93", "a94", "a95", "a96", "a97", "a98", "a99", "a100", "a101", "a102", "a103", "a104", "a105", "a106", "a107", "a108", "a109", "a110", "a111", "a112", "a113", "a114", "a115", "a116", "a117", "a118", "a119", "a120", "a121", "a122", "a123", "a124", "a125", "a126", "a127", "a128", "a129", "a130", "a131", "a132", "a133", "a134", "a135", "a136", "a137", "a138", "a139", "a140", "a141", "a142", "a143", "a144", "a145", "a146", "a147", "a148", "a149", "a150", "a151", "a152", "a153", "a154", "a155", "a156", "a157", "a158", "a159", "a160", "a161", "a162", "a163", "a164", "a165", "a166", "a167", "a168", "a169", "a170", "a171", "a172", "a173", "a174", "a175", "a176", "a177", "a178", "a179", "a180", "a181", "a182", "a183", "a184", "a185", "a186", "a187", "a188", "a189", "a190", "a191", "a192", "a193", "a194", "a195", "a196", "a197", "a198", "a199", "a200", "a201", "a202", "a203", "a204", "a205", "a206", "a207", "a208", "a209", "a210", "a211", "a212", "a213", "a214", "a215", "a216", "a217", "a218", "a219", "a220", "a221", "a222", "a223", "a224", "a225", "a226", "a227", "a228", "a229", "a230", "a231", "a232", "a233", "a234", "a235", "a236", "a237", "a238", "a239"
#define WAITV(n) asm volatile("s_waitcnt vmcnt(" #n ")" ::: "memory")
#define WAITL(n) asm volatile("s_waitcnt lgkmcnt(" #n ")")
#define WAITLB(n) __builtin_amdgcn_s_waitcnt(0xc07f | ((n) << 8))
#define BAR() asm volatile("s_barrier" ::: "memory")
#define BARL() asm volatile("s_waitcnt lgkmcnt(0)\n\ts_barrier" ::: "memory")
#define SCHED() __builtin_amdgcn_sched_barrier(0)
DI void setm0(u32 x) { asm volatile("s_mov_b32 m0, %0\n\ts_nop 0" ::"s"(x)); }
template <u32 IMM> DI void addm0(u32 x) { asm volatile("s_add_u32 m0, %0, %1" ::"s"(x), "i"(IMM) : "scc"); }
DI void mf32(f32x16& c, u32x4 a, u32x4 b) { asm volatile("v_mfma_f32_32x32x16_bf16 %0, %1, %2, %0" : "+v"(c) : "v"(a), "v"(b)); }
DI void mf320(f32x16& c, u32x4 a, u32x4 b) { asm volatile("v_mfma_f32_32x32x16_bf16 %0, %1, %2, 0" : "=&v"(c) : "v"(a), "v"(b)); }
template <int OFF> DI void dsr64(u32x2& d, u32 addr) { asm volatile("ds_read_b64 %0, %1 offset:%2" : "=v"(d) : "v"(addr), "i"(OFF)); }
template <int OFF> DI void dsr32(u32& d, u32 addr) { asm volatile("ds_read_b32 %0, %1 offset:%2" : "=v"(d) : "v"(addr), "i"(OFF)); }
template <int OFF> DI void dsrf128(f32x4& d, u32 addr) { asm volatile("ds_read_b128 %0, %1 offset:%2" : "=v"(d) : "v"(addr), "i"(OFF)); }
template <int OFF> DI void dsw64(u32 addr, u32x2 val) { asm volatile("ds_write_b64 %0, %1 offset:%2" ::"v"(addr), "v"(val), "i"(OFF)); }
DI void dot0(float& d, u32 a, u32 b) { asm volatile("v_dot2_f32_bf16 %0, %1, %2, 0" : "=v"(d) : "v"(a), "v"(b)); }
DI void dota(float& d, u32 a, u32 b) { asm volatile("v_dot2_f32_bf16 %0, %1, %2, %0" : "+v"(d) : "v"(a), "v"(b)); }
DI void dots(float& d, u32 a, u32 b) { asm volatile("v_dot2_f32_bf16 %0, %1, %2, %0" : "+v"(d) : "v"(a), "s"(b)); }
DI void dot0s(float& d, u32 a, u32 b) { asm volatile("v_dot2_f32_bf16 %0, %1, %2, 0" : "=v"(d) : "v"(a), "s"(b)); }
DI void algn(u32& d, u32 hi, u32 lo) { asm volatile("v_alignbit_b32 %0, %1, %2, 16" : "=v"(d) : "v"(hi), "v"(lo)); }
DI void ulo(float& d, u32 a) { asm volatile("v_lshlrev_b32 %0, 16, %1" : "=v"(d) : "v"(a)); }
DI void uhi(float& d, u32 a) { asm volatile("v_and_b32 %0, 0xffff0000, %1" : "=v"(d) : "v"(a)); }
DI void pkmul(f32x2& d, f32x2 a, f32x2 b) { asm volatile("v_pk_mul_f32 %0, %1, %2" : "=v"(d) : "v"(a), "v"(b)); }
DI void cvtpk(u32& d, f32x2 a) { asm volatile("v_cvt_pk_bf16_f32 %0, %1, %2" : "=v"(d) : "v"(a[0]), "v"(a[1])); }
template <int R> DI void ror_add(float& x) { asm volatile("v_add_f32_dpp %0, %0, %0 row_ror:%1 row_mask:0xf bank_mask:0xf" : "+v"(x) : "i"(R)); }
DI void bc_add(float& x) { asm volatile("v_add_f32_dpp %0, %0, %0 row_bcast:15 row_mask:0xa bank_mask:0xf" : "+v"(x)); }
DI void g_t(f32x2& d, f32x2 cs, u32x2 st, f32x2 b) { asm volatile("v_pk_fma_f32 %0, %1, %2, %3 op_sel:[0,1,0] op_sel_hi:[1,1,1]" : "=v"(d) : "v"(cs), "v"(st), "v"(b)); }
DI void g_u(f32x2& d, f32x2 a, u32x2 st) { asm volatile("v_pk_fma_f32 %0, %1, %2, %0 op_sel:[0,0,0] op_sel_hi:[1,0,1]" : "+v"(d) : "v"(a), "v"(st)); }
DI void g_p(f32x2& d, u32x2 cc) { asm volatile("v_pk_fma_f32 %0, %0, %1, %1 op_sel:[0,0,1] op_sel_hi:[1,0,1]" : "+v"(d) : "s"(cc)); }
DI void g_m(f32x2& d, f32x2 a) { asm volatile("v_pk_mul_f32 %0, %0, %1" : "+v"(d) : "v"(a)); }
DI void g_1(f32x2& d) { asm volatile("v_pk_add_f32 %0, %0, 1.0 op_sel_hi:[1,0]" : "+v"(d)); }
DI void vexp(float& d, float a) { asm volatile("v_exp_f32 %0, %1" : "=v"(d) : "v"(a)); }
DI void vrcp(float& d, float a) { asm volatile("v_rcp_f32 %0, %1" : "=v"(d) : "v"(a)); }
DI void st128n(u32x4 val, u32 off, rsrc_t r, u32 soff) { asm volatile("buffer_store_dwordx4 %0, %1, %2, %3 offen sc1" ::"v"(val), "v"(off), "s"(r), "s"(soff)); }
template <int IMM> DI void dmao(rsrc_t r, u32 voff, u32 soff) { asm volatile("buffer_load_dwordx4 %0, %1, %2 offen offset:%3 lds" ::"v"(voff), "s"(r), "s"(soff), "i"(IMM)); }
DI void dma0(rsrc_t r, u32 voff, u32 soff) { asm volatile("buffer_load_dwordx4 %0, %1, %2 offen lds" ::"v"(voff), "s"(r), "s"(soff)); }
DI void dma0n(rsrc_t r, u32 voff, u32 soff) { asm volatile("buffer_load_dwordx4 %0, %1, %2 offen nt lds" ::"v"(voff), "s"(r), "s"(soff)); }
DI void dma1(rsrc_t r, u32 voff, u32 soff) { asm volatile("buffer_load_dwordx4 %0, %1, %2 offen sc1 lds" ::"v"(voff), "s"(r), "s"(soff)); }
DI void mf(f32x4& c, u32x4 a, u32x4 b) { asm volatile("v_mfma_f32_16x16x32_bf16 %0, %1, %2, %0" : "+v"(c) : "v"(a), "v"(b)); }
DI void mf0(f32x4& c, u32x4 a, u32x4 b) { asm volatile("v_mfma_f32_16x16x32_bf16 %0, %1, %2, 0" : "=&v"(c) : "v"(a), "v"(b)); }
template <int AR> DI void mfa(f32x4& c, u32x4 b) { asm volatile("v_mfma_f32_16x16x32_bf16 %0, a[%c2:%c3], %1, %0" : "+v"(c) : "v"(b), "i"(AR), "i"(AR + 3)); }
template <int AR> DI void mfa0(f32x4& c, u32x4 b) { asm volatile("v_mfma_f32_16x16x32_bf16 %0, a[%c2:%c3], %1, 0" : "=&v"(c) : "v"(b), "i"(AR), "i"(AR + 3)); }
template <int OFF> DI void dsr128(u32x4& d, u32 addr) { asm volatile("ds_read_b128 %0, %1 offset:%2" : "=v"(d) : "v"(addr), "i"(OFF)); }
template <int OFF> DI void dstr(u32x2& d, u32 addr) { asm volatile("ds_read_b64_tr_b16 %0, %1 offset:%2" : "=v"(d) : "v"(addr), "i"(OFF)); }
template <int OFF> DI void dsw128(u32 addr, f32x4 val) { asm volatile("ds_write_b128 %0, %1 offset:%2" ::"v"(addr), "v"(val), "i"(OFF)); }
template <int AR, int IMM> DI void ldw2(u32 voff, rsrc_t r, u32 soff) {
    asm volatile("buffer_load_dwordx4 a[%c0:%c1], %2, %3, %4 offen offset:%5 sc1" ::"i"(AR), "i"(AR + 3), "v"(voff), "s"(r), "s"(soff), "i"(IMM)); }
template <int AR, int IMM> DI void ldxa(u32 voff, rsrc_t r, u32 soff) {
    asm volatile("buffer_load_dwordx4 a[%c0:%c1], %2, %3, %4 offen offset:%5" ::"i"(AR), "i"(AR + 3), "v"(voff), "s"(r), "s"(soff), "i"(IMM)); }
template <int IMM> DI void ldh(u32x4& d, u32 voff, rsrc_t r, u32 soff) { asm volatile("buffer_load_dwordx4 %0, %1, %2, %3 offen offset:%4 sc1" : "=v"(d) : "v"(voff), "s"(r), "s"(soff), "i"(IMM)); }
DI void load_b64(u32x2& d, u32 off, rsrc_t r) { asm volatile("buffer_load_dwordx2 %0, %1, %2, 0 offen sc1" : "=v"(d) : "v"(off), "s"(r)); }
DI void load_b32(u32& d, u32 off, rsrc_t r) { asm volatile("buffer_load_dword %0, %1, %2, 0 offen sc1" : "=v"(d) : "v"(off), "s"(r)); }
template <int IMM> DI void gld64(u32x2& d, rsrc_t r, u32 voff) { asm volatile("buffer_load_dwordx2 %0, %1, %2, 0 offen offset:%3" : "=v"(d) : "v"(voff), "s"(r), "i"(IMM)); }
DI void st128(u32x4 val, u32 off, rsrc_t r) { asm volatile("buffer_store_dwordx4 %0, %1, %2, 0 offen sc1\n\ts_nop 1" ::"v"(val), "v"(off), "s"(r)); }
DI void st64(u32x2 val, u32 off, rsrc_t r) { asm volatile("buffer_store_dwordx2 %0, %1, %2, 0 offen sc1" ::"v"(val), "v"(off), "s"(r)); }
template <int IMM> DI void gg64(u32x2& d, u32 voff, u64 base) { asm volatile("global_load_dwordx2 %0, %1, %2 offset:%3" : "=v"(d) : "v"(voff), "s"(base), "i"(IMM)); }
DI void ldu16(u32& d, u32 off, rsrc_t r) { asm volatile("buffer_load_ushort %0, %1, %2, 0 offen" : "=v"(d) : "v"(off), "s"(r)); }
DI void gldu16(u32& d, u64 base) { asm volatile("global_load_ushort %0, %1, %2" : "=v"(d) : "v"(0u), "s"(base)); }
template <int IMM> DI void ldf128(u32x4& d, u32 voff, rsrc_t r, u32 soff) { asm volatile("buffer_load_dwordx4 %0, %1, %2, %3 offen offset:%4" : "=v"(d) : "v"(voff), "s"(r), "s"(soff), "i"(IMM)); }
template <int IMM> DI void ldf32(u32& d, u32 voff, rsrc_t r, u32 soff) { asm volatile("buffer_load_dword %0, %1, %2, %3 offen offset:%4" : "=v"(d) : "v"(voff), "s"(r), "s"(soff), "i"(IMM)); }
template <int OFF> DI void dsw128u(u32 addr, u32x4 val) { asm volatile("ds_write_b128 %0, %1 offset:%2" ::"v"(addr), "v"(val), "i"(OFF)); }
DI void pfl(u32& d, u32 off, rsrc_t r) { asm volatile("buffer_load_dword %0, %1, %2, 0 offen" : "=v"(d) : "v"(off), "s"(r)); }
DI u64 csel64_eq(int x, int k, u64 a, u64 b) { u64 d; asm("s_cmp_eq_u32 %1, %2\n\ts_cselect_b64 %0, %3, %4" : "=s"(d) : "s"(x), "i"(k), "s"(a), "s"(b) : "scc"); return d; }
DI u32 csel32_eq(int x, int k, u32 a, u32 b) { u32 d; asm("s_cmp_eq_u32 %1, %2\n\ts_cselect_b32 %0, %3, %4" : "=s"(d) : "s"(x), "i"(k), "s"(a), "s"(b) : "scc"); return d; }
DI void gdma(u32 voff, u64 base) { asm volatile("global_load_lds_dwordx4 %0, %1" ::"v"(voff), "s"(base)); }
template <int IMM> DI void gst64(u32x2 val, u32 voff, u64 base) { asm volatile("global_store_dwordx2 %0, %1, %2 offset:%3" ::"v"(voff), "v"(val), "s"(base), "i"(IMM)); }
DI void gld32(u32& d, u32 voff, u64 base) { asm volatile("global_load_dword %0, %1, %2" : "=v"(d) : "v"(voff), "s"(base)); }
DI void sto64(u32x2 val, u32 off, rsrc_t r) { asm volatile("buffer_store_dwordx2 %0, %1, %2, 0 offen sc1" ::"v"(val), "v"(off), "s"(r)); }
template <class T> DI void tie(T& x) { asm volatile("" : "+v"(x)); }
template <class T> DI void ties(T& x) { asm volatile("" : "+s"(x)); }
#define SETM0(x) setm0((u32)(x))
#define ADDM0(x, imm) addm0<(imm)>(x)
#define MF32(R, c, a, b) asm volatile("v_mfma_f32_32x32x16_bf16 %0, %1, %2, %0" : "+{" R "}"(c) : "v"(a), "v"(b))
#define MF320(R, c, a, b) asm volatile("v_mfma_f32_32x32x16_bf16 %0, %1, %2, 0" : "=&{" R "}"(c) : "v"(a), "v"(b))
#define MF16E(R, D, C, c, a, b) asm volatile("v_mfma_f32_16x16x32_bf16 " D ", %1, %2, " C : "+{" R "}"(c) : "v"(a), "v"(b))
#define MF16A(R, D, C, c, a, B) asm volatile("v_mfma_f32_16x16x32_bf16 " D ", %1, " B ", " C : "+{" R "}"(c) : "v"(a))
#define LDXA(ar, voff, soff, imm) ldxa<ar, imm>(voff, rq, (u32)(soff))
#define DSR64(d, addr, off) dsr64<off>(d, addr)
#define DSR32(d, addr, off) dsr32<off>(d, addr)
#define DSRF(d, addr, off) dsrf128<off>(d, addr)
#define DSW64(addr, off, val) dsw64<off>(addr, val)
#define PAIR(v, i) __builtin_shufflevector(v, v, (i), (i) + 1)
#define DMAO(r, voff, soff, imm) dmao<imm>(r, voff, (u32)(soff))
#define DMA0(r, voff, soff) dma0(r, voff, (u32)(soff))
#define DMA1(r, voff, soff) dma1(r, voff, (u32)(soff))
#define DMA0N(r, voff, soff) dma0n(r, voff, (u32)(soff))
#define MF(c, a, b) mf(c, a, b)
#define MF0(c, a, b) mf0(c, a, b)
#define MFA(c, ar, b) mfa<ar>(c, b)
#define MFA0(c, ar, b) mfa0<ar>(c, b)
#define DSR128(d, addr, off) dsr128<off>(d, addr)
#define DSTR(d, addr, off) dstr<off>(d, addr)
#define DSW128(addr, off, val) dsw128<off>(addr, val)
#define LDW2(ar, soff, imm) ldw2<ar, imm>(vl16, rws, (u32)(soff))
#define LDH(d, soff, imm) ldh<imm>(d, vl16, rws, (u32)(soff))
#define LOAD_B64(d, off) load_b64(d, (u32)(off), rws)
#define LOAD_B32(d, off) load_b32(d, (u32)(off), rws)
#define GLD64(d, r, voff, imm) gld64<imm>(d, r, (u32)(voff))
#define ST128(val, off) st128(val, (u32)(off), rws)
#define ST64(val, off) st64(val, (u32)(off), rws)
#define PFQ(i) pfl(pf[i], opf[i], rq)
#define PFW(i) pfl(pf[i], opf[i], rw1)
#define PFS(i) pfl(pf[i], opf[i], rsk)
#define PFH(i) LOAD_B32(pf[i], opf[i])
#define LDF128(d, soff, imm) ldf128<imm>(d, vl16, rsk, (u32)(soff))
#define LDF32(d, soff, imm) ldf32<imm>(d, vl16, rsk, (u32)(soff))
#define LDFP(R, X, soff, imm) asm volatile("buffer_load_dword" X " " R ", %1, %2, %3 offen offset:" #imm : "+{v[96:127]}"(FLR) : "v"(vl16), "s"(rsk), "s"((u32)(soff)))
#define PFS2(ar, off, so) asm volatile("buffer_load_dword a" #ar ", %0, %1, %2 offen sc1" ::"v"(off), "s"(rws), "s"((u32)(so)) : "a" #ar)
#define LDQ(a0, a1, imm) asm volatile("buffer_load_dwordx2 a[" #a0 ":" #a1 "], %0, %1, 0 offen offset:" #imm ::"v"(vq), "s"(rq) : "a" #a0, "a" #a1)
#define PFA(ar, off) asm volatile("buffer_load_dword a" #ar ", %0, %1, 0 offen sc1" ::"v"(off), "s"(rws) : "a" #ar)
#define DSW128U(addr, off, val) dsw128u<off>(addr, val)
#define STORE_FLAG(off) ST64(evv, tid == 0 ? (u32)(off) : OOB)
#define POLL_LOAD(d, off) LOAD_B64(d, off)
#define CHECK_FLAGS(v, off, code) do { tie(v); const u32 a_ = v[0], b_ = v[1]; if (!__all(a_ == elo && b_ == ehi)) spin_flags(rws, off, elo, ehi, code); } while (0)
#define TIE(x) tie(x)
#define TIES(x) ties(x)
#define NOP1() asm volatile("s_nop 0")
#define NOP2() asm volatile("s_nop 1")
#define ST128N(val, off) st128n(val, (u32)(off), rws, 0u)
#define ST128S(val, off, soff) st128n(val, (u32)(off), rws, soff)
#define MK4(a) u32x4{a[0], a[1], a[2], a[3]}
#define BURN0(i) asm volatile("v_mfma_f32_32x32x16_bf16 a[0:15], a[16:19], a[20:23], 0")
#define BURN(i) do { if ((i) == 0) asm volatile("v_mfma_f32_32x32x16_bf16 a[0:15], a[16:19], a[20:23], a[0:15]"); else asm volatile("v_mfma_f32_32x32x16_bf16 a[0:15], a[24:27], a[28:31], a[0:15]"); } while (0)
#define STL i16
#define CBL ((u32)(g >> 1) * 64u + (u32)(g & 1) * 16u)
#define MFNOP() asm volatile("s_nop 7\n\ts_nop 1")

#define STAMP(i) do { } while (0)

__global__ __launch_bounds__(256, 1) void fused_kernel(const Args A_) {
    __shared__ __attribute__((aligned(1024))) char smem_[163840];
    u32x16 ka0, ka1; u32x8 ka2;
    asm volatile("s_load_dwordx16 %0, %3, 0x0\n\ts_load_dwordx16 %1, %3, 0x40\n\ts_load_dwordx8 %2, %3, 0x80"
                 : "=&s"(ka0), "=&s"(ka1), "=&s"(ka2) : "s"(__builtin_amdgcn_kernarg_segment_ptr()));
    SCHED();
    LAS char* const smem = (LAS char*)smem_;
    const u32 lds0 = (u32)(size_t)smem;
    const int tid = threadIdx.x, lane = tid & 63, w = rfl(tid >> 6);
    const int blk = blockIdx.x, xc = blk & 7, r = blk >> 3, tq = xc >> 1, hf = xc & 1;
    const int m1 = 4 * tq + (r >> 3), n1 = 8 * hf + (r & 7);
    const int m = 8 * tq + (r >> 2), n2 = 4 * hf + (r & 3);
    const int par = m & 1, pi = m >> 1;
    const int g = lane >> 4, i16 = lane & 15, qq = i16 >> 2, pp = i16 & 3;
    STAMP(0);
    asm volatile("" ::: ACLOB);

    __builtin_assume(blk < 256 && w < 4 && blk >= 0 && w >= 0);
    const u32 vl16 = lane * 16;
    const int h5 = lane >> 5, l31 = lane & 31;
    u32 vkv[3], vw2, vsm, vxs;
    {
        const u32 r6 = (u32)(lane * 26) >> 8;
        vkv[0] = vl16 + (u32)rfl((64 * m + 63 - 6 * w) * 1280 + 160 * n2) - r6 * 1440u; vkv[1] = vkv[0] - 30720; vkv[2] = vkv[0] - 61440;
        vw2 = vl16 + (u32)rfl((48 * m + 6 * w) * 1280 + 160 * n2) + r6 * 1120u;
        const u32 smax = (u32)rfl((0x2c9c3cfc >> (8 * w) & 0xff) << 2);
        vsm = vl16 < smax ? vl16 : smax;
        vxs = (u32)(l31 * 8) + (u32)rfl((64 * m + 8 * n2 + 2 * w) * 1280) + (u32)h5 * 1280u;
    }
    TIE(vkv[0]); TIE(vkv[1]); TIE(vkv[2]); TIE(vw2); TIE(vsm); TIE(vxs);
    const u32 m0x = lds0 + XR + w * 4096, m0w = lds0 + WR + w * 3072, m0c = lds0 + w * 1024, m0k = lds0 + P_K + w * 960;
    u64 E, pc; u32 spd;
    u32 vxj[4], vwj[3], pf[6], opf[6], wA[3], pB[4], aK, aT, aG, aO, aW[2], oW[2], aB1, aB1F, oS, aR, oR, oR2, pollo, ost, ogt, opref, aFc[3];
    u32 aC0, aC1, aC2, oH0, oH1, oH2, paF, pbF, paH, pbH, vfH, pollh, vfY, polly, aEx, aX, oY, gxw0, gxw1, gxw2, vyt, aGr, aYT, vq, aB2, vPE, vE, vth, aQ0, aQ1, aQ2, aQ3, aQ4;
    u32 okE;
    u32 osb; u32 hoff0, hoff1, hoff2, hoff3, dY2, ys0, ys1, ys2, ym0, ym1, ym2; u32x2 cc;
    u32 aX2, wB[3][2], hs0, hs1, hs2, vt2, vxa[2], aCs, aW2[2], oW2[2]; u32x2 st1;
    u32x16 CBV; u32x4 sel; u32x32 FLR; u32 aPre, aZ, pollw, otE, otL; u32x2 pollv2;
    const u32 psoff = csel32_eq(w, 3, 192 * n1, csel32_eq(w, 2, 640 * n2, 1024 * w));
    asm volatile("s_waitcnt lgkmcnt(0)" : "+s"(ka0), "+s"(ka1), "+s"(ka2));
#define SUB4(v, i) __builtin_shufflevector(v, v, (i), (i) + 1, (i) + 2, (i) + 3)
#define PTR(v, i) (((u64)v[(i) + 1] << 32) | v[i])
    const rsrc_t rq = SUB4(ka0, 0), rk = SUB4(ka0, 4), rv = SUB4(ka0, 8), rw1 = SUB4(ka0, 12), rw2 = SUB4(ka1, 0), rsk = SUB4(ka1, 4), rws = SUB4(ka1, 8);
    u32x2 xs[5];
    GLD64(xs[0], rq, vxs, 0); GLD64(xs[1], rq, vxs, 256); GLD64(xs[2], rq, vxs, 512); GLD64(xs[3], rq, vxs, 768); GLD64(xs[4], rq, vxs, 1024);
    SCHED();
    const u64 p_b2 = PTR(ka2, 2), p_ssmD = PTR(ka2, 4), out = PTR(ka2, 6), wsp = PTR(ka1, 8);
    const u64 psm = csel64_eq(w, 3, PTR(ka2, 0), csel64_eq(w, 2, PTR(ka1, 12), PTR(ka1, 14))) + psoff;
    SETM0(lds0 + SM_GW + w * 1024);
    gdma(vsm, psm);
    SETM0(lds0 + B2L);
    gdma(vl16 < 144 ? vl16 : 144u, p_b2 + 160 * n2);
    asm volatile("s_load_dwordx2 %0, %1, 0x0" : "=s"(E) : "s"(wsp));
    asm volatile("s_getpc_b64 %0" : "=s"(pc));
    SCHED();
    ADDM0(m0k, 0);
    NOP1();
    DMA0(rk, vkv[0], 0);
    SCHED();
    const int tw = (w + ROT2 * n2) & 3;
    const int ch0 = rfl(80 * n2 + 5 * pi + 2 * par), ch1 = ch0 + 1, ch2 = rfl(80 * n2 + 5 * pi + 4);
    const int wr = (int)csel32_eq(w, 3, 0, w);
    const int ftr[3] = {wr, (int)csel32_eq(wr, 2, 0, wr + 1), (int)csel32_eq(wr, 0, 2, wr - 1)};
    u32 sxo[10], swo[10];
    { const u32 c0_ = (u32)(5 * n2) >> 2, rho = (u32)((r & 7) + ROTM * (r >> 3)) & 7;
      u32 seq = (u32)(0x7654321098765432ull >> (4 * c0_)); TIES(seq);
      seq = (u32)((((u64)seq << 32) | seq) >> (4 * rho)); TIES(seq);
      sfor<10>([&](auto K) { constexpr u32 k = K; u32 c = k < 8 ? (seq >> (4 * k)) & 15 : c0_ + (k - 8); sxo[k] = c * 128; swo[k] = c * 196608; }); }
    u32x2 st0, pollv, evv; u32 gate_tok, pref, ddF, ddH, elo, ehi;
    const u64 pdF = p_ssmD + 2 * ((w >> 1) ? ch1 : ch0), pdH = p_ssmD + 2 * ch2;
    ADDM0(m0k, 3840);
    { aK = (u32)(lane >> 2) * 160u + (u32)((lane & 3) * 8) + (u32)rfl(lds0 + P_K + 16 * w * 160);
      aT = lds0 + SM_TAP + i16 * 8; aG = lds0 + SM_GW + l31 * 8;
      aB1 = lds0 + SM_B1 + lane * 4; aB1F = lds0 + B1F + lane * 8;
      TIE(aK); TIE(aT); TIE(aG); TIE(aB1); TIE(aB1F); }
    SCHED();
    DMA0(rk, vkv[1], 0);
    ADDM0(m0k, 7680);
    { const u32 tk = (u32)rfl(64 * m + 8 * n2 + 2 * w) + (u32)h5;
      const u32 so = WS_STATS + tk * 16; oS = l31 == 16 ? so : OOB; TIE(oS); }
    SCHED();
    DMA0(rk, vkv[2], 0);
    ADDM0(m0k, P_V - P_K + 0);
    { const u32 l3 = (u32)lane >> 3, z = (((u32)lane ^ (u32)g) & 7) << 4;
      const u32 vx = l3 * 1280u + z + (u32)rfl((128 * m1 + 32 * w) * 1280);
      vxj[0] = vx; vxj[1] = (vx ^ 64) + 9216; vxj[2] = vx + 2 * 9216; vxj[3] = vxj[1] + 2 * 9216;
      TIE(vxj[0]); TIE(vxj[1]); TIE(vxj[2]); TIE(vxj[3]); }
    SCHED();
    DMA0(rv, vkv[0], 0);
    ADDM0(m0k, P_V - P_K + 3840);
    { const u32 vb = vl16 + (u32)rfl(3072 * w + 192 * n1);
      const u32 r0 = ((u32)lane * 5462u + (u32)rfl(192 * w * 5462)) >> 16, r1 = ((u32)lane * 5462u + (u32)rfl((192 * w + 64) * 5462)) >> 16, r2 = ((u32)lane * 5462u + (u32)rfl((192 * w + 128) * 5462)) >> 16;
      vwj[0] = r0 * 2880u + (vb ^ (r0 * 2 & 16)); vwj[1] = r1 * 2880u + (vb ^ (r1 * 2 & 16)); vwj[2] = r2 * 2880u + (vb ^ (r2 * 2 & 16));
      TIE(vwj[0]); TIE(vwj[1]); TIE(vwj[2]); }
    SCHED();
    DMA0(rv, vkv[1], 0);
    ADDM0(m0k, P_V - P_K + 7680);
    { const u32 u = (u32)rfl(4 * r + w);
      u32 cl = (u * 3 + (u32)lane) * 128; TIE(cl); opf[4] = (lane < 3 && cl < CODE_BYTES) ? cl : 0u;
      TIE(opf[4]); }
    SCHED();
    DMA0(rv, vkv[2], 0);
    ADDM0(m0k, P_W2 - P_K + 0);
    NOP1();
    DMA0(rw2, vw2, 0);
    ADDM0(m0k, P_W2 - P_K + 3840);
    NOP1();
    DMA0(rw2, vw2, 30720);
    ADDM0(m0x, 0);
    NOP1();
    DMAO(rq, vxj[0], sxo[0], 0);
    DMAO(rq, vxj[1], sxo[0], 1024);
    DMAO(rq, vxj[2], sxo[0], 2048);
    DMAO(rq, vxj[3], sxo[0], 3072);
    ADDM0(m0w, 0);
    NOP1();
    DMAO(rw1, vwj[0], swo[0], 0);
    DMAO(rw1, vwj[1], swo[0], 1024);
    DMAO(rw1, vwj[2], swo[0], 2048);
    STAMP(1);
    const u32 w2o = rfl(WS_W2T + (n2 * 48 + 12 * w) * 5 * 1024);
    f32x16 acc[3]; f32x4 cs, acc4[12];
    {
        u32x2 Al[3][2], Ah[3][2]; u32x4 Bg[2][2]; f32x4 cs2[2];
        u32x2 gs[5], tp[5], kc[5], kn[5], vv[5], wl[2], wh[2], wl2[2], wh2[2], ko[5]; u32 b1r; u32x4 kr[3];
        float sA, sQ, sG, o_[4]; u32 pa[3]; f32x2 vf[2], of[2];
        u32x4 fA[3], fB[3]; u32 fC[3], f1[4], f3[4];
    WAITV(15);
    asm volatile("s_waitcnt lgkmcnt(0)" : "+s"(E));
    TIE(xs[0]); TIE(xs[1]); TIE(xs[2]); TIE(xs[3]); TIE(xs[4]);
    elo = (u32)(E + 1); ehi = (u32)((E + 1) >> 32); evv = u32x2{elo, ehi};
    BAR();
    STAMP(2);
    DSR64(gs[0], aG, 0);
    DSR64(gs[1], aG, 256);
    DSR64(gs[2], aG, 512);
    DSR64(gs[3], aG, 768);
    DSR64(gs[4], aG, 1024);
    DSR32(b1r, aB1, 0);
    DSR64(tp[0], aT, 0);
    DSR64(tp[1], aT, 128);
    DSR64(tp[2], aT, 256);
    DSR64(tp[3], aT, 384);
    DSR64(tp[4], aT, 512);
    asm volatile("v_dot2_f32_bf16 %[a], %[x00], %[one], 0\n\tv_dot2_f32_bf16 %[q], %[x00], %[x00], 0\n\tv_dot2_f32_bf16 %[a], %[x01], %[one], %[a]\n\tv_dot2_f32_bf16 %[q], %[x01], %[x01], %[q]\n\tv_dot2_f32_bf16 %[a], %[x10], %[one], %[a]\n\tv_dot2_f32_bf16 %[q], %[x10], %[x10], %[q]\n\tv_dot2_f32_bf16 %[a], %[x11], %[one], %[a]\n\tv_dot2_f32_bf16 %[q], %[x11], %[x11], %[q]\n\tv_dot2_f32_bf16 %[a], %[x20], %[one], %[a]\n\tv_dot2_f32_bf16 %[q], %[x20], %[x20], %[q]\n\tv_dot2_f32_bf16 %[a], %[x21], %[one], %[a]\n\tv_dot2_f32_bf16 %[q], %[x21], %[x21], %[q]\n\tv_dot2_f32_bf16 %[a], %[x30], %[one], %[a]\n\tv_dot2_f32_bf16 %[q], %[x30], %[x30], %[q]\n\tv_dot2_f32_bf16 %[a], %[x31], %[one], %[a]\n\tv_dot2_f32_bf16 %[q], %[x31], %[x31], %[q]\n\tv_dot2_f32_bf16 %[a], %[x40], %[one], %[a]\n\tv_dot2_f32_bf16 %[q], %[x40], %[x40], %[q]\n\tv_dot2_f32_bf16 %[a], %[x41], %[one], %[a]\n\tv_dot2_f32_bf16 %[q], %[x41], %[x41], %[q]" : [a] "=&v"(sA), [q] "=&v"(sQ) : [x00] "v"(xs[0][0]), [x01] "v"(xs[0][1]), [x10] "v"(xs[1][0]), [x11] "v"(xs[1][1]), [x20] "v"(xs[2][0]), [x21] "v"(xs[2][1]), [x30] "v"(xs[3][0]), [x31] "v"(xs[3][1]), [x40] "v"(xs[4][0]), [x41] "v"(xs[4][1]), [one] "s"(0x3f803f80u));
    WAITL(0);
    TIE(gs[0]); TIE(tp[0]);
    TIE(gs[1]); TIE(tp[1]);
    TIE(gs[2]); TIE(tp[2]);
    TIE(gs[3]); TIE(tp[3]);
    TIE(gs[4]); TIE(tp[4]);
    TIE(b1r);
    asm volatile("v_dot2_f32_bf16 %[g], %[x00], %[g00], 0\n\tv_dot2_f32_bf16 v48, %[x01], %[g01], 0\n\tv_dot2_f32_bf16 %[g], %[x10], %[g10], %[g]\n\tv_dot2_f32_bf16 v48, %[x11], %[g11], v48\n\tv_dot2_f32_bf16 %[g], %[x20], %[g20], %[g]\n\tv_dot2_f32_bf16 v48, %[x21], %[g21], v48\n\tv_dot2_f32_bf16 %[g], %[x30], %[g30], %[g]\n\tv_dot2_f32_bf16 v48, %[x31], %[g31], v48\n\tv_dot2_f32_bf16 %[g], %[x40], %[g40], %[g]\n\tv_dot2_f32_bf16 v48, %[x41], %[g41], v48\n\tv_lshlrev_b32 v50, 16, %[b]\n\tv_and_b32 v51, 0xffff0000, %[b]\n\tds_write_b64 %[ab], v[50:51]\n\tv_add_f32 %[g], %[g], v48\n\tv_add_f32_dpp %[a], %[a], %[a] row_ror:8 row_mask:0xf bank_mask:0xf\n\tv_add_f32_dpp %[q], %[q], %[q] row_ror:8 row_mask:0xf bank_mask:0xf\n\tv_add_f32_dpp %[g], %[g], %[g] row_ror:8 row_mask:0xf bank_mask:0xf\n\tv_add_f32_dpp %[a], %[a], %[a] row_ror:4 row_mask:0xf bank_mask:0xf\n\tv_add_f32_dpp %[q], %[q], %[q] row_ror:4 row_mask:0xf bank_mask:0xf\n\tv_add_f32_dpp %[g], %[g], %[g] row_ror:4 row_mask:0xf bank_mask:0xf\n\tv_add_f32_dpp %[a], %[a], %[a] row_ror:2 row_mask:0xf bank_mask:0xf\n\tv_add_f32_dpp %[q], %[q], %[q] row_ror:2 row_mask:0xf bank_mask:0xf\n\tv_add_f32_dpp %[g], %[g], %[g] row_ror:2 row_mask:0xf bank_mask:0xf\n\tv_add_f32_dpp %[a], %[a], %[a] row_ror:1 row_mask:0xf bank_mask:0xf\n\tv_add_f32_dpp %[q], %[q], %[q] row_ror:1 row_mask:0xf bank_mask:0xf\n\tv_add_f32_dpp %[g], %[g], %[g] row_ror:1 row_mask:0xf bank_mask:0xf\n\tv_add_f32_dpp %[a], %[a], %[a] row_bcast:15 row_mask:0xa bank_mask:0xf\n\tv_add_f32_dpp %[q], %[q], %[q] row_bcast:15 row_mask:0xa bank_mask:0xf\n\tv_add_f32_dpp %[g], %[g], %[g] row_bcast:15 row_mask:0xa bank_mask:0xf" : [a] "+v"(sA), [q] "+v"(sQ), [g] "=&v"(sG) : [x00] "v"(xs[0][0]), [x01] "v"(xs[0][1]), [x10] "v"(xs[1][0]), [x11] "v"(xs[1][1]), [x20] "v"(xs[2][0]), [x21] "v"(xs[2][1]), [x30] "v"(xs[3][0]), [x31] "v"(xs[3][1]), [x40] "v"(xs[4][0]), [x41] "v"(xs[4][1]), [g00] "v"(gs[0][0]), [g01] "v"(gs[0][1]), [g10] "v"(gs[1][0]), [g11] "v"(gs[1][1]), [g20] "v"(gs[2][0]), [g21] "v"(gs[2][1]), [g30] "v"(gs[3][0]), [g31] "v"(gs[3][1]), [g40] "v"(gs[4][0]), [g41] "v"(gs[4][1]), [b] "v"(b1r), [ab] "v"(aB1F) : "v48", "v50", "v51");
    { const float mean = sA * (1.f / D);
      const float var = __builtin_fmaxf(sQ * (1.f / D) - mean * mean, 0.f);
      const float rstd = __builtin_amdgcn_rsqf(var + 1e-5f);
      const float gate = __builtin_amdgcn_rcpf(1.f + __builtin_amdgcn_exp2f(sG * -1.4426950408889634f));
      u32x4 stv = u32x4{__float_as_uint(rstd), __float_as_uint(-mean * rstd), __float_as_uint(gate), 0u};
      TIE(stv);
      ST128N(stv, oS); NOP2(); }
    WAITV(10);
    BAR();
    DSTR(kc[0], aK, 0);
    DSTR(kn[0], aK, 640);
    DSTR(vv[0], aK, P_V - P_K + 0);
    DSTR(kc[1], aK, 32);
    DSTR(kn[1], aK, 672);
    DSTR(vv[1], aK, P_V - P_K + 32);
    DSTR(kc[2], aK, 64);
    DSTR(kn[2], aK, 704);
    DSTR(vv[2], aK, P_V - P_K + 64);
    asm volatile("global_load_dword a240, %0, %1" ::"v"(opf[4]), "s"(pc) : "a240");
    ADDM0(m0x, 16384);
    NOP1();
    DMAO(rq, vxj[0], sxo[1], 0);
    DSTR(kc[3], aK, 96);
    DSTR(kn[3], aK, 736);
    DSTR(vv[3], aK, P_V - P_K + 96);
    WAITL(9);
    TIE(kc[0]); TIE(kn[0]); TIE(vv[0]);
    asm volatile("v_alignbit_b32 v48, %[c1], %[c0], 16\n\tv_alignbit_b32 v49, %[n0], %[c1], 16\n\tv_alignbit_b32 v50, %[n1], %[n0], 16\n\tv_dot2_f32_bf16 v52, %[c1], %[t1], 0\n\tv_dot2_f32_bf16 v54, %[n0], %[t1], 0\n\tv_dot2_f32_bf16 v53, v49, %[t1], 0\n\tv_dot2_f32_bf16 v55, v50, %[t1], 0\n\tv_dot2_f32_bf16 v52, %[c0], %[t0], v52\n\tv_dot2_f32_bf16 v54, %[c1], %[t0], v54\n\tv_dot2_f32_bf16 v53, v48, %[t0], v53\n\tv_dot2_f32_bf16 v55, v49, %[t0], v55\n\tv_lshlrev_b32 v56, 16, %[v0]\n\tv_and_b32 v57, 0xffff0000, %[v0]\n\tv_lshlrev_b32 v58, 16, %[v1]\n\tv_and_b32 v59, 0xffff0000, %[v1]\n\tv_pk_mul_f32 v[52:53], v[52:53], v[56:57]\n\tv_pk_mul_f32 v[54:55], v[54:55], v[58:59]\n\tv_cvt_pk_bf16_f32 v60, v52, v53\n\tv_cvt_pk_bf16_f32 v61, v54, v55" : "={v[60:61]}"(ko[0]) : [c0] "v"(kc[0][0]), [c1] "v"(kc[0][1]), [n0] "v"(kn[0][0]), [n1] "v"(kn[0][1]), [v0] "v"(vv[0][0]), [v1] "v"(vv[0][1]), [t0] "v"(tp[0][0]), [t1] "v"(tp[0][1]) : "v48", "v49", "v50", "v52", "v53", "v54", "v55", "v56", "v57", "v58", "v59");
    DMAO(rq, vxj[1], sxo[1], 1024);
    { const u32 c8 = (u32)rfl(7 - 2 * w) - (u32)h5;
      aO = (u32)i16 * 128u + (((c8 ^ (u32)lane) & 7) << 4) + (u32)((lane & 16) >> 1) + (lds0 + P_KVO); TIE(aO); }
    SCHED();
    DMAO(rq, vxj[2], sxo[1], 2048);
    DMAO(rq, vxj[3], sxo[1], 3072);
    { const u32 hfi = (u32)rfl(4 * w + 0) + (u32)h5, hs = (hfi * 52u) >> 8, rt = hfi - 5u * hs;
      aW[0] = (hs * 16u + (u32)((lane >> 2) & 7)) * 160u + rt * 32u + (u32)((lane & 3) * 8) + (lds0 + P_W2);
      const u32 f16 = (u32)rfl(3 * m) + hs;
      const u32 o = ((f16 >> 1) * 5u + rt) * 1024u + (f16 & 1) * 512u + (u32)(l31 * 16) + (u32)rfl(WS_W2T + n2 * 48 * 5 * 1024);
      oW[0] = hfi < 15 ? o : OOB;
      TIE(aW[0]); TIE(oW[0]); }
    SCHED();
    DSTR(kc[4], aK, 128);
    DSTR(kn[4], aK, 768);
    DSTR(vv[4], aK, P_V - P_K + 128);
    WAITL(9);
    TIE(kc[1]); TIE(kn[1]); TIE(vv[1]);
    asm volatile("v_alignbit_b32 v48, %[c1], %[c0], 16\n\tv_alignbit_b32 v49, %[n0], %[c1], 16\n\tv_alignbit_b32 v50, %[n1], %[n0], 16\n\tv_dot2_f32_bf16 v52, %[c1], %[t1], 0\n\tv_dot2_f32_bf16 v54, %[n0], %[t1], 0\n\tv_dot2_f32_bf16 v53, v49, %[t1], 0\n\tv_dot2_f32_bf16 v55, v50, %[t1], 0\n\tv_dot2_f32_bf16 v52, %[c0], %[t0], v52\n\tv_dot2_f32_bf16 v54, %[c1], %[t0], v54\n\tv_dot2_f32_bf16 v53, v48, %[t0], v53\n\tv_dot2_f32_bf16 v55, v49, %[t0], v55\n\tv_lshlrev_b32 v56, 16, %[v0]\n\tv_and_b32 v57, 0xffff0000, %[v0]\n\tv_lshlrev_b32 v58, 16, %[v1]\n\tv_and_b32 v59, 0xffff0000, %[v1]\n\tv_pk_mul_f32 v[52:53], v[52:53], v[56:57]\n\tv_pk_mul_f32 v[54:55], v[54:55], v[58:59]\n\tv_cvt_pk_bf16_f32 v62, v52, v53\n\tv_cvt_pk_bf16_f32 v63, v54, v55" : "={v[62:63]}"(ko[1]) : [c0] "v"(kc[1][0]), [c1] "v"(kc[1][1]), [n0] "v"(kn[1][0]), [n1] "v"(kn[1][1]), [v0] "v"(vv[1][0]), [v1] "v"(vv[1][1]), [t0] "v"(tp[1][0]), [t1] "v"(tp[1][1]) : "v48", "v49", "v50", "v52", "v53", "v54", "v55", "v56", "v57", "v58", "v59");
    ADDM0(m0w, 12288);
    NOP1();
    DMAO(rw1, vwj[0], swo[1], 0);
    DMAO(rw1, vwj[1], swo[1], 1024);
    { const u32 hfi = (u32)rfl(4 * w + 2) + (u32)h5, hs = (hfi * 52u) >> 8, rt = hfi - 5u * hs;
      aW[1] = (hs * 16u + (u32)((lane >> 2) & 7)) * 160u + rt * 32u + (u32)((lane & 3) * 8) + (lds0 + P_W2);
      const u32 f16 = (u32)rfl(3 * m) + hs;
      const u32 o = ((f16 >> 1) * 5u + rt) * 1024u + (f16 & 1) * 512u + (u32)(l31 * 16) + (u32)rfl(WS_W2T + n2 * 48 * 5 * 1024);
      oW[1] = hfi < 15 ? o : OOB;
      TIE(aW[1]); TIE(oW[1]); }
    SCHED();
    DMAO(rw1, vwj[2], swo[1], 2048);
    WAITL(6);
    TIE(kc[2]); TIE(kn[2]); TIE(vv[2]);
    asm volatile("v_alignbit_b32 v48, %[c1], %[c0], 16\n\tv_alignbit_b32 v49, %[n0], %[c1], 16\n\tv_alignbit_b32 v50, %[n1], %[n0], 16\n\tv_dot2_f32_bf16 v52, %[c1], %[t1], 0\n\tv_dot2_f32_bf16 v54, %[n0], %[t1], 0\n\tv_dot2_f32_bf16 v53, v49, %[t1], 0\n\tv_dot2_f32_bf16 v55, v50, %[t1], 0\n\tv_dot2_f32_bf16 v52, %[c0], %[t0], v52\n\tv_dot2_f32_bf16 v54, %[c1], %[t0], v54\n\tv_dot2_f32_bf16 v53, v48, %[t0], v53\n\tv_dot2_f32_bf16 v55, v49, %[t0], v55\n\tv_lshlrev_b32 v56, 16, %[v0]\n\tv_and_b32 v57, 0xffff0000, %[v0]\n\tv_lshlrev_b32 v58, 16, %[v1]\n\tv_and_b32 v59, 0xffff0000, %[v1]\n\tv_pk_mul_f32 v[52:53], v[52:53], v[56:57]\n\tv_pk_mul_f32 v[54:55], v[54:55], v[58:59]\n\tv_cvt_pk_bf16_f32 v64, v52, v53\n\tv_cvt_pk_bf16_f32 v65, v54, v55" : "={v[64:65]}"(ko[2]) : [c0] "v"(kc[2][0]), [c1] "v"(kc[2][1]), [n0] "v"(kn[2][0]), [n1] "v"(kn[2][1]), [v0] "v"(vv[2][0]), [v1] "v"(vv[2][1]), [t0] "v"(tp[2][0]), [t1] "v"(tp[2][1]) : "v48", "v49", "v50", "v52", "v53", "v54", "v55", "v56", "v57", "v58", "v59");
    ADDM0(m0x, 32768);
    NOP1();
    DMAO(rq, vxj[0], sxo[2], 0);
    { const u32 l3 = (u32)lane >> 3, x7 = (((u32)lane ^ l3) & 7) << 4;
      aR = l3 * 128u + x7 + (u32)rfl(lds0 + P_KVO + 8 * w * 128);
      oR = l3 * 4096u + (u32)((lane & 7) * 16) + (u32)rfl(WS_KVT + (80 * n2 + 8 * w) * 4096 + 128 * m);
      oR2 = oR + csel32_eq(w >> 1, 0, 64 * 4096, OOB);
      TIE(aR); TIE(oR); TIE(oR2); }
    SCHED();
    DMAO(rq, vxj[1], sxo[2], 1024);
    DMAO(rq, vxj[2], sxo[2], 2048);
    { const u32 wb = ((u32)(g * 8) + (u32)qq) * 192u + (u32)((lane & 2) * 16) + (u32)((lane & 1) * 8) + (u32)(lane & 16) + (lds0 + WR);
      wB[0][0] = wb + (u32)rfl(64 * ftr[0]); wB[1][0] = wb + (u32)rfl(64 * ftr[1]); wB[2][0] = wb + (u32)rfl(64 * ftr[2]);
      wB[0][1] = wB[0][0] ^ 16; wB[1][1] = wB[1][0] ^ 16; wB[2][1] = wB[2][0] ^ 16;
      TIE(wB[0][0]); TIE(wB[1][0]); TIE(wB[2][0]); TIE(wB[0][1]); TIE(wB[1][1]); TIE(wB[2][1]); }
    SCHED();
    WAITL(3);
    TIE(kc[3]); TIE(kn[3]); TIE(vv[3]);
    asm volatile("v_alignbit_b32 v48, %[c1], %[c0], 16\n\tv_alignbit_b32 v49, %[n0], %[c1], 16\n\tv_alignbit_b32 v50, %[n1], %[n0], 16\n\tv_dot2_f32_bf16 v52, %[c1], %[t1], 0\n\tv_dot2_f32_bf16 v54, %[n0], %[t1], 0\n\tv_dot2_f32_bf16 v53, v49, %[t1], 0\n\tv_dot2_f32_bf16 v55, v50, %[t1], 0\n\tv_dot2_f32_bf16 v52, %[c0], %[t0], v52\n\tv_dot2_f32_bf16 v54, %[c1], %[t0], v54\n\tv_dot2_f32_bf16 v53, v48, %[t0], v53\n\tv_dot2_f32_bf16 v55, v49, %[t0], v55\n\tv_lshlrev_b32 v56, 16, %[v0]\n\tv_and_b32 v57, 0xffff0000, %[v0]\n\tv_lshlrev_b32 v58, 16, %[v1]\n\tv_and_b32 v59, 0xffff0000, %[v1]\n\tv_pk_mul_f32 v[52:53], v[52:53], v[56:57]\n\tv_pk_mul_f32 v[54:55], v[54:55], v[58:59]\n\tv_cvt_pk_bf16_f32 v66, v52, v53\n\tv_cvt_pk_bf16_f32 v67, v54, v55" : "={v[66:67]}"(ko[3]) : [c0] "v"(kc[3][0]), [c1] "v"(kc[3][1]), [n0] "v"(kn[3][0]), [n1] "v"(kn[3][1]), [v0] "v"(vv[3][0]), [v1] "v"(vv[3][1]), [t0] "v"(tp[3][0]), [t1] "v"(tp[3][1]) : "v48", "v49", "v50", "v52", "v53", "v54", "v55", "v56", "v57", "v58", "v59");
    DMAO(rq, vxj[3], sxo[2], 3072);
    ADDM0(m0w, 24576);
    NOP1();
    DMAO(rw1, vwj[0], swo[2], 0);
    { const u32 xb = (u32)i16 * 128u + ((((u32)g ^ ((u32)i16 >> 1)) & 7) << 4) + (u32)rfl(lds0 + XR + 32 * w * 128);
      pB[0] = xb; pB[1] = xb ^ 64;
      const u32 sv = i16 == 0 ? 0x3f803f80u : 0u; sel = u32x4{sv, sv, sv, sv};
      TIE(pB[0]); TIE(pB[1]); TIE(sel); }
    SCHED();
    DMAO(rw1, vwj[1], swo[2], 1024);
    WAITL(0);
    TIE(kc[4]); TIE(kn[4]); TIE(vv[4]);
    asm volatile("v_alignbit_b32 v48, %[c1], %[c0], 16\n\tv_alignbit_b32 v49, %[n0], %[c1], 16\n\tv_alignbit_b32 v50, %[n1], %[n0], 16\n\tv_dot2_f32_bf16 v52, %[c1], %[t1], 0\n\tv_dot2_f32_bf16 v54, %[n0], %[t1], 0\n\tv_dot2_f32_bf16 v53, v49, %[t1], 0\n\tv_dot2_f32_bf16 v55, v50, %[t1], 0\n\tv_dot2_f32_bf16 v52, %[c0], %[t0], v52\n\tv_dot2_f32_bf16 v54, %[c1], %[t0], v54\n\tv_dot2_f32_bf16 v53, v48, %[t0], v53\n\tv_dot2_f32_bf16 v55, v49, %[t0], v55\n\tv_lshlrev_b32 v56, 16, %[v0]\n\tv_and_b32 v57, 0xffff0000, %[v0]\n\tv_lshlrev_b32 v58, 16, %[v1]\n\tv_and_b32 v59, 0xffff0000, %[v1]\n\tv_pk_mul_f32 v[52:53], v[52:53], v[56:57]\n\tv_pk_mul_f32 v[54:55], v[54:55], v[58:59]\n\tv_cvt_pk_bf16_f32 v68, v52, v53\n\tv_cvt_pk_bf16_f32 v69, v54, v55" : "={v[68:69]}"(ko[4]) : [c0] "v"(kc[4][0]), [c1] "v"(kc[4][1]), [n0] "v"(kn[4][0]), [n1] "v"(kn[4][1]), [v0] "v"(vv[4][0]), [v1] "v"(vv[4][1]), [t0] "v"(tp[4][0]), [t1] "v"(tp[4][1]) : "v48", "v49", "v50", "v52", "v53", "v54", "v55", "v56", "v57", "v58", "v59");
    DMAO(rw1, vwj[2], swo[2], 2048);
    WAITL(0);
    WAITV(23);
    BAR();
    DSTR(wl[0], aW[0], 0);
    DSTR(wh[0], aW[0], 1280);
    DSTR(wl[1], aW[1], 0);
    DSTR(wh[1], aW[1], 1280);
    DSW64(aO, 0, ko[0]);
    DSW64(aO, 2048, ko[1]);
    DSW64(aO, 4096, ko[2]);
    DSW64(aO, 6144, ko[3]);
    DSW64(aO, 8192, ko[4]);
    WAITL(5);
    TIE(wl[0]); TIE(wh[0]); TIE(wl[1]); TIE(wh[1]);
    ST128N(MK(wl[0], wh[0]), oW[0]);
    ST128N(MK(wl[1], wh[1]), oW[1]);
    WAITL(0);
    WAITV(18);
    BAR();
    DSR128(kr[0], aR, 0);
    DSR128(kr[1], aR, 4096);
    DSR128(kr[2], aR, 8192);
    asm volatile("" : "={v[96:127]}"(FLR));
    asm volatile("" : "={v[0:3]}"(acc4[0])); asm volatile("" : "={v[4:7]}"(acc4[1])); asm volatile("" : "={v[8:11]}"(acc4[2])); asm volatile("" : "={v[12:15]}"(acc4[3])); asm volatile("" : "={v[16:19]}"(acc4[4])); asm volatile("" : "={v[20:23]}"(acc4[5])); asm volatile("" : "={v[24:27]}"(acc4[6])); asm volatile("" : "={v[28:31]}"(acc4[7])); asm volatile("" : "={v[32:35]}"(acc4[8])); asm volatile("" : "={v[36:39]}"(acc4[9])); asm volatile("" : "={v[40:43]}"(acc4[10])); asm volatile("" : "={v[44:47]}"(acc4[11]));
    DSTR(Al[0][0], wB[0][0], 0);
    DSTR(Ah[0][0], wB[0][0], 768);
    DSR128(Bg[0][0], pB[0], 0);
    DSR128(Bg[0][1], pB[0], 2048);
    DSTR(Al[0][1], wB[0][1], 0);
    DSTR(Ah[0][1], wB[0][1], 768);
    DSTR(Al[1][0], wB[1][0], 0);
    DSTR(Ah[1][0], wB[1][0], 768);
    DSTR(Al[1][1], wB[1][1], 0);
    DSTR(Ah[1][1], wB[1][1], 768);
    DSTR(Al[2][0], wB[2][0], 0);
    DSTR(Ah[2][0], wB[2][0], 768);
    DSTR(Al[2][1], wB[2][1], 0);
    DSTR(Ah[2][1], wB[2][1], 768);
    WAITL(15);
    TIE(kr[0]);
    ST128S(kr[0], oR, 0u);
    TIE(kr[1]);
    ST128S(kr[1], oR, 131072u);
    WAITL(14);
    TIE(kr[2]);
    ST128S(kr[2], oR2, 0u);
    STAMP(3);
    WAITLB(8);
    MF0(cs2[0], MK(Al[0][0], Ah[0][0]), sel);
    ADDM0(m0x, 49152);
    MF16E("v[0:3]", "v[0:3]", "0", acc4[0], MK(Al[0][0], Ah[0][0]), Bg[0][0]);
    DMAO(rq, vxj[0], sxo[3], 0);
    MF16E("v[8:11]", "v[8:11]", "0", acc4[2], MK(Al[0][0], Ah[0][0]), Bg[0][1]);
    DSTR(Al[0][0], wB[0][0], 6144);
    DSTR(Ah[0][0], wB[0][0], 6912);
    MF0(cs2[1], MK(Al[0][1], Ah[0][1]), sel);
    DSR128(Bg[1][0], pB[1], 0);
    DSR128(Bg[1][1], pB[1], 2048);
    MF16E("v[4:7]", "v[4:7]", "0", acc4[1], MK(Al[0][1], Ah[0][1]), Bg[0][0]);
    MF16E("v[12:15]", "v[12:15]", "0", acc4[3], MK(Al[0][1], Ah[0][1]), Bg[0][1]);
    DSTR(Al[0][1], wB[0][1], 6144);
    DSTR(Ah[0][1], wB[0][1], 6912);
    WAITLB(10);
    MF16E("v[16:19]", "v[16:19]", "0", acc4[4], MK(Al[1][0], Ah[1][0]), Bg[0][0]);
    DMAO(rq, vxj[1], sxo[3], 1024);
    MF16E("v[24:27]", "v[24:27]", "0", acc4[6], MK(Al[1][0], Ah[1][0]), Bg[0][1]);
    DSTR(Al[1][0], wB[1][0], 6144);
    DSTR(Ah[1][0], wB[1][0], 6912);
    MF16E("v[20:23]", "v[20:23]", "0", acc4[5], MK(Al[1][1], Ah[1][1]), Bg[0][0]);
    { const u32 l5_ = ((u32)lane >> 3) * 4096u + ((u32)lane & 7) * 128u; opf[5] = lane < 24 ? l5_ : OOB; opf[3] = (u32)lane * 128u;
      TIE(opf[3]); TIE(opf[5]); }
    SCHED();
    MF16E("v[28:31]", "v[28:31]", "0", acc4[7], MK(Al[1][1], Ah[1][1]), Bg[0][1]);
    DSTR(Al[1][1], wB[1][1], 6144);
    DSTR(Ah[1][1], wB[1][1], 6912);
    WAITLB(10);
    MF16E("v[32:35]", "v[32:35]", "0", acc4[8], MK(Al[2][0], Ah[2][0]), Bg[0][0]);
    DMAO(rq, vxj[2], sxo[3], 2048);
    MF16E("v[40:43]", "v[40:43]", "0", acc4[10], MK(Al[2][0], Ah[2][0]), Bg[0][1]);
    DSTR(Al[2][0], wB[2][0], 6144);
    DSTR(Ah[2][0], wB[2][0], 6912);
    MF16E("v[36:39]", "v[36:39]", "0", acc4[9], MK(Al[2][1], Ah[2][1]), Bg[0][0]);
    MF16E("v[44:47]", "v[44:47]", "0", acc4[11], MK(Al[2][1], Ah[2][1]), Bg[0][1]);
    DSTR(Al[2][1], wB[2][1], 6144);
    DSTR(Ah[2][1], wB[2][1], 6912);
    WAITLB(8);
    MF(cs2[0], MK(Al[0][0], Ah[0][0]), sel);
    MF16E("v[0:3]", "v[0:3]", "v[0:3]", acc4[0], MK(Al[0][0], Ah[0][0]), Bg[1][0]);
    DMAO(rq, vxj[3], sxo[3], 3072);
    WAITV(16);
    BAR();
    MF16E("v[8:11]", "v[8:11]", "v[8:11]", acc4[2], MK(Al[0][0], Ah[0][0]), Bg[1][1]);
    DSTR(Al[0][0], wB[0][0], 12288);
    DSTR(Ah[0][0], wB[0][0], 13056);
    MF(cs2[1], MK(Al[0][1], Ah[0][1]), sel);
    DSR128(Bg[0][0], pB[0], 16384);
    DSR128(Bg[0][1], pB[0], 18432);
    MF16E("v[4:7]", "v[4:7]", "v[4:7]", acc4[1], MK(Al[0][1], Ah[0][1]), Bg[1][0]);
    MF16E("v[12:15]", "v[12:15]", "v[12:15]", acc4[3], MK(Al[0][1], Ah[0][1]), Bg[1][1]);
    DSTR(Al[0][1], wB[0][1], 12288);
    DSTR(Ah[0][1], wB[0][1], 13056);
    ADDM0(m0w, 36864);
    WAITLB(10);
    MF16E("v[16:19]", "v[16:19]", "v[16:19]", acc4[4], MK(Al[1][0], Ah[1][0]), Bg[1][0]);
    DMAO(rw1, vwj[0], swo[3], 0);
    MF16E("v[24:27]", "v[24:27]", "v[24:27]", acc4[6], MK(Al[1][0], Ah[1][0]), Bg[1][1]);
    DSTR(Al[1][0], wB[1][0], 12288);
    DSTR(Ah[1][0], wB[1][0], 13056);
    MF16E("v[20:23]", "v[20:23]", "v[20:23]", acc4[5], MK(Al[1][1], Ah[1][1]), Bg[1][0]);
    DMAO(rw1, vwj[1], swo[3], 1024);
    MF16E("v[28:31]", "v[28:31]", "v[28:31]", acc4[7], MK(Al[1][1], Ah[1][1]), Bg[1][1]);
    DSTR(Al[1][1], wB[1][1], 12288);
    DSTR(Ah[1][1], wB[1][1], 13056);
    { opref = ((u32)i16 + (u32)g) * 2u - 32u; TIE(opref); }
    SCHED();
    WAITLB(10);
    MF16E("v[32:35]", "v[32:35]", "v[32:35]", acc4[8], MK(Al[2][0], Ah[2][0]), Bg[1][0]);
    MF16E("v[40:43]", "v[40:43]", "v[40:43]", acc4[10], MK(Al[2][0], Ah[2][0]), Bg[1][1]);
    DSTR(Al[2][0], wB[2][0], 12288);
    DSTR(Ah[2][0], wB[2][0], 13056);
    MF16E("v[36:39]", "v[36:39]", "v[36:39]", acc4[9], MK(Al[2][1], Ah[2][1]), Bg[1][0]);
    DMAO(rw1, vwj[2], swo[3], 2048);
    MF16E("v[44:47]", "v[44:47]", "v[44:47]", acc4[11], MK(Al[2][1], Ah[2][1]), Bg[1][1]);
    DSTR(Al[2][1], wB[2][1], 12288);
    DSTR(Ah[2][1], wB[2][1], 13056);
    STAMP(10);
    WAITLB(8);
    MF(cs2[0], MK(Al[0][0], Ah[0][0]), sel);
    ADDM0(m0w, 49152);
    MF16E("v[0:3]", "v[0:3]", "v[0:3]", acc4[0], MK(Al[0][0], Ah[0][0]), Bg[0][0]);
    DMAO(rw1, vwj[0], swo[4], 0);
    MF16E("v[8:11]", "v[8:11]", "v[8:11]", acc4[2], MK(Al[0][0], Ah[0][0]), Bg[0][1]);
    DSTR(Al[0][0], wB[0][0], 18432);
    DSTR(Ah[0][0], wB[0][0], 19200);
    MF(cs2[1], MK(Al[0][1], Ah[0][1]), sel);
    DSR128(Bg[1][0], pB[1], 16384);
    DSR128(Bg[1][1], pB[1], 18432);
    MF16E("v[4:7]", "v[4:7]", "v[4:7]", acc4[1], MK(Al[0][1], Ah[0][1]), Bg[0][0]);
    MF16E("v[12:15]", "v[12:15]", "v[12:15]", acc4[3], MK(Al[0][1], Ah[0][1]), Bg[0][1]);
    DSTR(Al[0][1], wB[0][1], 18432);
    DSTR(Ah[0][1], wB[0][1], 19200);
    WAITLB(10);
    MF16E("v[16:19]", "v[16:19]", "v[16:19]", acc4[4], MK(Al[1][0], Ah[1][0]), Bg[0][0]);
    DMAO(rw1, vwj[1], swo[4], 1024);
    MF16E("v[24:27]", "v[24:27]", "v[24:27]", acc4[6], MK(Al[1][0], Ah[1][0]), Bg[0][1]);
    DSTR(Al[1][0], wB[1][0], 18432);
    DSTR(Ah[1][0], wB[1][0], 19200);
    MF16E("v[20:23]", "v[20:23]", "v[20:23]", acc4[5], MK(Al[1][1], Ah[1][1]), Bg[0][0]);
    { const u32 pa_ = (u32)lane * 8 + (u32)rfl(WS_FP + n2 * 256), pb_ = (u32)(lane & 7) * 256u + (u32)(lane & 8) + (u32)rfl(WS_FP + 2 * m1 * 8);
      pollo = lane < 32 ? pa_ : pb_; ost = (u32)i16 * 16; TIE(pollo); TIE(ost); }
    SCHED();
    MF16E("v[28:31]", "v[28:31]", "v[28:31]", acc4[7], MK(Al[1][1], Ah[1][1]), Bg[0][1]);
    DSTR(Al[1][1], wB[1][1], 18432);
    DSTR(Ah[1][1], wB[1][1], 19200);
    WAITLB(10);
    MF16E("v[32:35]", "v[32:35]", "v[32:35]", acc4[8], MK(Al[2][0], Ah[2][0]), Bg[0][0]);
    DMAO(rw1, vwj[2], swo[4], 2048);
    MF16E("v[40:43]", "v[40:43]", "v[40:43]", acc4[10], MK(Al[2][0], Ah[2][0]), Bg[0][1]);
    DSTR(Al[2][0], wB[2][0], 18432);
    DSTR(Ah[2][0], wB[2][0], 19200);
    MF16E("v[36:39]", "v[36:39]", "v[36:39]", acc4[9], MK(Al[2][1], Ah[2][1]), Bg[0][0]);
    MF16E("v[44:47]", "v[44:47]", "v[44:47]", acc4[11], MK(Al[2][1], Ah[2][1]), Bg[0][1]);
    DSTR(Al[2][1], wB[2][1], 18432);
    DSTR(Ah[2][1], wB[2][1], 19200);
    WAITLB(8);
    MF(cs2[0], MK(Al[0][0], Ah[0][0]), sel);
    ADDM0(m0x, 0);
    MF16E("v[0:3]", "v[0:3]", "v[0:3]", acc4[0], MK(Al[0][0], Ah[0][0]), Bg[1][0]);
    DMAO(rq, vxj[0], sxo[4], 0);
    WAITV(11);
    BAR();
    MF16E("v[8:11]", "v[8:11]", "v[8:11]", acc4[2], MK(Al[0][0], Ah[0][0]), Bg[1][1]);
    DSTR(Al[0][0], wB[0][0], 24576);
    DSTR(Ah[0][0], wB[0][0], 25344);
    MF(cs2[1], MK(Al[0][1], Ah[0][1]), sel);
    DSR128(Bg[0][0], pB[0], 32768);
    DSR128(Bg[0][1], pB[0], 34816);
    MF16E("v[4:7]", "v[4:7]", "v[4:7]", acc4[1], MK(Al[0][1], Ah[0][1]), Bg[1][0]);
    STORE_FLAG(WS_FP + (n2 * 32 + m) * 8);
    MF16E("v[12:15]", "v[12:15]", "v[12:15]", acc4[3], MK(Al[0][1], Ah[0][1]), Bg[1][1]);
    DSTR(Al[0][1], wB[0][1], 24576);
    DSTR(Ah[0][1], wB[0][1], 25344);
    WAITLB(10);
    MF16E("v[16:19]", "v[16:19]", "v[16:19]", acc4[4], MK(Al[1][0], Ah[1][0]), Bg[1][0]);
    DMAO(rq, vxj[1], sxo[4], 1024);
    MF16E("v[24:27]", "v[24:27]", "v[24:27]", acc4[6], MK(Al[1][0], Ah[1][0]), Bg[1][1]);
    DSTR(Al[1][0], wB[1][0], 24576);
    DSTR(Ah[1][0], wB[1][0], 25344);
    MF16E("v[20:23]", "v[20:23]", "v[20:23]", acc4[5], MK(Al[1][1], Ah[1][1]), Bg[1][0]);
    DMAO(rq, vxj[2], sxo[4], 2048);
    MF16E("v[28:31]", "v[28:31]", "v[28:31]", acc4[7], MK(Al[1][1], Ah[1][1]), Bg[1][1]);
    DSTR(Al[1][1], wB[1][1], 24576);
    DSTR(Ah[1][1], wB[1][1], 25344);
    { const u32 ab = vl16 + (u32)rfl(lds0 + w * 1024 + 32); aFc[0] = ab + CF0; aFc[1] = ab + CF1; aFc[2] = ab + CF2; TIE(aFc[0]); TIE(aFc[1]); TIE(aFc[2]); }
    SCHED();
    WAITLB(10);
    MF16E("v[32:35]", "v[32:35]", "v[32:35]", acc4[8], MK(Al[2][0], Ah[2][0]), Bg[1][0]);
    MF16E("v[40:43]", "v[40:43]", "v[40:43]", acc4[10], MK(Al[2][0], Ah[2][0]), Bg[1][1]);
    DSTR(Al[2][0], wB[2][0], 24576);
    DSTR(Ah[2][0], wB[2][0], 25344);
    MF16E("v[36:39]", "v[36:39]", "v[36:39]", acc4[9], MK(Al[2][1], Ah[2][1]), Bg[1][0]);
    DMAO(rq, vxj[3], sxo[4], 3072);
    MF16E("v[44:47]", "v[44:47]", "v[44:47]", acc4[11], MK(Al[2][1], Ah[2][1]), Bg[1][1]);
    DSTR(Al[2][1], wB[2][1], 24576);
    DSTR(Ah[2][1], wB[2][1], 25344);
    WAITLB(8);
    MF(cs2[0], MK(Al[0][0], Ah[0][0]), sel);
    ADDM0(m0w, 0);
    MF16E("v[0:3]", "v[0:3]", "v[0:3]", acc4[0], MK(Al[0][0], Ah[0][0]), Bg[0][0]);
    DMAO(rw1, vwj[0], swo[5], 0);
    MF16E("v[8:11]", "v[8:11]", "v[8:11]", acc4[2], MK(Al[0][0], Ah[0][0]), Bg[0][1]);
    DSTR(Al[0][0], wB[0][0], 30720);
    DSTR(Ah[0][0], wB[0][0], 31488);
    MF(cs2[1], MK(Al[0][1], Ah[0][1]), sel);
    DSR128(Bg[1][0], pB[1], 32768);
    DSR128(Bg[1][1], pB[1], 34816);
    MF16E("v[4:7]", "v[4:7]", "v[4:7]", acc4[1], MK(Al[0][1], Ah[0][1]), Bg[0][0]);
    MF16E("v[12:15]", "v[12:15]", "v[12:15]", acc4[3], MK(Al[0][1], Ah[0][1]), Bg[0][1]);
    DSTR(Al[0][1], wB[0][1], 30720);
    DSTR(Ah[0][1], wB[0][1], 31488);
    WAITLB(10);
    MF16E("v[16:19]", "v[16:19]", "v[16:19]", acc4[4], MK(Al[1][0], Ah[1][0]), Bg[0][0]);
    DMAO(rw1, vwj[1], swo[5], 1024);
    MF16E("v[24:27]", "v[24:27]", "v[24:27]", acc4[6], MK(Al[1][0], Ah[1][0]), Bg[0][1]);
    DSTR(Al[1][0], wB[1][0], 30720);
    DSTR(Ah[1][0], wB[1][0], 31488);
    MF16E("v[20:23]", "v[20:23]", "v[20:23]", acc4[5], MK(Al[1][1], Ah[1][1]), Bg[0][0]);
    { const u32 cb = CBL + (lds0 + B1F); aC0 = cb + (u32)rfl(128 * ftr[0]); aC1 = cb + (u32)rfl(128 * ftr[1]); aC2 = cb + (u32)rfl(128 * ftr[2]);
      TIE(aC0); TIE(aC1); TIE(aC2); }
    SCHED();
    MF16E("v[28:31]", "v[28:31]", "v[28:31]", acc4[7], MK(Al[1][1], Ah[1][1]), Bg[0][1]);
    DSTR(Al[1][1], wB[1][1], 30720);
    DSTR(Ah[1][1], wB[1][1], 31488);
    WAITLB(10);
    MF16E("v[32:35]", "v[32:35]", "v[32:35]", acc4[8], MK(Al[2][0], Ah[2][0]), Bg[0][0]);
    DMAO(rw1, vwj[2], swo[5], 2048);
    MF16E("v[40:43]", "v[40:43]", "v[40:43]", acc4[10], MK(Al[2][0], Ah[2][0]), Bg[0][1]);
    DSTR(Al[2][0], wB[2][0], 30720);
    DSTR(Ah[2][0], wB[2][0], 31488);
    MF16E("v[36:39]", "v[36:39]", "v[36:39]", acc4[9], MK(Al[2][1], Ah[2][1]), Bg[0][0]);
    MF16E("v[44:47]", "v[44:47]", "v[44:47]", acc4[11], MK(Al[2][1], Ah[2][1]), Bg[0][1]);
    DSTR(Al[2][1], wB[2][1], 30720);
    DSTR(Ah[2][1], wB[2][1], 31488);
    WAITLB(8);
    MF(cs2[0], MK(Al[0][0], Ah[0][0]), sel);
    ADDM0(m0x, 16384);
    MF16E("v[0:3]", "v[0:3]", "v[0:3]", acc4[0], MK(Al[0][0], Ah[0][0]), Bg[1][0]);
    DMAO(rq, vxj[0], sxo[5], 0);
    WAITV(12);
    BAR();
    MF16E("v[8:11]", "v[8:11]", "v[8:11]", acc4[2], MK(Al[0][0], Ah[0][0]), Bg[1][1]);
    DSTR(Al[0][0], wB[0][0], 36864);
    DSTR(Ah[0][0], wB[0][0], 37632);
    MF(cs2[1], MK(Al[0][1], Ah[0][1]), sel);
    DSR128(Bg[0][0], pB[0], 49152);
    DSR128(Bg[0][1], pB[0], 51200);
    MF16E("v[4:7]", "v[4:7]", "v[4:7]", acc4[1], MK(Al[0][1], Ah[0][1]), Bg[1][0]);
    MF16E("v[12:15]", "v[12:15]", "v[12:15]", acc4[3], MK(Al[0][1], Ah[0][1]), Bg[1][1]);
    DSTR(Al[0][1], wB[0][1], 36864);
    DSTR(Ah[0][1], wB[0][1], 37632);
    WAITLB(10);
    MF16E("v[16:19]", "v[16:19]", "v[16:19]", acc4[4], MK(Al[1][0], Ah[1][0]), Bg[1][0]);
    DMAO(rq, vxj[1], sxo[5], 1024);
    MF16E("v[24:27]", "v[24:27]", "v[24:27]", acc4[6], MK(Al[1][0], Ah[1][0]), Bg[1][1]);
    DSTR(Al[1][0], wB[1][0], 36864);
    DSTR(Ah[1][0], wB[1][0], 37632);
    MF16E("v[20:23]", "v[20:23]", "v[20:23]", acc4[5], MK(Al[1][1], Ah[1][1]), Bg[1][0]);
    DMAO(rq, vxj[2], sxo[5], 2048);
    MF16E("v[28:31]", "v[28:31]", "v[28:31]", acc4[7], MK(Al[1][1], Ah[1][1]), Bg[1][1]);
    DSTR(Al[1][1], wB[1][1], 36864);
    DSTR(Ah[1][1], wB[1][1], 37632);
    { const u32 os = WS_H + (((2 * m1 + (w >> 1)) * 4 + 2 * (w & 1)) * 48 + 3 * n1) * 1024;
      hs0 = os + 1024 * ftr[0]; hs1 = os + 1024 * ftr[1]; hs2 = os + 1024 * ftr[2]; TIES(hs0); TIES(hs1); TIES(hs2); }
    SCHED();
    WAITLB(10);
    MF16E("v[32:35]", "v[32:35]", "v[32:35]", acc4[8], MK(Al[2][0], Ah[2][0]), Bg[1][0]);
    MF16E("v[40:43]", "v[40:43]", "v[40:43]", acc4[10], MK(Al[2][0], Ah[2][0]), Bg[1][1]);
    DSTR(Al[2][0], wB[2][0], 36864);
    DSTR(Ah[2][0], wB[2][0], 37632);
    MF16E("v[36:39]", "v[36:39]", "v[36:39]", acc4[9], MK(Al[2][1], Ah[2][1]), Bg[1][0]);
    DMAO(rq, vxj[3], sxo[5], 3072);
    MF16E("v[44:47]", "v[44:47]", "v[44:47]", acc4[11], MK(Al[2][1], Ah[2][1]), Bg[1][1]);
    DSTR(Al[2][1], wB[2][1], 36864);
    DSTR(Ah[2][1], wB[2][1], 37632);
    WAITLB(8);
    MF(cs2[0], MK(Al[0][0], Ah[0][0]), sel);
    ADDM0(m0w, 12288);
    MF16E("v[0:3]", "v[0:3]", "v[0:3]", acc4[0], MK(Al[0][0], Ah[0][0]), Bg[0][0]);
    DMAO(rw1, vwj[0], swo[6], 0);
    MF16E("v[8:11]", "v[8:11]", "v[8:11]", acc4[2], MK(Al[0][0], Ah[0][0]), Bg[0][1]);
    DSTR(Al[0][0], wB[0][0], 43008);
    DSTR(Ah[0][0], wB[0][0], 43776);
    MF(cs2[1], MK(Al[0][1], Ah[0][1]), sel);
    DSR128(Bg[1][0], pB[1], 49152);
    DSR128(Bg[1][1], pB[1], 51200);
    MF16E("v[4:7]", "v[4:7]", "v[4:7]", acc4[1], MK(Al[0][1], Ah[0][1]), Bg[0][0]);
    MF16E("v[12:15]", "v[12:15]", "v[12:15]", acc4[3], MK(Al[0][1], Ah[0][1]), Bg[0][1]);
    DSTR(Al[0][1], wB[0][1], 43008);
    DSTR(Ah[0][1], wB[0][1], 43776);
    WAITLB(10);
    MF16E("v[16:19]", "v[16:19]", "v[16:19]", acc4[4], MK(Al[1][0], Ah[1][0]), Bg[0][0]);
    DMAO(rw1, vwj[1], swo[6], 1024);
    MF16E("v[24:27]", "v[24:27]", "v[24:27]", acc4[6], MK(Al[1][0], Ah[1][0]), Bg[0][1]);
    DSTR(Al[1][0], wB[1][0], 43008);
    DSTR(Ah[1][0], wB[1][0], 43776);
    MF16E("v[20:23]", "v[20:23]", "v[20:23]", acc4[5], MK(Al[1][1], Ah[1][1]), Bg[0][0]);
    { const u32 la0 = __builtin_amdgcn_perm((56u << 16) | (3 * CPY + 48), ((2 * CPY + 48) << 16) | (CPY + 48), (u32)qq * 0x0202u + 0x0c0c0100u);
      const u32 la = lds0 + la0 + 8 * (u32)pp - 16 * (u32)g;
      const int sF = (w & 1) ? 5 : 0, sH = 4 * (w >> 1);
      paF = la + (u32)rfl(((w >> 1) ? CF1 : CF0) + 64 * sF); paH = la + (u32)rfl(CF2 + 64 * sH);
      TIE(paF); TIE(paH); }
    SCHED();
    MF16E("v[28:31]", "v[28:31]", "v[28:31]", acc4[7], MK(Al[1][1], Ah[1][1]), Bg[0][1]);
    DSTR(Al[1][1], wB[1][1], 43008);
    DSTR(Ah[1][1], wB[1][1], 43776);
    WAITLB(10);
    MF16E("v[32:35]", "v[32:35]", "v[32:35]", acc4[8], MK(Al[2][0], Ah[2][0]), Bg[0][0]);
    DMAO(rw1, vwj[2], swo[6], 2048);
    MF16E("v[40:43]", "v[40:43]", "v[40:43]", acc4[10], MK(Al[2][0], Ah[2][0]), Bg[0][1]);
    DSTR(Al[2][0], wB[2][0], 43008);
    DSTR(Ah[2][0], wB[2][0], 43776);
    MF16E("v[36:39]", "v[36:39]", "v[36:39]", acc4[9], MK(Al[2][1], Ah[2][1]), Bg[0][0]);
    MF16E("v[44:47]", "v[44:47]", "v[44:47]", acc4[11], MK(Al[2][1], Ah[2][1]), Bg[0][1]);
    DSTR(Al[2][1], wB[2][1], 43008);
    DSTR(Ah[2][1], wB[2][1], 43776);
    WAITLB(8);
    MF(cs2[0], MK(Al[0][0], Ah[0][0]), sel);
    ADDM0(m0x, 32768);
    MF16E("v[0:3]", "v[0:3]", "v[0:3]", acc4[0], MK(Al[0][0], Ah[0][0]), Bg[1][0]);
    DMAO(rq, vxj[0], sxo[6], 0);
    WAITV(11);
    BAR();
    MF16E("v[8:11]", "v[8:11]", "v[8:11]", acc4[2], MK(Al[0][0], Ah[0][0]), Bg[1][1]);
    DSTR(Al[0][0], wB[0][0], 49152);
    DSTR(Ah[0][0], wB[0][0], 49920);
    MF(cs2[1], MK(Al[0][1], Ah[0][1]), sel);
    DSR128(Bg[0][0], pB[0], 0);
    DSR128(Bg[0][1], pB[0], 2048);
    MF16E("v[4:7]", "v[4:7]", "v[4:7]", acc4[1], MK(Al[0][1], Ah[0][1]), Bg[1][0]);
    { u32 so_ = ch0 * 4096u + w * 1024u; TIES(so_); asm volatile("buffer_load_dword %0, %1, %2, %3 offen" : "=v"(pf[5]) : "v"(opf[5]), "s"(rsk), "s"(so_)); }
    MF16E("v[12:15]", "v[12:15]", "v[12:15]", acc4[3], MK(Al[0][1], Ah[0][1]), Bg[1][1]);
    DSTR(Al[0][1], wB[0][1], 49152);
    DSTR(Ah[0][1], wB[0][1], 49920);
    WAITLB(10);
    MF16E("v[16:19]", "v[16:19]", "v[16:19]", acc4[4], MK(Al[1][0], Ah[1][0]), Bg[1][0]);
    DMAO(rq, vxj[1], sxo[6], 1024);
    MF16E("v[24:27]", "v[24:27]", "v[24:27]", acc4[6], MK(Al[1][0], Ah[1][0]), Bg[1][1]);
    DSTR(Al[1][0], wB[1][0], 49152);
    DSTR(Ah[1][0], wB[1][0], 49920);
    MF16E("v[20:23]", "v[20:23]", "v[20:23]", acc4[5], MK(Al[1][1], Ah[1][1]), Bg[1][0]);
    DMAO(rq, vxj[2], sxo[6], 2048);
    MF16E("v[28:31]", "v[28:31]", "v[28:31]", acc4[7], MK(Al[1][1], Ah[1][1]), Bg[1][1]);
    DSTR(Al[1][1], wB[1][1], 49152);
    DSTR(Ah[1][1], wB[1][1], 49920);
    { const u32 lb = lds0 + CXP + 512 + 16 * (2 * i16 + g - 2) - 448;
      const int sF = (w & 1) ? 5 : 0, sH = 4 * (w >> 1);
      pbF = lb + (u32)rfl((w >> 1) * CXS - 64 * sF); pbH = lb + (u32)rfl(2 * CXS - 64 * sH);
      aX = lds0 + CXP + 512 + (2 * i16 + (g >> 1)) * 16 + ((g & 1) ? 0 : 8) + (u32)rfl((w >> 1) * CXS);
      TIE(pbF); TIE(pbH); TIE(aX); }
    SCHED();
    WAITLB(10);
    MF16E("v[32:35]", "v[32:35]", "v[32:35]", acc4[8], MK(Al[2][0], Ah[2][0]), Bg[1][0]);
    MF16E("v[40:43]", "v[40:43]", "v[40:43]", acc4[10], MK(Al[2][0], Ah[2][0]), Bg[1][1]);
    DSTR(Al[2][0], wB[2][0], 49152);
    DSTR(Ah[2][0], wB[2][0], 49920);
    MF16E("v[36:39]", "v[36:39]", "v[36:39]", acc4[9], MK(Al[2][1], Ah[2][1]), Bg[1][0]);
    DMAO(rq, vxj[3], sxo[6], 3072);
    MF16E("v[44:47]", "v[44:47]", "v[44:47]", acc4[11], MK(Al[2][1], Ah[2][1]), Bg[1][1]);
    DSTR(Al[2][1], wB[2][1], 49152);
    DSTR(Ah[2][1], wB[2][1], 49920);
    WAITLB(8);
    MF(cs2[0], MK(Al[0][0], Ah[0][0]), sel);
    ADDM0(m0w, 24576);
    MF16E("v[0:3]", "v[0:3]", "v[0:3]", acc4[0], MK(Al[0][0], Ah[0][0]), Bg[0][0]);
    DMAO(rw1, vwj[0], swo[7], 0);
    MF16E("v[8:11]", "v[8:11]", "v[8:11]", acc4[2], MK(Al[0][0], Ah[0][0]), Bg[0][1]);
    DSTR(Al[0][0], wB[0][0], 55296);
    DSTR(Ah[0][0], wB[0][0], 56064);
    MF(cs2[1], MK(Al[0][1], Ah[0][1]), sel);
    DSR128(Bg[1][0], pB[1], 0);
    DSR128(Bg[1][1], pB[1], 2048);
    MF16E("v[4:7]", "v[4:7]", "v[4:7]", acc4[1], MK(Al[0][1], Ah[0][1]), Bg[0][0]);
    MF16E("v[12:15]", "v[12:15]", "v[12:15]", acc4[3], MK(Al[0][1], Ah[0][1]), Bg[0][1]);
    DSTR(Al[0][1], wB[0][1], 55296);
    DSTR(Ah[0][1], wB[0][1], 56064);
    WAITLB(10);
    MF16E("v[16:19]", "v[16:19]", "v[16:19]", acc4[4], MK(Al[1][0], Ah[1][0]), Bg[0][0]);
    DMAO(rw1, vwj[1], swo[7], 1024);
    MF16E("v[24:27]", "v[24:27]", "v[24:27]", acc4[6], MK(Al[1][0], Ah[1][0]), Bg[0][1]);
    DSTR(Al[1][0], wB[1][0], 55296);
    DSTR(Ah[1][0], wB[1][0], 56064);
    MF16E("v[20:23]", "v[20:23]", "v[20:23]", acc4[5], MK(Al[1][1], Ah[1][1]), Bg[0][0]);
    { u32 vt0 = tid == 0 ? 0u : OOB; TIE(vt0);
      vfH = vt0 | (u32)rfl(WS_FH + (m1 * 16 + n1) * 8); vfY = vt0 | (u32)rfl(WS_FY + (n2 * 32 + m) * 8);
      pollh = (u32)i16 * 8 + (u32)rfl(WS_FH + m1 * 128); polly = (u32)l31 * 8 + (u32)rfl(WS_FY + n2 * 256);
      u32 s0_ = blk == 0 ? WS_E : OOB, s1_ = (blk | w) == 0 ? WS_FP : OOB; TIES(s0_); TIES(s1_);
      vE = vt0 | s0_; vPE = vl16 + s1_;
      TIE(vfH); TIE(vfY); TIE(pollh); TIE(polly); TIE(vPE); TIE(vE); }
    SCHED();
    MF16E("v[28:31]", "v[28:31]", "v[28:31]", acc4[7], MK(Al[1][1], Ah[1][1]), Bg[0][1]);
    DSTR(Al[1][1], wB[1][1], 55296);
    DSTR(Ah[1][1], wB[1][1], 56064);
    WAITLB(10);
    MF16E("v[32:35]", "v[32:35]", "v[32:35]", acc4[8], MK(Al[2][0], Ah[2][0]), Bg[0][0]);
    DMAO(rw1, vwj[2], swo[7], 2048);
    MF16E("v[40:43]", "v[40:43]", "v[40:43]", acc4[10], MK(Al[2][0], Ah[2][0]), Bg[0][1]);
    DSTR(Al[2][0], wB[2][0], 55296);
    DSTR(Ah[2][0], wB[2][0], 56064);
    MF16E("v[36:39]", "v[36:39]", "v[36:39]", acc4[9], MK(Al[2][1], Ah[2][1]), Bg[0][0]);
    MF16E("v[44:47]", "v[44:47]", "v[44:47]", acc4[11], MK(Al[2][1], Ah[2][1]), Bg[0][1]);
    DSTR(Al[2][1], wB[2][1], 55296);
    DSTR(Ah[2][1], wB[2][1], 56064);
    WAITLB(8);
    MF(cs2[0], MK(Al[0][0], Ah[0][0]), sel);
    ADDM0(m0x, 49152);
    MF16E("v[0:3]", "v[0:3]", "v[0:3]", acc4[0], MK(Al[0][0], Ah[0][0]), Bg[1][0]);
    DMAO(rq, vxj[0], sxo[7], 0);
    WAITV(12);
    BAR();
    MF16E("v[8:11]", "v[8:11]", "v[8:11]", acc4[2], MK(Al[0][0], Ah[0][0]), Bg[1][1]);
    DSTR(Al[0][0], wB[0][0], 0);
    DSTR(Ah[0][0], wB[0][0], 768);
    MF(cs2[1], MK(Al[0][1], Ah[0][1]), sel);
    DSR128(Bg[0][0], pB[0], 16384);
    DSR128(Bg[0][1], pB[0], 18432);
    MF16E("v[4:7]", "v[4:7]", "v[4:7]", acc4[1], MK(Al[0][1], Ah[0][1]), Bg[1][0]);
    { u32 so_ = (w >= 2 ? ch2 : ch0 + w) * 4096u; TIES(so_); asm volatile("buffer_load_ushort %0, %1, %2, %3 offen" : "=v"(pref) : "v"(opref), "s"(rsk), "s"(so_)); }
    { const u32 od_ = 2u * (lane < 2 ? (u32)ch0 + (u32)lane : (u32)ch2); asm volatile("global_load_ushort %0, %1, %2" : "=v"(ddF) : "v"(od_), "s"(p_ssmD)); }
    MF16E("v[12:15]", "v[12:15]", "v[12:15]", acc4[3], MK(Al[0][1], Ah[0][1]), Bg[1][1]);
    DSTR(Al[0][1], wB[0][1], 0);
    DSTR(Ah[0][1], wB[0][1], 768);
    WAITLB(10);
    MF16E("v[16:19]", "v[16:19]", "v[16:19]", acc4[4], MK(Al[1][0], Ah[1][0]), Bg[1][0]);
    DMAO(rq, vxj[1], sxo[7], 1024);
    MF16E("v[24:27]", "v[24:27]", "v[24:27]", acc4[6], MK(Al[1][0], Ah[1][0]), Bg[1][1]);
    DSTR(Al[1][0], wB[1][0], 0);
    DSTR(Ah[1][0], wB[1][0], 768);
    MF16E("v[20:23]", "v[20:23]", "v[20:23]", acc4[5], MK(Al[1][1], Ah[1][1]), Bg[1][0]);
    DMAO(rq, vxj[2], sxo[7], 2048);
    MF16E("v[28:31]", "v[28:31]", "v[28:31]", acc4[7], MK(Al[1][1], Ah[1][1]), Bg[1][1]);
    DSTR(Al[1][1], wB[1][1], 0);
    DSTR(Ah[1][1], wB[1][1], 768);
    { aEx = vl16 + (u32)rfl(lds0 + CX + (w >> 1) * 8192); aGr = vl16 + (u32)rfl(lds0 + GX + w * 15 * 1024);
      oY = (u32)(16 * i16 + 4 * g) * 2 + (u32)rfl(WS_YT + ((w >> 1) ? ch1 : ch0) * 4096);
      TIE(aEx); TIE(aGr); TIE(oY); }
    SCHED();
    WAITLB(10);
    MF16E("v[32:35]", "v[32:35]", "v[32:35]", acc4[8], MK(Al[2][0], Ah[2][0]), Bg[1][0]);
    MF16E("v[40:43]", "v[40:43]", "v[40:43]", acc4[10], MK(Al[2][0], Ah[2][0]), Bg[1][1]);
    DSTR(Al[2][0], wB[2][0], 0);
    DSTR(Ah[2][0], wB[2][0], 768);
    MF16E("v[36:39]", "v[36:39]", "v[36:39]", acc4[9], MK(Al[2][1], Ah[2][1]), Bg[1][0]);
    DMAO(rq, vxj[3], sxo[7], 3072);
    MF16E("v[44:47]", "v[44:47]", "v[44:47]", acc4[11], MK(Al[2][1], Ah[2][1]), Bg[1][1]);
    DSTR(Al[2][1], wB[2][1], 0);
    DSTR(Ah[2][1], wB[2][1], 768);
    WAITLB(8);
    MF(cs2[0], MK(Al[0][0], Ah[0][0]), sel);
    ADDM0(m0w, 36864);
    MF16E("v[0:3]", "v[0:3]", "v[0:3]", acc4[0], MK(Al[0][0], Ah[0][0]), Bg[0][0]);
    DMAO(rw1, vwj[0], swo[8], 0);
    MF16E("v[8:11]", "v[8:11]", "v[8:11]", acc4[2], MK(Al[0][0], Ah[0][0]), Bg[0][1]);
    DSTR(Al[0][0], wB[0][0], 6144);
    DSTR(Ah[0][0], wB[0][0], 6912);
    MF(cs2[1], MK(Al[0][1], Ah[0][1]), sel);
    DSR128(Bg[1][0], pB[1], 16384);
    DSR128(Bg[1][1], pB[1], 18432);
    MF16E("v[4:7]", "v[4:7]", "v[4:7]", acc4[1], MK(Al[0][1], Ah[0][1]), Bg[0][0]);
    MF16E("v[12:15]", "v[12:15]", "v[12:15]", acc4[3], MK(Al[0][1], Ah[0][1]), Bg[0][1]);
    DSTR(Al[0][1], wB[0][1], 6144);
    DSTR(Ah[0][1], wB[0][1], 6912);
    WAITLB(10);
    MF16E("v[16:19]", "v[16:19]", "v[16:19]", acc4[4], MK(Al[1][0], Ah[1][0]), Bg[0][0]);
    DMAO(rw1, vwj[1], swo[8], 1024);
    MF16E("v[24:27]", "v[24:27]", "v[24:27]", acc4[6], MK(Al[1][0], Ah[1][0]), Bg[0][1]);
    DSTR(Al[1][0], wB[1][0], 6144);
    DSTR(Ah[1][0], wB[1][0], 6912);
    MF16E("v[20:23]", "v[20:23]", "v[20:23]", acc4[5], MK(Al[1][1], Ah[1][1]), Bg[0][0]);
    { gxw0 = vl16 + (u32)rfl(lds0 + GX + ((w + 1) & 3) * 15 * 1024); gxw1 = vl16 + (u32)rfl(lds0 + GX + ((w + 2) & 3) * 15 * 1024);
      gxw2 = vl16 + (u32)rfl(lds0 + GX + ((w + 3) & 3) * 15 * 1024);
      vyt = (u32)((lane >> 3) * 4096 + (((lane & 7) ^ ((lane >> 3) & 6)) * 16));
      TIE(gxw0); TIE(gxw1); TIE(gxw2); TIE(vyt); }
    SCHED();
    MF16E("v[28:31]", "v[28:31]", "v[28:31]", acc4[7], MK(Al[1][1], Ah[1][1]), Bg[0][1]);
    DSTR(Al[1][1], wB[1][1], 6144);
    DSTR(Ah[1][1], wB[1][1], 6912);
    WAITLB(10);
    MF16E("v[32:35]", "v[32:35]", "v[32:35]", acc4[8], MK(Al[2][0], Ah[2][0]), Bg[0][0]);
    DMAO(rw1, vwj[2], swo[8], 2048);
    MF16E("v[40:43]", "v[40:43]", "v[40:43]", acc4[10], MK(Al[2][0], Ah[2][0]), Bg[0][1]);
    DSTR(Al[2][0], wB[2][0], 6144);
    DSTR(Ah[2][0], wB[2][0], 6912);
    MF16E("v[36:39]", "v[36:39]", "v[36:39]", acc4[9], MK(Al[2][1], Ah[2][1]), Bg[0][0]);
    MF16E("v[44:47]", "v[44:47]", "v[44:47]", acc4[11], MK(Al[2][1], Ah[2][1]), Bg[0][1]);
    DSTR(Al[2][1], wB[2][1], 6144);
    DSTR(Ah[2][1], wB[2][1], 6912);
    WAITLB(8);
    MF(cs2[0], MK(Al[0][0], Ah[0][0]), sel);
    ADDM0(m0x, 0);
    MF16E("v[0:3]", "v[0:3]", "v[0:3]", acc4[0], MK(Al[0][0], Ah[0][0]), Bg[1][0]);
    DMAO(rq, vxj[0], sxo[8], 0);
    WAITV(13);
    TIE(pf[5]);
    BAR();
    MF16E("v[8:11]", "v[8:11]", "v[8:11]", acc4[2], MK(Al[0][0], Ah[0][0]), Bg[1][1]);
    DSTR(Al[0][0], wB[0][0], 12288);
    DSTR(Ah[0][0], wB[0][0], 13056);
    MF(cs2[1], MK(Al[0][1], Ah[0][1]), sel);
    DSR128(Bg[0][0], pB[0], 32768);
    DSR128(Bg[0][1], pB[0], 34816);
    MF16E("v[4:7]", "v[4:7]", "v[4:7]", acc4[1], MK(Al[0][1], Ah[0][1]), Bg[1][0]);
    MF16E("v[12:15]", "v[12:15]", "v[12:15]", acc4[3], MK(Al[0][1], Ah[0][1]), Bg[1][1]);
    DSTR(Al[0][1], wB[0][1], 12288);
    DSTR(Ah[0][1], wB[0][1], 13056);
    WAITLB(10);
    MF16E("v[16:19]", "v[16:19]", "v[16:19]", acc4[4], MK(Al[1][0], Ah[1][0]), Bg[1][0]);
    DMAO(rq, vxj[1], sxo[8], 1024);
    MF16E("v[24:27]", "v[24:27]", "v[24:27]", acc4[6], MK(Al[1][0], Ah[1][0]), Bg[1][1]);
    DSTR(Al[1][0], wB[1][0], 12288);
    DSTR(Ah[1][0], wB[1][0], 13056);
    MF16E("v[20:23]", "v[20:23]", "v[20:23]", acc4[5], MK(Al[1][1], Ah[1][1]), Bg[1][0]);
    DMAO(rq, vxj[2], sxo[8], 2048);
    MF16E("v[28:31]", "v[28:31]", "v[28:31]", acc4[7], MK(Al[1][1], Ah[1][1]), Bg[1][1]);
    DSTR(Al[1][1], wB[1][1], 12288);
    DSTR(Ah[1][1], wB[1][1], 13056);
    { u32 w2_ = w < 2 ? w : 2; TIES(w2_);
      aPre = (u32)g * CPY + (u32)i16 * 2 + lds0 + csel32_eq(w2_, 0, CF0, csel32_eq(w2_, 1, CF1, CF2)); aZ = (u32)lane * 8 + (lds0 + CXP + w2_ * CXS);
      TIE(aPre); TIE(aZ); }
    SCHED();
    WAITLB(10);
    MF16E("v[32:35]", "v[32:35]", "v[32:35]", acc4[8], MK(Al[2][0], Ah[2][0]), Bg[1][0]);
    MF16E("v[40:43]", "v[40:43]", "v[40:43]", acc4[10], MK(Al[2][0], Ah[2][0]), Bg[1][1]);
    DSTR(Al[2][0], wB[2][0], 12288);
    DSTR(Ah[2][0], wB[2][0], 13056);
    MF16E("v[36:39]", "v[36:39]", "v[36:39]", acc4[9], MK(Al[2][1], Ah[2][1]), Bg[1][0]);
    DMAO(rq, vxj[3], sxo[8], 3072);
    MF16E("v[44:47]", "v[44:47]", "v[44:47]", acc4[11], MK(Al[2][1], Ah[2][1]), Bg[1][1]);
    DSTR(Al[2][1], wB[2][1], 12288);
    DSTR(Ah[2][1], wB[2][1], 13056);
    STAMP(11);
    WAITLB(8);
    MF(cs2[0], MK(Al[0][0], Ah[0][0]), sel);
    ADDM0(m0w, 49152);
    MF16E("v[0:3]", "v[0:3]", "v[0:3]", acc4[0], MK(Al[0][0], Ah[0][0]), Bg[0][0]);
    DMAO(rw1, vwj[0], swo[9], 0);
    MF16E("v[8:11]", "v[8:11]", "v[8:11]", acc4[2], MK(Al[0][0], Ah[0][0]), Bg[0][1]);
    DSTR(Al[0][0], wB[0][0], 18432);
    DSTR(Ah[0][0], wB[0][0], 19200);
    MF(cs2[1], MK(Al[0][1], Ah[0][1]), sel);
    DSR128(Bg[1][0], pB[1], 32768);
    DSR128(Bg[1][1], pB[1], 34816);
    MF16E("v[4:7]", "v[4:7]", "v[4:7]", acc4[1], MK(Al[0][1], Ah[0][1]), Bg[0][0]);
    MF16E("v[12:15]", "v[12:15]", "v[12:15]", acc4[3], MK(Al[0][1], Ah[0][1]), Bg[0][1]);
    DSTR(Al[0][1], wB[0][1], 18432);
    DSTR(Ah[0][1], wB[0][1], 19200);
    WAITLB(10);
    MF16E("v[16:19]", "v[16:19]", "v[16:19]", acc4[4], MK(Al[1][0], Ah[1][0]), Bg[0][0]);
    DMAO(rw1, vwj[1], swo[9], 1024);
    MF16E("v[24:27]", "v[24:27]", "v[24:27]", acc4[6], MK(Al[1][0], Ah[1][0]), Bg[0][1]);
    DSTR(Al[1][0], wB[1][0], 18432);
    DSTR(Ah[1][0], wB[1][0], 19200);
    MF16E("v[20:23]", "v[20:23]", "v[20:23]", acc4[5], MK(Al[1][1], Ah[1][1]), Bg[0][0]);
    POLL_LOAD(pollv, pollo);
    MF16E("v[28:31]", "v[28:31]", "v[28:31]", acc4[7], MK(Al[1][1], Ah[1][1]), Bg[0][1]);
    DSTR(Al[1][1], wB[1][1], 18432);
    DSTR(Ah[1][1], wB[1][1], 19200);
    { const int row = 4 * g + qq;
      aYT = lds0 + YT + row * 128 + (((2 * tw + (pp >> 1)) ^ (row & 6)) * 16) + (pp & 1) * 8;
      aB2 = (u32)(8 * g) + (lds0 + B2L); vq = (u32)i16 * 1280u + (u32)(8 * g) + (u32)rfl((64 * m + 16 * tw) * 1280 + 160 * n2);
      TIE(aYT); TIE(aB2); TIE(vq); }
    SCHED();
    WAITLB(10);
    MF16E("v[32:35]", "v[32:35]", "v[32:35]", acc4[8], MK(Al[2][0], Ah[2][0]), Bg[0][0]);
    DMAO(rw1, vwj[2], swo[9], 2048);
    MF16E("v[40:43]", "v[40:43]", "v[40:43]", acc4[10], MK(Al[2][0], Ah[2][0]), Bg[0][1]);
    DSTR(Al[2][0], wB[2][0], 18432);
    DSTR(Ah[2][0], wB[2][0], 19200);
    MF16E("v[36:39]", "v[36:39]", "v[36:39]", acc4[9], MK(Al[2][1], Ah[2][1]), Bg[0][0]);
    MF16E("v[44:47]", "v[44:47]", "v[44:47]", acc4[11], MK(Al[2][1], Ah[2][1]), Bg[0][1]);
    DSTR(Al[2][1], wB[2][1], 18432);
    DSTR(Ah[2][1], wB[2][1], 19200);
    WAITLB(8);
    MF(cs2[0], MK(Al[0][0], Ah[0][0]), sel);
    ADDM0(m0x, 16384);
    MF16E("v[0:3]", "v[0:3]", "v[0:3]", acc4[0], MK(Al[0][0], Ah[0][0]), Bg[1][0]);
    DMAO(rq, vxj[0], sxo[9], 0);
    WAITV(12);
    BAR();
    MF16E("v[8:11]", "v[8:11]", "v[8:11]", acc4[2], MK(Al[0][0], Ah[0][0]), Bg[1][1]);
    DSTR(Al[0][0], wB[0][0], 24576);
    DSTR(Ah[0][0], wB[0][0], 25344);
    MF(cs2[1], MK(Al[0][1], Ah[0][1]), sel);
    DSR128(Bg[0][0], pB[0], 49152);
    DSR128(Bg[0][1], pB[0], 51200);
    MF16E("v[4:7]", "v[4:7]", "v[4:7]", acc4[1], MK(Al[0][1], Ah[0][1]), Bg[1][0]);
    MF16E("v[12:15]", "v[12:15]", "v[12:15]", acc4[3], MK(Al[0][1], Ah[0][1]), Bg[1][1]);
    DSTR(Al[0][1], wB[0][1], 24576);
    DSTR(Ah[0][1], wB[0][1], 25344);
    WAITLB(10);
    MF16E("v[16:19]", "v[16:19]", "v[16:19]", acc4[4], MK(Al[1][0], Ah[1][0]), Bg[1][0]);
    DMAO(rq, vxj[1], sxo[9], 1024);
    MF16E("v[24:27]", "v[24:27]", "v[24:27]", acc4[6], MK(Al[1][0], Ah[1][0]), Bg[1][1]);
    DSTR(Al[1][0], wB[1][0], 24576);
    DSTR(Ah[1][0], wB[1][0], 25344);
    MF16E("v[20:23]", "v[20:23]", "v[20:23]", acc4[5], MK(Al[1][1], Ah[1][1]), Bg[1][0]);
    DMAO(rq, vxj[2], sxo[9], 2048);
    MF16E("v[28:31]", "v[28:31]", "v[28:31]", acc4[7], MK(Al[1][1], Ah[1][1]), Bg[1][1]);
    DSTR(Al[1][1], wB[1][1], 24576);
    DSTR(Ah[1][1], wB[1][1], 25344);
    { const u32 rr = (u32)(16 * (w & 1)) + (u32)i16;
      u32 qb_ = rr * 128 + ((((u32)g >> 1) ^ (rr >> 1)) & 7) * 16 + ((u32)g & 1) * 8 + (u32)rfl((w >> 1) * 4096 + par * 8192), mk_ = 0x4060u; TIE(qb_); TIE(mk_);
      u32 t0_ = (u32)(n2 & 3) * 0x1020u; TIES(t0_);
      u32 t1_ = t0_ + 0x1020u, t2_ = t0_ + 2 * 0x1020u, t3_ = t0_ + 3 * 0x1020u, t4_ = t0_ + 4 * 0x1020u; TIES(t1_); TIES(t2_); TIES(t3_); TIES(t4_);
      aQ0 = qb_ ^ (t0_ & mk_); aQ1 = qb_ ^ (t1_ & mk_); aQ2 = qb_ ^ (t2_ & mk_); aQ3 = qb_ ^ (t3_ & mk_); aQ4 = qb_ ^ (t4_ & mk_);
      TIE(aQ0); TIE(aQ1); TIE(aQ2); TIE(aQ3); TIE(aQ4); }
    SCHED();
    WAITLB(10);
    MF16E("v[32:35]", "v[32:35]", "v[32:35]", acc4[8], MK(Al[2][0], Ah[2][0]), Bg[1][0]);
    MF16E("v[40:43]", "v[40:43]", "v[40:43]", acc4[10], MK(Al[2][0], Ah[2][0]), Bg[1][1]);
    DSTR(Al[2][0], wB[2][0], 24576);
    DSTR(Ah[2][0], wB[2][0], 25344);
    MF16E("v[36:39]", "v[36:39]", "v[36:39]", acc4[9], MK(Al[2][1], Ah[2][1]), Bg[1][0]);
    DMAO(rq, vxj[3], sxo[9], 3072);
    MF16E("v[44:47]", "v[44:47]", "v[44:47]", acc4[11], MK(Al[2][1], Ah[2][1]), Bg[1][1]);
    DSTR(Al[2][1], wB[2][1], 24576);
    DSTR(Ah[2][1], wB[2][1], 25344);
    WAITLB(8);
    MF(cs2[0], MK(Al[0][0], Ah[0][0]), sel);
    MF16E("v[0:3]", "v[0:3]", "v[0:3]", acc4[0], MK(Al[0][0], Ah[0][0]), Bg[0][0]);
    LDFP("v[96:99]", "x4", ch0 * 4096 + w * 1024, 0);
    MF16E("v[8:11]", "v[8:11]", "v[8:11]", acc4[2], MK(Al[0][0], Ah[0][0]), Bg[0][1]);
    DSTR(Al[0][0], wB[0][0], 30720);
    DSTR(Ah[0][0], wB[0][0], 31488);
    MF(cs2[1], MK(Al[0][1], Ah[0][1]), sel);
    DSR128(Bg[1][0], pB[1], 49152);
    DSR128(Bg[1][1], pB[1], 51200);
    MF16E("v[4:7]", "v[4:7]", "v[4:7]", acc4[1], MK(Al[0][1], Ah[0][1]), Bg[0][0]);
    MF16E("v[12:15]", "v[12:15]", "v[12:15]", acc4[3], MK(Al[0][1], Ah[0][1]), Bg[0][1]);
    DSTR(Al[0][1], wB[0][1], 30720);
    DSTR(Ah[0][1], wB[0][1], 31488);
    WAITLB(10);
    MF16E("v[16:19]", "v[16:19]", "v[16:19]", acc4[4], MK(Al[1][0], Ah[1][0]), Bg[0][0]);
    LDFP("v[100:103]", "x4", ch0 * 4096 + w * 1024, 4);
    MF16E("v[24:27]", "v[24:27]", "v[24:27]", acc4[6], MK(Al[1][0], Ah[1][0]), Bg[0][1]);
    DSTR(Al[1][0], wB[1][0], 30720);
    DSTR(Ah[1][0], wB[1][0], 31488);
    MF16E("v[20:23]", "v[20:23]", "v[20:23]", acc4[5], MK(Al[1][1], Ah[1][1]), Bg[0][0]);
    WAITV(7);
    CHECK_FLAGS(pollv, pollo, 1);
    { u32 so_ = WS_STATS + (128 * m1 + 32 * w) * 16; TIES(so_);
    asm volatile("buffer_load_dwordx2 %0, %1, %2, %3 offen sc1" : "=v"(st0) : "v"(ost), "s"(rws), "s"(so_));
    asm volatile("buffer_load_dwordx2 %0, %1, %2, %3 offen offset:256 sc1" : "=v"(st1) : "v"(ost), "s"(rws), "s"(so_)); }
    MF16E("v[28:31]", "v[28:31]", "v[28:31]", acc4[7], MK(Al[1][1], Ah[1][1]), Bg[0][1]);
    DSTR(Al[1][1], wB[1][1], 30720);
    DSTR(Ah[1][1], wB[1][1], 31488);
    { u32 so_ = WS_STATS + (64 * m + 16 * tw) * 16 + 8; TIES(so_); asm volatile("buffer_load_dword %0, %1, %2, %3 offen sc1" : "=v"(gate_tok) : "v"(ost), "s"(rws), "s"(so_)); }
    ADDM0(m0c, CXP + 0 * CXS + 512);
    WAITLB(10);
    MF16E("v[32:35]", "v[32:35]", "v[32:35]", acc4[8], MK(Al[2][0], Ah[2][0]), Bg[0][0]);
    DMA1(rws, vl16, WS_KVT + ch0 * 4096 + w * 1024);
    MF16E("v[40:43]", "v[40:43]", "v[40:43]", acc4[10], MK(Al[2][0], Ah[2][0]), Bg[0][1]);
    DSTR(Al[2][0], wB[2][0], 30720);
    DSTR(Ah[2][0], wB[2][0], 31488);
    MF16E("v[36:39]", "v[36:39]", "v[36:39]", acc4[9], MK(Al[2][1], Ah[2][1]), Bg[0][0]);
    MF16E("v[44:47]", "v[44:47]", "v[44:47]", acc4[11], MK(Al[2][1], Ah[2][1]), Bg[0][1]);
    DSTR(Al[2][1], wB[2][1], 30720);
    DSTR(Ah[2][1], wB[2][1], 31488);
    WAITLB(8);
    MF(cs2[0], MK(Al[0][0], Ah[0][0]), sel);
    ADDM0(m0c, CXP + 1 * CXS + 512);
    MF16E("v[0:3]", "v[0:3]", "v[0:3]", acc4[0], MK(Al[0][0], Ah[0][0]), Bg[1][0]);
    DMA1(rws, vl16, WS_KVT + ch1 * 4096 + w * 1024);
    BAR();
    MF16E("v[8:11]", "v[8:11]", "v[8:11]", acc4[2], MK(Al[0][0], Ah[0][0]), Bg[1][1]);
    DSTR(Al[0][0], wB[0][0], 36864);
    DSTR(Ah[0][0], wB[0][0], 37632);
    MF(cs2[1], MK(Al[0][1], Ah[0][1]), sel);
    DSR128(Bg[0][0], pB[0], 0);
    DSR128(Bg[0][1], pB[0], 2048);
    MF16E("v[4:7]", "v[4:7]", "v[4:7]", acc4[1], MK(Al[0][1], Ah[0][1]), Bg[1][0]);
    { const u32 hb = WS_H + (m * 4 * 48 + 12 * w) * 1024;
      hoff0 = hb + ((tw + 1) & 3) * 49152; hoff1 = hb + ((tw + 2) & 3) * 49152; hoff2 = hb + ((tw + 3) & 3) * 49152; hoff3 = hb + tw * 49152;
      TIES(hoff0); TIES(hoff1); TIES(hoff2); TIES(hoff3); }
    SCHED();
    MF16E("v[12:15]", "v[12:15]", "v[12:15]", acc4[3], MK(Al[0][1], Ah[0][1]), Bg[1][1]);
    DSTR(Al[0][1], wB[0][1], 36864);
    DSTR(Ah[0][1], wB[0][1], 37632);
    ADDM0(m0c, CXP + 2 * CXS + 512);
    WAITLB(10);
    MF16E("v[16:19]", "v[16:19]", "v[16:19]", acc4[4], MK(Al[1][0], Ah[1][0]), Bg[1][0]);
    DMA1(rws, vl16, WS_KVT + ch2 * 4096 + w * 1024);
    MF16E("v[24:27]", "v[24:27]", "v[24:27]", acc4[6], MK(Al[1][0], Ah[1][0]), Bg[1][1]);
    DSTR(Al[1][0], wB[1][0], 36864);
    DSTR(Ah[1][0], wB[1][0], 37632);
    MF16E("v[20:23]", "v[20:23]", "v[20:23]", acc4[5], MK(Al[1][1], Ah[1][1]), Bg[1][0]);
    LDFP("v104", "", ch0 * 4096 + w * 1024, 20);
    MF16E("v[28:31]", "v[28:31]", "v[28:31]", acc4[7], MK(Al[1][1], Ah[1][1]), Bg[1][1]);
    DSTR(Al[1][1], wB[1][1], 36864);
    DSTR(Ah[1][1], wB[1][1], 37632);
    { const u32 yb = WS_YT + (80 * n2 + 8 * w) * 4096 + 128 * m;
      ys0 = yb; ys1 = yb + 32 * 4096; ys2 = w < 2 ? yb + 64 * 4096 : OOB;
      ym0 = lds0 + YT + w * 1024; ym1 = ym0 + 4096; ym2 = lds0 + YT + (w < 2 ? w + 8 : 10) * 1024;
      TIES(ys0); TIES(ys1); TIES(ys2); TIES(ym0); TIES(ym1); TIES(ym2); }
    SCHED();
    WAITLB(10);
    MF16E("v[32:35]", "v[32:35]", "v[32:35]", acc4[8], MK(Al[2][0], Ah[2][0]), Bg[1][0]);
    MF16E("v[40:43]", "v[40:43]", "v[40:43]", acc4[10], MK(Al[2][0], Ah[2][0]), Bg[1][1]);
    DSTR(Al[2][0], wB[2][0], 36864);
    DSTR(Ah[2][0], wB[2][0], 37632);
    MF16E("v[36:39]", "v[36:39]", "v[36:39]", acc4[9], MK(Al[2][1], Ah[2][1]), Bg[1][0]);
    LDFP("v[106:109]", "x4", ch1 * 4096 + w * 1024, 0);
    MF16E("v[44:47]", "v[44:47]", "v[44:47]", acc4[11], MK(Al[2][1], Ah[2][1]), Bg[1][1]);
    DSTR(Al[2][1], wB[2][1], 36864);
    DSTR(Ah[2][1], wB[2][1], 37632);
    WAITLB(8);
    MF(cs2[0], MK(Al[0][0], Ah[0][0]), sel);
    MF16E("v[0:3]", "v[0:3]", "v[0:3]", acc4[0], MK(Al[0][0], Ah[0][0]), Bg[0][0]);
    LDFP("v[110:113]", "x4", ch1 * 4096 + w * 1024, 4);
    MF16E("v[8:11]", "v[8:11]", "v[8:11]", acc4[2], MK(Al[0][0], Ah[0][0]), Bg[0][1]);
    DSTR(Al[0][0], wB[0][0], 43008);
    DSTR(Ah[0][0], wB[0][0], 43776);
    MF(cs2[1], MK(Al[0][1], Ah[0][1]), sel);
    DSR128(Bg[1][0], pB[1], 0);
    DSR128(Bg[1][1], pB[1], 2048);
    MF16E("v[4:7]", "v[4:7]", "v[4:7]", acc4[1], MK(Al[0][1], Ah[0][1]), Bg[0][0]);
    { constexpr float C1 = -2.3022081983f, C2 = C1 * 0.044715f;
      cc = u32x2{__float_as_uint(C2), __float_as_uint(C1)}; dY2 = (u32)(ch2 - ((w >> 1) ? ch1 : ch0)) * 4096;
      TIES(cc); TIES(dY2); }
    SCHED();
    MF16E("v[12:15]", "v[12:15]", "v[12:15]", acc4[3], MK(Al[0][1], Ah[0][1]), Bg[0][1]);
    DSTR(Al[0][1], wB[0][1], 43008);
    DSTR(Ah[0][1], wB[0][1], 43776);
    WAITLB(10);
    MF16E("v[16:19]", "v[16:19]", "v[16:19]", acc4[4], MK(Al[1][0], Ah[1][0]), Bg[0][0]);
    LDFP("v114", "", ch1 * 4096 + w * 1024, 20);
    MF16E("v[24:27]", "v[24:27]", "v[24:27]", acc4[6], MK(Al[1][0], Ah[1][0]), Bg[0][1]);
    DSTR(Al[1][0], wB[1][0], 43008);
    DSTR(Ah[1][0], wB[1][0], 43776);
    MF16E("v[20:23]", "v[20:23]", "v[20:23]", acc4[5], MK(Al[1][1], Ah[1][1]), Bg[0][0]);
    MF16E("v[28:31]", "v[28:31]", "v[28:31]", acc4[7], MK(Al[1][1], Ah[1][1]), Bg[0][1]);
    DSTR(Al[1][1], wB[1][1], 43008);
    DSTR(Ah[1][1], wB[1][1], 43776);
    WAITLB(10);
    MF16E("v[32:35]", "v[32:35]", "v[32:35]", acc4[8], MK(Al[2][0], Ah[2][0]), Bg[0][0]);
    LDFP("v[116:119]", "x4", ch2 * 4096 + w * 1024, 0);
    MF16E("v[40:43]", "v[40:43]", "v[40:43]", acc4[10], MK(Al[2][0], Ah[2][0]), Bg[0][1]);
    DSTR(Al[2][0], wB[2][0], 43008);
    DSTR(Ah[2][0], wB[2][0], 43776);
    MF16E("v[36:39]", "v[36:39]", "v[36:39]", acc4[9], MK(Al[2][1], Ah[2][1]), Bg[0][0]);
    MF16E("v[44:47]", "v[44:47]", "v[44:47]", acc4[11], MK(Al[2][1], Ah[2][1]), Bg[0][1]);
    DSTR(Al[2][1], wB[2][1], 43008);
    DSTR(Ah[2][1], wB[2][1], 43776);
    WAITLB(8);
    MF(cs2[0], MK(Al[0][0], Ah[0][0]), sel);
    MF16E("v[0:3]", "v[0:3]", "v[0:3]", acc4[0], MK(Al[0][0], Ah[0][0]), Bg[1][0]);
    LDFP("v[120:123]", "x4", ch2 * 4096 + w * 1024, 4);
    WAITV(14);
    BAR();
    MF16E("v[8:11]", "v[8:11]", "v[8:11]", acc4[2], MK(Al[0][0], Ah[0][0]), Bg[1][1]);
    DSTR(Al[0][0], wB[0][0], 49152);
    DSTR(Ah[0][0], wB[0][0], 49920);
    MF(cs2[1], MK(Al[0][1], Ah[0][1]), sel);
    DSR128(Bg[0][0], pB[0], 16384);
    DSR128(Bg[0][1], pB[0], 18432);
    MF16E("v[4:7]", "v[4:7]", "v[4:7]", acc4[1], MK(Al[0][1], Ah[0][1]), Bg[1][0]);
    MF16E("v[12:15]", "v[12:15]", "v[12:15]", acc4[3], MK(Al[0][1], Ah[0][1]), Bg[1][1]);
    DSTR(Al[0][1], wB[0][1], 49152);
    DSTR(Ah[0][1], wB[0][1], 49920);
    WAITLB(10);
    MF16E("v[16:19]", "v[16:19]", "v[16:19]", acc4[4], MK(Al[1][0], Ah[1][0]), Bg[1][0]);
    LDFP("v124", "", ch2 * 4096 + w * 1024, 20);
    MF16E("v[24:27]", "v[24:27]", "v[24:27]", acc4[6], MK(Al[1][0], Ah[1][0]), Bg[1][1]);
    DSTR(Al[1][0], wB[1][0], 49152);
    DSTR(Ah[1][0], wB[1][0], 49920);
    MF16E("v[20:23]", "v[20:23]", "v[20:23]", acc4[5], MK(Al[1][1], Ah[1][1]), Bg[1][0]);
    MF16E("v[28:31]", "v[28:31]", "v[28:31]", acc4[7], MK(Al[1][1], Ah[1][1]), Bg[1][1]);
    DSTR(Al[1][1], wB[1][1], 49152);
    DSTR(Ah[1][1], wB[1][1], 49920);
    { u32 so_ = w2o + (r >> 2) * 7680u; TIES(so_); asm volatile("buffer_load_dword a240, %0, %1, %2 offen sc1" ::"v"(opf[3]), "s"(rws), "s"(so_) : "a240"); }
    WAITLB(10);
    MF16E("v[32:35]", "v[32:35]", "v[32:35]", acc4[8], MK(Al[2][0], Ah[2][0]), Bg[1][0]);
    MF16E("v[40:43]", "v[40:43]", "v[40:43]", acc4[10], MK(Al[2][0], Ah[2][0]), Bg[1][1]);
    DSTR(Al[2][0], wB[2][0], 49152);
    DSTR(Ah[2][0], wB[2][0], 49920);
    MF16E("v[36:39]", "v[36:39]", "v[36:39]", acc4[9], MK(Al[2][1], Ah[2][1]), Bg[1][0]);
    MF16E("v[44:47]", "v[44:47]", "v[44:47]", acc4[11], MK(Al[2][1], Ah[2][1]), Bg[1][1]);
    DSTR(Al[2][1], wB[2][1], 49152);
    DSTR(Ah[2][1], wB[2][1], 49920);
    WAITLB(8);
    MF(cs2[0], MK(Al[0][0], Ah[0][0]), sel);
    MF16E("v[0:3]", "v[0:3]", "v[0:3]", acc4[0], MK(Al[0][0], Ah[0][0]), Bg[0][0]);
    MF16E("v[8:11]", "v[8:11]", "v[8:11]", acc4[2], MK(Al[0][0], Ah[0][0]), Bg[0][1]);
    DSTR(Al[0][0], wB[0][0], 55296);
    DSTR(Ah[0][0], wB[0][0], 56064);
    MF(cs2[1], MK(Al[0][1], Ah[0][1]), sel);
    DSR128(Bg[1][0], pB[1], 16384);
    DSR128(Bg[1][1], pB[1], 18432);
    MF16E("v[4:7]", "v[4:7]", "v[4:7]", acc4[1], MK(Al[0][1], Ah[0][1]), Bg[0][0]);
    MF16E("v[12:15]", "v[12:15]", "v[12:15]", acc4[3], MK(Al[0][1], Ah[0][1]), Bg[0][1]);
    DSTR(Al[0][1], wB[0][1], 55296);
    DSTR(Ah[0][1], wB[0][1], 56064);
    WAITLB(10);
    MF16E("v[16:19]", "v[16:19]", "v[16:19]", acc4[4], MK(Al[1][0], Ah[1][0]), Bg[0][0]);
    MF16E("v[24:27]", "v[24:27]", "v[24:27]", acc4[6], MK(Al[1][0], Ah[1][0]), Bg[0][1]);
    DSTR(Al[1][0], wB[1][0], 55296);
    DSTR(Ah[1][0], wB[1][0], 56064);
    MF16E("v[20:23]", "v[20:23]", "v[20:23]", acc4[5], MK(Al[1][1], Ah[1][1]), Bg[0][0]);
    MF16E("v[28:31]", "v[28:31]", "v[28:31]", acc4[7], MK(Al[1][1], Ah[1][1]), Bg[0][1]);
    DSTR(Al[1][1], wB[1][1], 55296);
    DSTR(Ah[1][1], wB[1][1], 56064);
    WAITLB(10);
    MF16E("v[32:35]", "v[32:35]", "v[32:35]", acc4[8], MK(Al[2][0], Ah[2][0]), Bg[0][0]);
    MF16E("v[40:43]", "v[40:43]", "v[40:43]", acc4[10], MK(Al[2][0], Ah[2][0]), Bg[0][1]);
    DSTR(Al[2][0], wB[2][0], 55296);
    DSTR(Ah[2][0], wB[2][0], 56064);
    MF16E("v[36:39]", "v[36:39]", "v[36:39]", acc4[9], MK(Al[2][1], Ah[2][1]), Bg[0][0]);
    MF16E("v[44:47]", "v[44:47]", "v[44:47]", acc4[11], MK(Al[2][1], Ah[2][1]), Bg[0][1]);
    DSTR(Al[2][1], wB[2][1], 55296);
    DSTR(Ah[2][1], wB[2][1], 56064);
    WAITLB(8);
    MF(cs2[0], MK(Al[0][0], Ah[0][0]), sel);
    MF16E("v[0:3]", "v[0:3]", "v[0:3]", acc4[0], MK(Al[0][0], Ah[0][0]), Bg[1][0]);
    MF16E("v[8:11]", "v[8:11]", "v[8:11]", acc4[2], MK(Al[0][0], Ah[0][0]), Bg[1][1]);
    MF(cs2[1], MK(Al[0][1], Ah[0][1]), sel);
    MF16E("v[4:7]", "v[4:7]", "v[4:7]", acc4[1], MK(Al[0][1], Ah[0][1]), Bg[1][0]);
    MF16E("v[12:15]", "v[12:15]", "v[12:15]", acc4[3], MK(Al[0][1], Ah[0][1]), Bg[1][1]);
    WAITLB(4);
    MF16E("v[16:19]", "v[16:19]", "v[16:19]", acc4[4], MK(Al[1][0], Ah[1][0]), Bg[1][0]);
    MF16E("v[24:27]", "v[24:27]", "v[24:27]", acc4[6], MK(Al[1][0], Ah[1][0]), Bg[1][1]);
    MF16E("v[20:23]", "v[20:23]", "v[20:23]", acc4[5], MK(Al[1][1], Ah[1][1]), Bg[1][0]);
    MF16E("v[28:31]", "v[28:31]", "v[28:31]", acc4[7], MK(Al[1][1], Ah[1][1]), Bg[1][1]);
    asm volatile("s_mov_b32 exec_lo, 0x10001\n\ts_mov_b32 exec_hi, 0x10001\n\tds_write_b128 %0, %1 offset:384\n\tds_write_b128 %0, %2 offset:416\n\ts_mov_b64 exec, -1" ::"v"(aC0), "v"(cs2[0]), "v"(cs2[1]));
    WAITLB(2);
    MF16E("v[32:35]", "v[32:35]", "v[32:35]", acc4[8], MK(Al[2][0], Ah[2][0]), Bg[1][0]);
    MF16E("v[40:43]", "v[40:43]", "v[40:43]", acc4[10], MK(Al[2][0], Ah[2][0]), Bg[1][1]);
    WAITLB(0);
    BAR();
    asm volatile("ds_read_b128 v[164:167], %1\n\tds_read_b128 v[172:175], %1 offset:384\n\tds_read_b128 v[168:171], %1 offset:32\n\tds_read_b128 v[176:179], %1 offset:416" : "={v[164:179]}"(CBV) : "v"(aC0));
    MF16E("v[36:39]", "v[36:39]", "v[36:39]", acc4[9], MK(Al[2][1], Ah[2][1]), Bg[1][0]);
    MF16E("v[44:47]", "v[44:47]", "v[44:47]", acc4[11], MK(Al[2][1], Ah[2][1]), Bg[1][1]);
    STAMP(4);
    }
    WAITV(11);
    TIE(st0); TIE(gate_tok); TIE(st1);
    STAMP(5);
    {
        u32 bt0, bt1; u64 btm;
    asm volatile(
        "s_bitcmp1_b32 %[w], 0\n\t"
        "s_cbranch_scc1 Lodd_%=\n\t"
        "s_waitcnt lgkmcnt(0)\n\t"
        "v_pk_fma_f32 v[180:181], v[172:173], %[st0], v[164:165] op_sel:[0,1,0] op_sel_hi:[1,1,1]\n\t"
        "v_pk_fma_f32 v[182:183], v[174:175], %[st0], v[166:167] op_sel:[0,1,0] op_sel_hi:[1,1,1]\n\t"
        "v_pk_fma_f32 v[184:185], v[176:177], %[st0], v[168:169] op_sel:[0,1,0] op_sel_hi:[1,1,1]\n\t"
        "v_pk_fma_f32 v[186:187], v[178:179], %[st0], v[170:171] op_sel:[0,1,0] op_sel_hi:[1,1,1]\n\t"
        "v_pk_fma_f32 v[180:181], v[0:1], %[st0], v[180:181] op_sel:[0,0,0] op_sel_hi:[1,0,1]\n\t"
        "v_pk_fma_f32 v[182:183], v[2:3], %[st0], v[182:183] op_sel:[0,0,0] op_sel_hi:[1,0,1]\n\t"
        "v_pk_fma_f32 v[184:185], v[4:5], %[st0], v[184:185] op_sel:[0,0,0] op_sel_hi:[1,0,1]\n\t"
        "v_pk_fma_f32 v[186:187], v[6:7], %[st0], v[186:187] op_sel:[0,0,0] op_sel_hi:[1,0,1]\n\t"
        "v_pk_mul_f32 v[188:189], v[180:181], v[180:181]\n\t"
        "v_pk_mul_f32 v[190:191], v[182:183], v[182:183]\n\t"
        "v_pk_mul_f32 v[192:193], v[184:185], v[184:185]\n\t"
        "v_pk_mul_f32 v[194:195], v[186:187], v[186:187]\n\t"
        "v_pk_fma_f32 v[188:189], v[188:189], %[cc], %[cc] op_sel:[0,0,1] op_sel_hi:[1,0,1]\n\t"
        "v_pk_fma_f32 v[190:191], v[190:191], %[cc], %[cc] op_sel:[0,0,1] op_sel_hi:[1,0,1]\n\t"
        "v_pk_fma_f32 v[192:193], v[192:193], %[cc], %[cc] op_sel:[0,0,1] op_sel_hi:[1,0,1]\n\t"
        "v_pk_fma_f32 v[194:195], v[194:195], %[cc], %[cc] op_sel:[0,0,1] op_sel_hi:[1,0,1]\n\t"
        "v_pk_mul_f32 v[188:189], v[188:189], v[180:181]\n\t"
        "v_pk_mul_f32 v[190:191], v[190:191], v[182:183]\n\t"
        "v_pk_mul_f32 v[192:193], v[192:193], v[184:185]\n\t"
        "v_pk_mul_f32 v[194:195], v[194:195], v[186:187]\n\t"
        "v_exp_f32 v188, v188\n\t"
        "v_exp_f32 v189, v189\n\t"
        "v_exp_f32 v190, v190\n\t"
        "s_add_u32 %[t0], %[w2o], 0\n\t"
        "buffer_load_dwordx4 a[0:3], %[vl16], %[rws], %[t0] offen offset:0 sc1\n\t"
        "v_exp_f32 v191, v191\n\t"
        "v_exp_f32 v192, v192\n\t"
        "v_exp_f32 v193, v193\n\t"
        "v_exp_f32 v194, v194\n\t"
        "v_exp_f32 v195, v195\n\t"
        "v_pk_add_f32 v[188:189], v[188:189], 1.0 op_sel_hi:[1,0]\n\t"
        "v_pk_add_f32 v[190:191], v[190:191], 1.0 op_sel_hi:[1,0]\n\t"
        "v_pk_add_f32 v[192:193], v[192:193], 1.0 op_sel_hi:[1,0]\n\t"
        "v_pk_add_f32 v[194:195], v[194:195], 1.0 op_sel_hi:[1,0]\n\t"
        "v_rcp_f32 v188, v188\n\t"
        "v_rcp_f32 v189, v189\n\t"
        "v_rcp_f32 v190, v190\n\t"
        "buffer_load_dwordx4 a[4:7], %[vl16], %[rws], %[t0] offen offset:1024 sc1\n\t"
        "v_rcp_f32 v191, v191\n\t"
        "v_rcp_f32 v192, v192\n\t"
        "v_rcp_f32 v193, v193\n\t"
        "v_rcp_f32 v194, v194\n\t"
        "v_rcp_f32 v195, v195\n\t"
        "v_pk_mul_f32 v[188:189], v[188:189], v[180:181]\n\t"
        "v_pk_mul_f32 v[190:191], v[190:191], v[182:183]\n\t"
        "v_pk_mul_f32 v[192:193], v[192:193], v[184:185]\n\t"
        "v_pk_mul_f32 v[194:195], v[194:195], v[186:187]\n\t"
        "v_cvt_pk_bf16_f32 v196, v188, v189\n\t"
        "v_cvt_pk_bf16_f32 v197, v190, v191\n\t"
        "v_cvt_pk_bf16_f32 v198, v192, v193\n\t"
        "v_cvt_pk_bf16_f32 v199, v194, v195\n\t"
        "buffer_store_dwordx4 v[196:199], %[vl16], %[rws], %[hs0] offen sc1\n\t"
        "v_mov_b32 v136, 0\n\t"
        "v_mov_b32 v137, 0\n\t"
        "v_readlane_b32 %[t0], %[ddF], %[w]\n\t"
        "v_lshlrev_b32 v128, 16, %[pref]\n\t"
        "s_lshl_b32 %[t0], %[t0], 16\n\t"
        "v_add_f32 v128, %[t0], v128\n\t"
        "s_mov_b32 vcc_lo, 0x80000000\n\t"
        "s_mov_b32 vcc_hi, 0x20004000\n\t"
        "v_cvt_pk_bf16_f32 v128, v128, v128\n\t"
        "v_cndmask_b32 v128, %[pref], v128, vcc\n\t"
        "ds_write_b16 %[aPre], v128\n\t"
        "ds_write_b64 %[aZ], v[136:137]\n\t"
        "v_pk_fma_f32 v[180:181], v[172:173], %[st1], v[164:165] op_sel:[0,1,0] op_sel_hi:[1,1,1]\n\t"
        "v_pk_fma_f32 v[182:183], v[174:175], %[st1], v[166:167] op_sel:[0,1,0] op_sel_hi:[1,1,1]\n\t"
        "v_pk_fma_f32 v[184:185], v[176:177], %[st1], v[168:169] op_sel:[0,1,0] op_sel_hi:[1,1,1]\n\t"
        "v_pk_fma_f32 v[186:187], v[178:179], %[st1], v[170:171] op_sel:[0,1,0] op_sel_hi:[1,1,1]\n\t"
        "v_pk_fma_f32 v[180:181], v[8:9], %[st1], v[180:181] op_sel:[0,0,0] op_sel_hi:[1,0,1]\n\t"
        "v_pk_fma_f32 v[182:183], v[10:11], %[st1], v[182:183] op_sel:[0,0,0] op_sel_hi:[1,0,1]\n\t"
        "v_pk_fma_f32 v[184:185], v[12:13], %[st1], v[184:185] op_sel:[0,0,0] op_sel_hi:[1,0,1]\n\t"
        "v_pk_fma_f32 v[186:187], v[14:15], %[st1], v[186:187] op_sel:[0,0,0] op_sel_hi:[1,0,1]\n\t"
        "v_pk_mul_f32 v[188:189], v[180:181], v[180:181]\n\t"
        "v_pk_mul_f32 v[190:191], v[182:183], v[182:183]\n\t"
        "v_pk_mul_f32 v[192:193], v[184:185], v[184:185]\n\t"
        "v_pk_mul_f32 v[194:195], v[186:187], v[186:187]\n\t"
        "v_pk_fma_f32 v[188:189], v[188:189], %[cc], %[cc] op_sel:[0,0,1] op_sel_hi:[1,0,1]\n\t"
        "v_pk_fma_f32 v[190:191], v[190:191], %[cc], %[cc] op_sel:[0,0,1] op_sel_hi:[1,0,1]\n\t"
        "v_pk_fma_f32 v[192:193], v[192:193], %[cc], %[cc] op_sel:[0,0,1] op_sel_hi:[1,0,1]\n\t"
        "v_pk_fma_f32 v[194:195], v[194:195], %[cc], %[cc] op_sel:[0,0,1] op_sel_hi:[1,0,1]\n\t"
        "v_pk_mul_f32 v[188:189], v[188:189], v[180:181]\n\t"
        "v_pk_mul_f32 v[190:191], v[190:191], v[182:183]\n\t"
        "v_pk_mul_f32 v[192:193], v[192:193], v[184:185]\n\t"
        "v_pk_mul_f32 v[194:195], v[194:195], v[186:187]\n\t"
        "ds_read_b128 v[164:167], %[aC1] offset:0\n\t"
        "ds_read_b128 v[172:175], %[aC1] offset:384\n\t"
        "ds_read_b128 v[168:171], %[aC1] offset:32\n\t"
        "ds_read_b128 v[176:179], %[aC1] offset:416\n\t"
        "v_exp_f32 v188, v188\n\t"
        "s_waitcnt vmcnt(10)\n\t"
        "v_exp_f32 v189, v189\n\t"
        "v_readlane_b32 %[t0], %[ddF], 0\n\t"
        "v_lshlrev_b32 v128, 16, v96\n\t"
        "s_lshl_b32 %[t0], %[t0], 16\n\t"
        "v_add_f32 v128, %[t0], v128\n\t"
        "v_exp_f32 v190, v190\n\t"
        "s_add_u32 %[t0], %[w2o], 0\n\t"
        "buffer_load_dwordx4 a[8:11], %[vl16], %[rws], %[t0] offen offset:2048 sc1\n\t"
        "v_and_b32 v129, 0xffff0000, v96\n\t"
        "s_cmp_eq_u32 %[w], 0\n\t"
        "v_cvt_pk_bf16_f32 v128, v128, v129\n\t"
        "s_cselect_b64 vcc, 1, 0\n\t"
        "v_cndmask_b32 v96, v96, v128, vcc\n\t"
        "v_exp_f32 v191, v191\n\t"
        "ds_write_b128 %[aFc0], v[96:99] offset:0\n\t"
        "v_exp_f32 v192, v192\n\t"
        "v_alignbit_b32 v128, v100, v96, 16\n\t"
        "v_exp_f32 v193, v193\n\t"
        "v_exp_f32 v194, v194\n\t"
        "v_exp_f32 v195, v195\n\t"
        "v_pk_add_f32 v[188:189], v[188:189], 1.0 op_sel_hi:[1,0]\n\t"
        "v_pk_add_f32 v[190:191], v[190:191], 1.0 op_sel_hi:[1,0]\n\t"
        "v_pk_add_f32 v[192:193], v[192:193], 1.0 op_sel_hi:[1,0]\n\t"
        "v_pk_add_f32 v[194:195], v[194:195], 1.0 op_sel_hi:[1,0]\n\t"
        "v_alignbit_b32 v129, v101, v97, 16\n\t"
        "v_alignbit_b32 v130, v102, v98, 16\n\t"
        "v_rcp_f32 v188, v188\n\t"
        "v_alignbit_b32 v131, v103, v99, 16\n\t"
        "ds_write_b128 %[aFc0], v[100:103] offset:8320\n\t"
        "v_rcp_f32 v189, v189\n\t"
        "v_alignbit_b32 v132, v101, v100, 16\n\t"
        "v_alignbit_b32 v133, v102, v101, 16\n\t"
        "v_rcp_f32 v190, v190\n\t"
        "buffer_load_dwordx4 a[12:15], %[vl16], %[rws], %[t0] offen offset:3072 sc1\n\t"
        "v_alignbit_b32 v134, v103, v102, 16\n\t"
        "v_alignbit_b32 v135, v104, v103, 16\n\t"
        "v_rcp_f32 v191, v191\n\t"
        "ds_write_b128 %[aFc0], v[128:131] offset:4160\n\t"
        "ds_write_b128 %[aFc0], v[132:135] offset:12480\n\t"
        "v_rcp_f32 v192, v192\n\t"
        "v_rcp_f32 v193, v193\n\t"
        "v_rcp_f32 v194, v194\n\t"
        "v_rcp_f32 v195, v195\n\t"
        "v_pk_mul_f32 v[188:189], v[188:189], v[180:181]\n\t"
        "v_pk_mul_f32 v[190:191], v[190:191], v[182:183]\n\t"
        "v_pk_mul_f32 v[192:193], v[192:193], v[184:185]\n\t"
        "v_pk_mul_f32 v[194:195], v[194:195], v[186:187]\n\t"
        "v_cvt_pk_bf16_f32 v196, v188, v189\n\t"
        "v_cvt_pk_bf16_f32 v197, v190, v191\n\t"
        "v_cvt_pk_bf16_f32 v198, v192, v193\n\t"
        "v_cvt_pk_bf16_f32 v199, v194, v195\n\t"
        "s_add_u32 %[t1], %[hs0], 0xc000\n\t"
        "buffer_store_dwordx4 v[196:199], %[vl16], %[rws], %[t1] offen sc1\n\t"
        "s_waitcnt lgkmcnt(4)\n\t"
        "v_pk_fma_f32 v[180:181], v[172:173], %[st0], v[164:165] op_sel:[0,1,0] op_sel_hi:[1,1,1]\n\t"
        "v_pk_fma_f32 v[182:183], v[174:175], %[st0], v[166:167] op_sel:[0,1,0] op_sel_hi:[1,1,1]\n\t"
        "v_pk_fma_f32 v[184:185], v[176:177], %[st0], v[168:169] op_sel:[0,1,0] op_sel_hi:[1,1,1]\n\t"
        "v_pk_fma_f32 v[186:187], v[178:179], %[st0], v[170:171] op_sel:[0,1,0] op_sel_hi:[1,1,1]\n\t"
        "v_pk_fma_f32 v[180:181], v[16:17], %[st0], v[180:181] op_sel:[0,0,0] op_sel_hi:[1,0,1]\n\t"
        "v_pk_fma_f32 v[182:183], v[18:19], %[st0], v[182:183] op_sel:[0,0,0] op_sel_hi:[1,0,1]\n\t"
        "v_pk_fma_f32 v[184:185], v[20:21], %[st0], v[184:185] op_sel:[0,0,0] op_sel_hi:[1,0,1]\n\t"
        "v_pk_fma_f32 v[186:187], v[22:23], %[st0], v[186:187] op_sel:[0,0,0] op_sel_hi:[1,0,1]\n\t"
        "v_pk_mul_f32 v[188:189], v[180:181], v[180:181]\n\t"
        "v_pk_mul_f32 v[190:191], v[182:183], v[182:183]\n\t"
        "v_pk_mul_f32 v[192:193], v[184:185], v[184:185]\n\t"
        "v_pk_mul_f32 v[194:195], v[186:187], v[186:187]\n\t"
        "v_pk_fma_f32 v[188:189], v[188:189], %[cc], %[cc] op_sel:[0,0,1] op_sel_hi:[1,0,1]\n\t"
        "v_pk_fma_f32 v[190:191], v[190:191], %[cc], %[cc] op_sel:[0,0,1] op_sel_hi:[1,0,1]\n\t"
        "v_pk_fma_f32 v[192:193], v[192:193], %[cc], %[cc] op_sel:[0,0,1] op_sel_hi:[1,0,1]\n\t"
        "v_pk_fma_f32 v[194:195], v[194:195], %[cc], %[cc] op_sel:[0,0,1] op_sel_hi:[1,0,1]\n\t"
        "v_pk_mul_f32 v[188:189], v[188:189], v[180:181]\n\t"
        "v_pk_mul_f32 v[190:191], v[190:191], v[182:183]\n\t"
        "v_pk_mul_f32 v[192:193], v[192:193], v[184:185]\n\t"
        "v_pk_mul_f32 v[194:195], v[194:195], v[186:187]\n\t"
        "v_exp_f32 v188, v188\n\t"
        "s_waitcnt vmcnt(10)\n\t"
        "v_exp_f32 v189, v189\n\t"
        "v_readlane_b32 %[t0], %[ddF], 1\n\t"
        "v_lshlrev_b32 v128, 16, v106\n\t"
        "s_lshl_b32 %[t0], %[t0], 16\n\t"
        "v_add_f32 v128, %[t0], v128\n\t"
        "v_exp_f32 v190, v190\n\t"
        "s_add_u32 %[t0], %[w2o], 4096\n\t"
        "buffer_load_dwordx4 a[16:19], %[vl16], %[rws], %[t0] offen offset:0 sc1\n\t"
        "v_and_b32 v129, 0xffff0000, v106\n\t"
        "s_cmp_eq_u32 %[w], 0\n\t"
        "v_cvt_pk_bf16_f32 v128, v128, v129\n\t"
        "s_cselect_b64 vcc, 1, 0\n\t"
        "v_cndmask_b32 v106, v106, v128, vcc\n\t"
        "v_exp_f32 v191, v191\n\t"
        "ds_write_b128 %[aFc1], v[106:109] offset:0\n\t"
        "v_exp_f32 v192, v192\n\t"
        "v_alignbit_b32 v128, v110, v106, 16\n\t"
        "v_exp_f32 v193, v193\n\t"
        "v_exp_f32 v194, v194\n\t"
        "v_exp_f32 v195, v195\n\t"
        "v_pk_add_f32 v[188:189], v[188:189], 1.0 op_sel_hi:[1,0]\n\t"
        "v_pk_add_f32 v[190:191], v[190:191], 1.0 op_sel_hi:[1,0]\n\t"
        "v_pk_add_f32 v[192:193], v[192:193], 1.0 op_sel_hi:[1,0]\n\t"
        "v_pk_add_f32 v[194:195], v[194:195], 1.0 op_sel_hi:[1,0]\n\t"
        "v_alignbit_b32 v129, v111, v107, 16\n\t"
        "v_alignbit_b32 v130, v112, v108, 16\n\t"
        "v_rcp_f32 v188, v188\n\t"
        "v_alignbit_b32 v131, v113, v109, 16\n\t"
        "ds_write_b128 %[aFc1], v[110:113] offset:8320\n\t"
        "v_rcp_f32 v189, v189\n\t"
        "v_alignbit_b32 v132, v111, v110, 16\n\t"
        "v_alignbit_b32 v133, v112, v111, 16\n\t"
        "v_rcp_f32 v190, v190\n\t"
        "buffer_load_dwordx4 a[20:23], %[vl16], %[rws], %[t0] offen offset:1024 sc1\n\t"
        "v_alignbit_b32 v134, v113, v112, 16\n\t"
        "v_alignbit_b32 v135, v114, v113, 16\n\t"
        "v_rcp_f32 v191, v191\n\t"
        "ds_write_b128 %[aFc1], v[128:131] offset:4160\n\t"
        "ds_write_b128 %[aFc1], v[132:135] offset:12480\n\t"
        "v_rcp_f32 v192, v192\n\t"
        "v_rcp_f32 v193, v193\n\t"
        "v_rcp_f32 v194, v194\n\t"
        "v_rcp_f32 v195, v195\n\t"
        "v_pk_mul_f32 v[188:189], v[188:189], v[180:181]\n\t"
        "v_pk_mul_f32 v[190:191], v[190:191], v[182:183]\n\t"
        "v_pk_mul_f32 v[192:193], v[192:193], v[184:185]\n\t"
        "v_pk_mul_f32 v[194:195], v[194:195], v[186:187]\n\t"
        "v_cvt_pk_bf16_f32 v196, v188, v189\n\t"
        "v_cvt_pk_bf16_f32 v197, v190, v191\n\t"
        "v_cvt_pk_bf16_f32 v198, v192, v193\n\t"
        "v_cvt_pk_bf16_f32 v199, v194, v195\n\t"
        "buffer_store_dwordx4 v[196:199], %[vl16], %[rws], %[hs1] offen sc1\n\t"
        "v_pk_fma_f32 v[180:181], v[172:173], %[st1], v[164:165] op_sel:[0,1,0] op_sel_hi:[1,1,1]\n\t"
        "v_pk_fma_f32 v[182:183], v[174:175], %[st1], v[166:167] op_sel:[0,1,0] op_sel_hi:[1,1,1]\n\t"
        "v_pk_fma_f32 v[184:185], v[176:177], %[st1], v[168:169] op_sel:[0,1,0] op_sel_hi:[1,1,1]\n\t"
        "v_pk_fma_f32 v[186:187], v[178:179], %[st1], v[170:171] op_sel:[0,1,0] op_sel_hi:[1,1,1]\n\t"
        "v_pk_fma_f32 v[180:181], v[24:25], %[st1], v[180:181] op_sel:[0,0,0] op_sel_hi:[1,0,1]\n\t"
        "v_pk_fma_f32 v[182:183], v[26:27], %[st1], v[182:183] op_sel:[0,0,0] op_sel_hi:[1,0,1]\n\t"
        "v_pk_fma_f32 v[184:185], v[28:29], %[st1], v[184:185] op_sel:[0,0,0] op_sel_hi:[1,0,1]\n\t"
        "v_pk_fma_f32 v[186:187], v[30:31], %[st1], v[186:187] op_sel:[0,0,0] op_sel_hi:[1,0,1]\n\t"
        "v_pk_mul_f32 v[188:189], v[180:181], v[180:181]\n\t"
        "v_pk_mul_f32 v[190:191], v[182:183], v[182:183]\n\t"
        "v_pk_mul_f32 v[192:193], v[184:185], v[184:185]\n\t"
        "v_pk_mul_f32 v[194:195], v[186:187], v[186:187]\n\t"
        "v_pk_fma_f32 v[188:189], v[188:189], %[cc], %[cc] op_sel:[0,0,1] op_sel_hi:[1,0,1]\n\t"
        "v_pk_fma_f32 v[190:191], v[190:191], %[cc], %[cc] op_sel:[0,0,1] op_sel_hi:[1,0,1]\n\t"
        "v_pk_fma_f32 v[192:193], v[192:193], %[cc], %[cc] op_sel:[0,0,1] op_sel_hi:[1,0,1]\n\t"
        "v_pk_fma_f32 v[194:195], v[194:195], %[cc], %[cc] op_sel:[0,0,1] op_sel_hi:[1,0,1]\n\t"
        "v_pk_mul_f32 v[188:189], v[188:189], v[180:181]\n\t"
        "v_pk_mul_f32 v[190:191], v[190:191], v[182:183]\n\t"
        "v_pk_mul_f32 v[192:193], v[192:193], v[184:185]\n\t"
        "v_pk_mul_f32 v[194:195], v[194:195], v[186:187]\n\t"
        "ds_read_b128 v[164:167], %[aC2] offset:0\n\t"
        "ds_read_b128 v[172:175], %[aC2] offset:384\n\t"
        "ds_read_b128 v[168:171], %[aC2] offset:32\n\t"
        "ds_read_b128 v[176:179], %[aC2] offset:416\n\t"
        "v_exp_f32 v188, v188\n\t"
        "s_waitcnt vmcnt(10)\n\t"
        "v_exp_f32 v189, v189\n\t"
        "v_readlane_b32 %[t0], %[ddF], 2\n\t"
        "v_lshlrev_b32 v128, 16, v116\n\t"
        "s_lshl_b32 %[t0], %[t0], 16\n\t"
        "v_add_f32 v128, %[t0], v128\n\t"
        "v_exp_f32 v190, v190\n\t"
        "s_add_u32 %[t0], %[w2o], 4096\n\t"
        "buffer_load_dwordx4 a[24:27], %[vl16], %[rws], %[t0] offen offset:2048 sc1\n\t"
        "v_and_b32 v129, 0xffff0000, v116\n\t"
        "s_cmp_eq_u32 %[w], 0\n\t"
        "v_cvt_pk_bf16_f32 v128, v128, v129\n\t"
        "s_cselect_b64 vcc, 1, 0\n\t"
        "v_cndmask_b32 v116, v116, v128, vcc\n\t"
        "v_exp_f32 v191, v191\n\t"
        "ds_write_b128 %[aFc2], v[116:119] offset:0\n\t"
        "v_exp_f32 v192, v192\n\t"
        "v_alignbit_b32 v128, v120, v116, 16\n\t"
        "v_exp_f32 v193, v193\n\t"
        "v_exp_f32 v194, v194\n\t"
        "v_exp_f32 v195, v195\n\t"
        "v_pk_add_f32 v[188:189], v[188:189], 1.0 op_sel_hi:[1,0]\n\t"
        "v_pk_add_f32 v[190:191], v[190:191], 1.0 op_sel_hi:[1,0]\n\t"
        "v_pk_add_f32 v[192:193], v[192:193], 1.0 op_sel_hi:[1,0]\n\t"
        "v_pk_add_f32 v[194:195], v[194:195], 1.0 op_sel_hi:[1,0]\n\t"
        "v_alignbit_b32 v129, v121, v117, 16\n\t"
        "v_alignbit_b32 v130, v122, v118, 16\n\t"
        "v_rcp_f32 v188, v188\n\t"
        "v_alignbit_b32 v131, v123, v119, 16\n\t"
        "ds_write_b128 %[aFc2], v[120:123] offset:8320\n\t"
        "v_rcp_f32 v189, v189\n\t"
        "v_alignbit_b32 v132, v121, v120, 16\n\t"
        "v_alignbit_b32 v133, v122, v121, 16\n\t"
        "v_rcp_f32 v190, v190\n\t"
        "buffer_load_dwordx4 a[28:31], %[vl16], %[rws], %[t0] offen offset:3072 sc1\n\t"
        "v_alignbit_b32 v134, v123, v122, 16\n\t"
        "v_alignbit_b32 v135, v124, v123, 16\n\t"
        "v_rcp_f32 v191, v191\n\t"
        "ds_write_b128 %[aFc2], v[128:131] offset:4160\n\t"
        "ds_write_b128 %[aFc2], v[132:135] offset:12480\n\t"
        "v_rcp_f32 v192, v192\n\t"
        "v_rcp_f32 v193, v193\n\t"
        "v_rcp_f32 v194, v194\n\t"
        "v_rcp_f32 v195, v195\n\t"
        "v_pk_mul_f32 v[188:189], v[188:189], v[180:181]\n\t"
        "v_pk_mul_f32 v[190:191], v[190:191], v[182:183]\n\t"
        "v_pk_mul_f32 v[192:193], v[192:193], v[184:185]\n\t"
        "v_pk_mul_f32 v[194:195], v[194:195], v[186:187]\n\t"
        "v_cvt_pk_bf16_f32 v196, v188, v189\n\t"
        "v_cvt_pk_bf16_f32 v197, v190, v191\n\t"
        "v_cvt_pk_bf16_f32 v198, v192, v193\n\t"
        "v_cvt_pk_bf16_f32 v199, v194, v195\n\t"
        "s_add_u32 %[t1], %[hs1], 0xc000\n\t"
        "buffer_store_dwordx4 v[196:199], %[vl16], %[rws], %[t1] offen sc1\n\t"
        "s_waitcnt lgkmcnt(4)\n\t"
        "v_pk_fma_f32 v[180:181], v[172:173], %[st0], v[164:165] op_sel:[0,1,0] op_sel_hi:[1,1,1]\n\t"
        "v_pk_fma_f32 v[182:183], v[174:175], %[st0], v[166:167] op_sel:[0,1,0] op_sel_hi:[1,1,1]\n\t"
        "v_pk_fma_f32 v[184:185], v[176:177], %[st0], v[168:169] op_sel:[0,1,0] op_sel_hi:[1,1,1]\n\t"
        "v_pk_fma_f32 v[186:187], v[178:179], %[st0], v[170:171] op_sel:[0,1,0] op_sel_hi:[1,1,1]\n\t"
        "v_pk_fma_f32 v[180:181], v[32:33], %[st0], v[180:181] op_sel:[0,0,0] op_sel_hi:[1,0,1]\n\t"
        "v_pk_fma_f32 v[182:183], v[34:35], %[st0], v[182:183] op_sel:[0,0,0] op_sel_hi:[1,0,1]\n\t"
        "v_pk_fma_f32 v[184:185], v[36:37], %[st0], v[184:185] op_sel:[0,0,0] op_sel_hi:[1,0,1]\n\t"
        "v_pk_fma_f32 v[186:187], v[38:39], %[st0], v[186:187] op_sel:[0,0,0] op_sel_hi:[1,0,1]\n\t"
        "v_pk_mul_f32 v[188:189], v[180:181], v[180:181]\n\t"
        "v_pk_mul_f32 v[190:191], v[182:183], v[182:183]\n\t"
        "v_pk_mul_f32 v[192:193], v[184:185], v[184:185]\n\t"
        "v_pk_mul_f32 v[194:195], v[186:187], v[186:187]\n\t"
        "v_pk_fma_f32 v[188:189], v[188:189], %[cc], %[cc] op_sel:[0,0,1] op_sel_hi:[1,0,1]\n\t"
        "v_pk_fma_f32 v[190:191], v[190:191], %[cc], %[cc] op_sel:[0,0,1] op_sel_hi:[1,0,1]\n\t"
        "v_pk_fma_f32 v[192:193], v[192:193], %[cc], %[cc] op_sel:[0,0,1] op_sel_hi:[1,0,1]\n\t"
        "v_pk_fma_f32 v[194:195], v[194:195], %[cc], %[cc] op_sel:[0,0,1] op_sel_hi:[1,0,1]\n\t"
        "v_pk_mul_f32 v[188:189], v[188:189], v[180:181]\n\t"
        "v_pk_mul_f32 v[190:191], v[190:191], v[182:183]\n\t"
        "v_pk_mul_f32 v[192:193], v[192:193], v[184:185]\n\t"
        "v_pk_mul_f32 v[194:195], v[194:195], v[186:187]\n\t"
        "v_exp_f32 v188, v188\n\t"
        "v_exp_f32 v189, v189\n\t"
        "v_exp_f32 v190, v190\n\t"
        "s_add_u32 %[t0], %[w2o], 8192\n\t"
        "buffer_load_dwordx4 a[32:35], %[vl16], %[rws], %[t0] offen offset:0 sc1\n\t"
        "v_exp_f32 v191, v191\n\t"
        "v_exp_f32 v192, v192\n\t"
        "v_exp_f32 v193, v193\n\t"
        "v_exp_f32 v194, v194\n\t"
        "v_exp_f32 v195, v195\n\t"
        "v_pk_add_f32 v[188:189], v[188:189], 1.0 op_sel_hi:[1,0]\n\t"
        "v_pk_add_f32 v[190:191], v[190:191], 1.0 op_sel_hi:[1,0]\n\t"
        "v_pk_add_f32 v[192:193], v[192:193], 1.0 op_sel_hi:[1,0]\n\t"
        "v_pk_add_f32 v[194:195], v[194:195], 1.0 op_sel_hi:[1,0]\n\t"
        "v_rcp_f32 v188, v188\n\t"
        "v_rcp_f32 v189, v189\n\t"
        "v_rcp_f32 v190, v190\n\t"
        "buffer_load_dwordx4 a[36:39], %[vl16], %[rws], %[t0] offen offset:1024 sc1\n\t"
        "v_rcp_f32 v191, v191\n\t"
        "v_rcp_f32 v192, v192\n\t"
        "v_rcp_f32 v193, v193\n\t"
        "v_rcp_f32 v194, v194\n\t"
        "v_rcp_f32 v195, v195\n\t"
        "v_pk_mul_f32 v[188:189], v[188:189], v[180:181]\n\t"
        "v_pk_mul_f32 v[190:191], v[190:191], v[182:183]\n\t"
        "v_pk_mul_f32 v[192:193], v[192:193], v[184:185]\n\t"
        "v_pk_mul_f32 v[194:195], v[194:195], v[186:187]\n\t"
        "v_cvt_pk_bf16_f32 v196, v188, v189\n\t"
        "v_cvt_pk_bf16_f32 v197, v190, v191\n\t"
        "v_cvt_pk_bf16_f32 v198, v192, v193\n\t"
        "v_cvt_pk_bf16_f32 v199, v194, v195\n\t"
        "buffer_store_dwordx4 v[196:199], %[vl16], %[rws], %[hs2] offen sc1\n\t"
        "v_pk_fma_f32 v[180:181], v[172:173], %[st1], v[164:165] op_sel:[0,1,0] op_sel_hi:[1,1,1]\n\t"
        "v_pk_fma_f32 v[182:183], v[174:175], %[st1], v[166:167] op_sel:[0,1,0] op_sel_hi:[1,1,1]\n\t"
        "v_pk_fma_f32 v[184:185], v[176:177], %[st1], v[168:169] op_sel:[0,1,0] op_sel_hi:[1,1,1]\n\t"
        "v_pk_fma_f32 v[186:187], v[178:179], %[st1], v[170:171] op_sel:[0,1,0] op_sel_hi:[1,1,1]\n\t"
        "v_pk_fma_f32 v[180:181], v[40:41], %[st1], v[180:181] op_sel:[0,0,0] op_sel_hi:[1,0,1]\n\t"
        "v_pk_fma_f32 v[182:183], v[42:43], %[st1], v[182:183] op_sel:[0,0,0] op_sel_hi:[1,0,1]\n\t"
        "v_pk_fma_f32 v[184:185], v[44:45], %[st1], v[184:185] op_sel:[0,0,0] op_sel_hi:[1,0,1]\n\t"
        "v_pk_fma_f32 v[186:187], v[46:47], %[st1], v[186:187] op_sel:[0,0,0] op_sel_hi:[1,0,1]\n\t"
        "v_pk_mul_f32 v[188:189], v[180:181], v[180:181]\n\t"
        "v_pk_mul_f32 v[190:191], v[182:183], v[182:183]\n\t"
        "v_pk_mul_f32 v[192:193], v[184:185], v[184:185]\n\t"
        "v_pk_mul_f32 v[194:195], v[186:187], v[186:187]\n\t"
        "v_pk_fma_f32 v[188:189], v[188:189], %[cc], %[cc] op_sel:[0,0,1] op_sel_hi:[1,0,1]\n\t"
        "v_pk_fma_f32 v[190:191], v[190:191], %[cc], %[cc] op_sel:[0,0,1] op_sel_hi:[1,0,1]\n\t"
        "v_pk_fma_f32 v[192:193], v[192:193], %[cc], %[cc] op_sel:[0,0,1] op_sel_hi:[1,0,1]\n\t"
        "v_pk_fma_f32 v[194:195], v[194:195], %[cc], %[cc] op_sel:[0,0,1] op_sel_hi:[1,0,1]\n\t"
        "v_pk_mul_f32 v[188:189], v[188:189], v[180:181]\n\t"
        "v_pk_mul_f32 v[190:191], v[190:191], v[182:183]\n\t"
        "v_pk_mul_f32 v[192:193], v[192:193], v[184:185]\n\t"
        "v_pk_mul_f32 v[194:195], v[194:195], v[186:187]\n\t"
        "v_exp_f32 v188, v188\n\t"
        "v_exp_f32 v189, v189\n\t"
        "v_exp_f32 v190, v190\n\t"
        "buffer_load_dwordx4 a[40:43], %[vl16], %[rws], %[t0] offen offset:2048 sc1\n\t"
        "v_exp_f32 v191, v191\n\t"
        "v_exp_f32 v192, v192\n\t"
        "v_exp_f32 v193, v193\n\t"
        "v_exp_f32 v194, v194\n\t"
        "v_exp_f32 v195, v195\n\t"
        "v_pk_add_f32 v[188:189], v[188:189], 1.0 op_sel_hi:[1,0]\n\t"
        "v_pk_add_f32 v[190:191], v[190:191], 1.0 op_sel_hi:[1,0]\n\t"
        "v_pk_add_f32 v[192:193], v[192:193], 1.0 op_sel_hi:[1,0]\n\t"
        "v_pk_add_f32 v[194:195], v[194:195], 1.0 op_sel_hi:[1,0]\n\t"
        "v_rcp_f32 v188, v188\n\t"
        "v_rcp_f32 v189, v189\n\t"
        "v_rcp_f32 v190, v190\n\t"
        "buffer_load_dwordx4 a[44:47], %[vl16], %[rws], %[t0] offen offset:3072 sc1\n\t"
        "v_rcp_f32 v191, v191\n\t"
        "v_rcp_f32 v192, v192\n\t"
        "v_rcp_f32 v193, v193\n\t"
        "v_rcp_f32 v194, v194\n\t"
        "v_rcp_f32 v195, v195\n\t"
        "v_pk_mul_f32 v[188:189], v[188:189], v[180:181]\n\t"
        "v_pk_mul_f32 v[190:191], v[190:191], v[182:183]\n\t"
        "v_pk_mul_f32 v[192:193], v[192:193], v[184:185]\n\t"
        "v_pk_mul_f32 v[194:195], v[194:195], v[186:187]\n\t"
        "v_cvt_pk_bf16_f32 v196, v188, v189\n\t"
        "v_cvt_pk_bf16_f32 v197, v190, v191\n\t"
        "v_cvt_pk_bf16_f32 v198, v192, v193\n\t"
        "v_cvt_pk_bf16_f32 v199, v194, v195\n\t"
        "s_add_u32 %[t1], %[hs2], 0xc000\n\t"
        "buffer_store_dwordx4 v[196:199], %[vl16], %[rws], %[t1] offen sc1\n\t"
        "s_waitcnt lgkmcnt(0)\n\t"
        "s_barrier\n\t"
        "ds_read_b128 v[152:155], %[pbF] offset:4032\n\t"
        "ds_read_b64_tr_b16 v[96:97], %[paF] offset:0\n\t"
        "ds_read_b64_tr_b16 v[98:99], %[paF] offset:8\n\t"
        "ds_read_b128 v[148:151], %[pbF] offset:3520\n\t"
        "ds_read_b128 v[144:147], %[pbF] offset:3008\n\t"
        "ds_read_b128 v[140:143], %[pbF] offset:2496\n\t"
        "ds_read_b128 v[136:139], %[pbF] offset:1984\n\t"
        "ds_read_b128 v[132:135], %[pbF] offset:1472\n\t"
        "ds_read_b128 v[128:131], %[pbF] offset:960\n\t"
        "ds_read_b128 v[156:159], %[pbF] offset:448\n\t"
        "ds_read_b64_tr_b16 v[100:101], %[paF] offset:512\n\t"
        "ds_read_b64_tr_b16 v[102:103], %[paF] offset:520\n\t"
        "ds_read_b64_tr_b16 v[104:105], %[paF] offset:1024\n\t"
        "ds_read_b64_tr_b16 v[106:107], %[paF] offset:1032\n\t"
        "ds_read_b64_tr_b16 v[108:109], %[paF] offset:1536\n\t"
        "ds_read_b64_tr_b16 v[110:111], %[paF] offset:1544\n\t"
        "ds_read_b64_tr_b16 v[112:113], %[paF] offset:2048\n\t"
        "ds_read_b64_tr_b16 v[114:115], %[paF] offset:2056\n\t"
        "ds_read_b64_tr_b16 v[116:117], %[paF] offset:2560\n\t"
        "ds_read_b64_tr_b16 v[118:119], %[paF] offset:2568\n\t"
        "ds_read_b64_tr_b16 v[120:121], %[paF] offset:3072\n\t"
        "ds_read_b64_tr_b16 v[122:123], %[paF] offset:3080\n\t"
        "ds_read_b64_tr_b16 v[124:125], %[paF] offset:3584\n\t"
        "ds_read_b64_tr_b16 v[126:127], %[paF] offset:3592\n\t"
        "ds_read_b128 v[160:163], %[pbF] offset:384\n\t"
        "ds_read_b64 a[246:247], %[aQ0] offset:61440\n\t"
        "ds_read_b64 a[248:249], %[aQ1] offset:61440\n\t"
        "ds_read_b64 a[250:251], %[aQ2] offset:61440\n\t"
        "ds_read_b64 a[252:253], %[aQ3] offset:61440\n\t"
        "ds_read_b64 a[254:255], %[aQ4] offset:61440\n\t"
        "s_waitcnt lgkmcnt(15)\n\t"
        "v_mfma_f32_16x16x32_bf16 v[76:79], v[96:99], v[152:155], 0\n\t"
        "ds_read_b128 v[152:155], %[pbF] offset:3968\n\t"
        "v_mfma_f32_16x16x32_bf16 v[72:75], v[96:99], v[148:151], 0\n\t"
        "v_mfma_f32_16x16x32_bf16 v[68:71], v[96:99], v[144:147], 0\n\t"
        "s_add_u32 %[t0], %[w2o], 12288\n\t"
        "buffer_load_dwordx4 a[48:51], %[vl16], %[rws], %[t0] offen offset:0 sc1\n\t"
        "v_mfma_f32_16x16x32_bf16 v[64:67], v[96:99], v[140:143], 0\n\t"
        "v_mfma_f32_16x16x32_bf16 v[60:63], v[96:99], v[136:139], 0\n\t"
        "v_mfma_f32_16x16x32_bf16 v[56:59], v[96:99], v[132:135], 0\n\t"
        "buffer_load_dwordx4 a[52:55], %[vl16], %[rws], %[t0] offen offset:1024 sc1\n\t"
        "v_mfma_f32_16x16x32_bf16 v[52:55], v[96:99], v[128:131], 0\n\t"
        "v_mfma_f32_16x16x32_bf16 v[48:51], v[96:99], v[156:159], 0\n\t"
        "ds_read_b64_tr_b16 v[96:97], %[paF] offset:64\n\t"
        "v_mfma_f32_16x16x32_bf16 v[76:79], v[100:103], v[148:151], v[76:79]\n\t"
        "ds_read_b64_tr_b16 v[98:99], %[paF] offset:72\n\t"
        "v_mfma_f32_16x16x32_bf16 v[72:75], v[100:103], v[144:147], v[72:75]\n\t"
        "ds_read_b128 v[148:151], %[pbF] offset:3456\n\t"
        "buffer_load_dwordx4 a[56:59], %[vl16], %[rws], %[t0] offen offset:2048 sc1\n\t"
        "v_mfma_f32_16x16x32_bf16 v[68:71], v[100:103], v[140:143], v[68:71]\n\t"
        "v_mfma_f32_16x16x32_bf16 v[64:67], v[100:103], v[136:139], v[64:67]\n\t"
        "v_mfma_f32_16x16x32_bf16 v[60:63], v[100:103], v[132:135], v[60:63]\n\t"
        "buffer_load_dwordx4 a[60:63], %[vl16], %[rws], %[t0] offen offset:3072 sc1\n\t"
        "v_mfma_f32_16x16x32_bf16 v[56:59], v[100:103], v[128:131], v[56:59]\n\t"
        "v_mfma_f32_16x16x32_bf16 v[52:55], v[100:103], v[156:159], v[52:55]\n\t"
        "ds_read_b64_tr_b16 v[100:101], %[paF] offset:576\n\t"
        "v_mfma_f32_16x16x32_bf16 v[76:79], v[104:107], v[144:147], v[76:79]\n\t"
        "ds_read_b128 v[144:147], %[pbF] offset:2944\n\t"
        "s_add_u32 %[t0], %[w2o], 16384\n\t"
        "buffer_load_dwordx4 a[64:67], %[vl16], %[rws], %[t0] offen offset:0 sc1\n\t"
        "v_mfma_f32_16x16x32_bf16 v[72:75], v[104:107], v[140:143], v[72:75]\n\t"
        "ds_read_b64_tr_b16 v[102:103], %[paF] offset:584\n\t"
        "v_mfma_f32_16x16x32_bf16 v[68:71], v[104:107], v[136:139], v[68:71]\n\t"
        "v_mfma_f32_16x16x32_bf16 v[64:67], v[104:107], v[132:135], v[64:67]\n\t"
        "v_mfma_f32_16x16x32_bf16 v[60:63], v[104:107], v[128:131], v[60:63]\n\t"
        "buffer_load_dwordx4 a[68:71], %[vl16], %[rws], %[t0] offen offset:1024 sc1\n\t"
        "v_mfma_f32_16x16x32_bf16 v[56:59], v[104:107], v[156:159], v[56:59]\n\t"
        "ds_read_b64_tr_b16 v[104:105], %[paF] offset:1088\n\t"
        "s_waitcnt lgkmcnt(15)\n\t"
        "v_mfma_f32_16x16x32_bf16 v[76:79], v[108:111], v[140:143], v[76:79]\n\t"
        "ds_read_b128 v[140:143], %[pbF] offset:2432\n\t"
        "v_mfma_f32_16x16x32_bf16 v[72:75], v[108:111], v[136:139], v[72:75]\n\t"
        "ds_read_b64_tr_b16 v[106:107], %[paF] offset:1096\n\t"
        "buffer_load_dwordx4 a[72:75], %[vl16], %[rws], %[t0] offen offset:2048 sc1\n\t"
        "v_mfma_f32_16x16x32_bf16 v[68:71], v[108:111], v[132:135], v[68:71]\n\t"
        "v_mfma_f32_16x16x32_bf16 v[64:67], v[108:111], v[128:131], v[64:67]\n\t"
        "s_waitcnt vmcnt(7)\n\t"
        "s_barrier\n\t"
        "buffer_store_dwordx2 %[evv], %[vfH], %[rws], 0 offen sc1\n\t"
        "v_mfma_f32_16x16x32_bf16 v[60:63], v[108:111], v[156:159], v[60:63]\n\t"
        "ds_read_b64_tr_b16 v[108:109], %[paF] offset:1600\n\t"
        "buffer_load_dwordx4 a[76:79], %[vl16], %[rws], %[t0] offen offset:3072 sc1\n\t"
        "v_mfma_f32_16x16x32_bf16 v[76:79], v[112:115], v[136:139], v[76:79]\n\t"
        "ds_read_b128 v[136:139], %[pbF] offset:1920\n\t"
        "v_mfma_f32_16x16x32_bf16 v[72:75], v[112:115], v[132:135], v[72:75]\n\t"
        "ds_read_b64_tr_b16 v[110:111], %[paF] offset:1608\n\t"
        "v_mfma_f32_16x16x32_bf16 v[68:71], v[112:115], v[128:131], v[68:71]\n\t"
        "v_mfma_f32_16x16x32_bf16 v[64:67], v[112:115], v[156:159], v[64:67]\n\t"
        "ds_read_b64_tr_b16 v[112:113], %[paF] offset:2112\n\t"
        "s_add_u32 %[t0], %[w2o], 20480\n\t"
        "buffer_load_dwordx4 a[80:83], %[vl16], %[rws], %[t0] offen offset:0 sc1\n\t"
        "v_mfma_f32_16x16x32_bf16 v[76:79], v[116:119], v[132:135], v[76:79]\n\t"
        "ds_read_b128 v[132:135], %[pbF] offset:1408\n\t"
        "v_mfma_f32_16x16x32_bf16 v[72:75], v[116:119], v[128:131], v[72:75]\n\t"
        "ds_read_b64_tr_b16 v[114:115], %[paF] offset:2120\n\t"
        "v_mfma_f32_16x16x32_bf16 v[68:71], v[116:119], v[156:159], v[68:71]\n\t"
        "ds_read_b64_tr_b16 v[116:117], %[paF] offset:2624\n\t"
        "buffer_load_dwordx4 a[84:87], %[vl16], %[rws], %[t0] offen offset:1024 sc1\n\t"
        "v_mfma_f32_16x16x32_bf16 v[76:79], v[120:123], v[128:131], v[76:79]\n\t"
        "ds_read_b128 v[128:131], %[pbF] offset:896\n\t"
        "v_mfma_f32_16x16x32_bf16 v[72:75], v[120:123], v[156:159], v[72:75]\n\t"
        "ds_read_b64_tr_b16 v[118:119], %[paF] offset:2632\n\t"
        "s_waitcnt lgkmcnt(15)\n\t"
        "v_mfma_f32_16x16x32_bf16 v[76:79], v[124:127], v[156:159], v[76:79]\n\t"
        "ds_read_b64_tr_b16 v[120:121], %[paF] offset:3136\n\t"
        "buffer_load_dwordx4 a[88:91], %[vl16], %[rws], %[t0] offen offset:2048 sc1\n\t"
        "v_mfma_f32_16x16x32_bf16 v[76:79], v[96:99], v[152:155], v[76:79]\n\t"
        "ds_read_b64_tr_b16 v[122:123], %[paF] offset:3144\n\t"
        "v_mfma_f32_16x16x32_bf16 v[72:75], v[96:99], v[148:151], v[72:75]\n\t"
        "ds_read_b64_tr_b16 v[124:125], %[paF] offset:3648\n\t"
        "s_waitcnt lgkmcnt(15)\n\t"
        "v_mfma_f32_16x16x32_bf16 v[68:71], v[96:99], v[144:147], v[68:71]\n\t"
        "ds_read_b64_tr_b16 v[126:127], %[paF] offset:3656\n\t"
        "s_waitcnt lgkmcnt(14)\n\t"
        "v_mfma_f32_16x16x32_bf16 v[64:67], v[96:99], v[140:143], v[64:67]\n\t"
        "ds_read_b128 v[152:155], %[pbF] offset:3904\n\t"
        "buffer_load_dwordx4 a[92:95], %[vl16], %[rws], %[t0] offen offset:3072 sc1\n\t"
        "s_waitcnt lgkmcnt(12)\n\t"
        "v_mfma_f32_16x16x32_bf16 v[60:63], v[96:99], v[136:139], v[60:63]\n\t"
        "ds_read_b128 v[156:159], %[pbF] offset:320\n\t"
        "s_waitcnt lgkmcnt(10)\n\t"
        "v_mfma_f32_16x16x32_bf16 v[56:59], v[96:99], v[132:135], v[56:59]\n\t"
        "s_waitcnt lgkmcnt(7)\n\t"
        "v_mfma_f32_16x16x32_bf16 v[52:55], v[96:99], v[128:131], v[52:55]\n\t"
        "s_add_u32 %[t0], %[w2o], 24576\n\t"
        "buffer_load_dwordx4 a[96:99], %[vl16], %[rws], %[t0] offen offset:0 sc1\n\t"
        "v_mfma_f32_16x16x32_bf16 v[48:51], v[96:99], v[160:163], v[48:51]\n\t"
        "ds_read_b64_tr_b16 v[96:97], %[paF] offset:128\n\t"
        "v_mfma_f32_16x16x32_bf16 v[76:79], v[100:103], v[148:151], v[76:79]\n\t"
        "ds_read_b64_tr_b16 v[98:99], %[paF] offset:136\n\t"
        "v_mfma_f32_16x16x32_bf16 v[72:75], v[100:103], v[144:147], v[72:75]\n\t"
        "ds_read_b128 v[148:151], %[pbF] offset:3392\n\t"
        "buffer_load_dwordx4 a[100:103], %[vl16], %[rws], %[t0] offen offset:1024 sc1\n\t"
        "v_mfma_f32_16x16x32_bf16 v[68:71], v[100:103], v[140:143], v[68:71]\n\t"
        "v_mfma_f32_16x16x32_bf16 v[64:67], v[100:103], v[136:139], v[64:67]\n\t"
        "v_mfma_f32_16x16x32_bf16 v[60:63], v[100:103], v[132:135], v[60:63]\n\t"
        "v_mfma_f32_16x16x32_bf16 v[56:59], v[100:103], v[128:131], v[56:59]\n\t"
        "buffer_load_dwordx4 a[104:107], %[vl16], %[rws], %[t0] offen offset:2048 sc1\n\t"
        "v_mfma_f32_16x16x32_bf16 v[52:55], v[100:103], v[160:163], v[52:55]\n\t"
        "ds_read_b64_tr_b16 v[100:101], %[paF] offset:640\n\t"
        "v_mfma_f32_16x16x32_bf16 v[76:79], v[104:107], v[144:147], v[76:79]\n\t"
        "ds_read_b128 v[144:147], %[pbF] offset:2880\n\t"
        "v_mfma_f32_16x16x32_bf16 v[72:75], v[104:107], v[140:143], v[72:75]\n\t"
        "ds_read_b64_tr_b16 v[102:103], %[paF] offset:648\n\t"
        "buffer_load_dwordx4 a[108:111], %[vl16], %[rws], %[t0] offen offset:3072 sc1\n\t"
        "v_mfma_f32_16x16x32_bf16 v[68:71], v[104:107], v[136:139], v[68:71]\n\t"
        "v_mfma_f32_16x16x32_bf16 v[64:67], v[104:107], v[132:135], v[64:67]\n\t"
        "v_mfma_f32_16x16x32_bf16 v[60:63], v[104:107], v[128:131], v[60:63]\n\t"
        "s_add_u32 %[t0], %[w2o], 28672\n\t"
        "buffer_load_dwordx4 a[112:115], %[vl16], %[rws], %[t0] offen offset:0 sc1\n\t"
        "v_mfma_f32_16x16x32_bf16 v[56:59], v[104:107], v[160:163], v[56:59]\n\t"
        "ds_read_b64_tr_b16 v[104:105], %[paF] offset:1152\n\t"
        "v_mfma_f32_16x16x32_bf16 v[76:79], v[108:111], v[140:143], v[76:79]\n\t"
        "ds_read_b128 v[140:143], %[pbF] offset:2368\n\t"
        "v_mfma_f32_16x16x32_bf16 v[72:75], v[108:111], v[136:139], v[72:75]\n\t"
        "ds_read_b64_tr_b16 v[106:107], %[paF] offset:1160\n\t"
        "v_mfma_f32_16x16x32_bf16 v[68:71], v[108:111], v[132:135], v[68:71]\n\t"
        "buffer_load_dwordx4 a[116:119], %[vl16], %[rws], %[t0] offen offset:1024 sc1\n\t"
        "v_mfma_f32_16x16x32_bf16 v[64:67], v[108:111], v[128:131], v[64:67]\n\t"
        "v_mfma_f32_16x16x32_bf16 v[60:63], v[108:111], v[160:163], v[60:63]\n\t"
        "ds_read_b64_tr_b16 v[108:109], %[paF] offset:1664\n\t"
        "v_mfma_f32_16x16x32_bf16 v[76:79], v[112:115], v[136:139], v[76:79]\n\t"
        "ds_read_b128 v[136:139], %[pbF] offset:1856\n\t"
        "buffer_load_dwordx4 a[120:123], %[vl16], %[rws], %[t0] offen offset:2048 sc1\n\t"
        "v_mfma_f32_16x16x32_bf16 v[72:75], v[112:115], v[132:135], v[72:75]\n\t"
        "ds_read_b64_tr_b16 v[110:111], %[paF] offset:1672\n\t"
        "v_mfma_f32_16x16x32_bf16 v[68:71], v[112:115], v[128:131], v[68:71]\n\t"
        "v_mfma_f32_16x16x32_bf16 v[64:67], v[112:115], v[160:163], v[64:67]\n\t"
        "ds_read_b64_tr_b16 v[112:113], %[paF] offset:2176\n\t"
        "buffer_load_dwordx4 a[124:127], %[vl16], %[rws], %[t0] offen offset:3072 sc1\n\t"
        "s_waitcnt lgkmcnt(15)\n\t"
        "v_mfma_f32_16x16x32_bf16 v[76:79], v[116:119], v[132:135], v[76:79]\n\t"
        "ds_read_b128 v[132:135], %[pbF] offset:1344\n\t"
        "v_mfma_f32_16x16x32_bf16 v[72:75], v[116:119], v[128:131], v[72:75]\n\t"
        "ds_read_b64_tr_b16 v[114:115], %[paF] offset:2184\n\t"
        "v_mfma_f32_16x16x32_bf16 v[68:71], v[116:119], v[160:163], v[68:71]\n\t"
        "ds_read_b64_tr_b16 v[116:117], %[paF] offset:2688\n\t"
        "v_mfma_f32_16x16x32_bf16 v[76:79], v[120:123], v[128:131], v[76:79]\n\t"
        "ds_read_b128 v[128:131], %[pbF] offset:832\n\t"
        "s_add_u32 %[t0], %[w2o], 32768\n\t"
        "buffer_load_dwordx4 a[128:131], %[vl16], %[rws], %[t0] offen offset:0 sc1\n\t"
        "v_mfma_f32_16x16x32_bf16 v[72:75], v[120:123], v[160:163], v[72:75]\n\t"
        "ds_read_b64_tr_b16 v[118:119], %[paF] offset:2696\n\t"
        "v_mfma_f32_16x16x32_bf16 v[76:79], v[124:127], v[160:163], v[76:79]\n\t"
        "ds_read_b64_tr_b16 v[120:121], %[paF] offset:3200\n\t"
        "s_waitcnt lgkmcnt(15)\n\t"
        "v_mfma_f32_16x16x32_bf16 v[76:79], v[96:99], v[152:155], v[76:79]\n\t"
        "ds_read_b64_tr_b16 v[122:123], %[paF] offset:3208\n\t"
        "buffer_load_dwordx4 a[132:135], %[vl16], %[rws], %[t0] offen offset:1024 sc1\n\t"
        "v_mfma_f32_16x16x32_bf16 v[72:75], v[96:99], v[148:151], v[72:75]\n\t"
        "ds_read_b64_tr_b16 v[124:125], %[paF] offset:3712\n\t"
        "s_waitcnt lgkmcnt(15)\n\t"
        "v_mfma_f32_16x16x32_bf16 v[68:71], v[96:99], v[144:147], v[68:71]\n\t"
        "ds_read_b64_tr_b16 v[126:127], %[paF] offset:3720\n\t"
        "s_waitcnt lgkmcnt(14)\n\t"
        "v_mfma_f32_16x16x32_bf16 v[64:67], v[96:99], v[140:143], v[64:67]\n\t"
        "ds_read_b128 v[152:155], %[pbF] offset:3840\n\t"
        "buffer_load_dwordx4 a[136:139], %[vl16], %[rws], %[t0] offen offset:2048 sc1\n\t"
        "s_waitcnt lgkmcnt(12)\n\t"
        "v_mfma_f32_16x16x32_bf16 v[60:63], v[96:99], v[136:139], v[60:63]\n\t"
        "ds_read_b128 v[160:163], %[pbF] offset:256\n\t"
        "s_waitcnt lgkmcnt(10)\n\t"
        "v_mfma_f32_16x16x32_bf16 v[56:59], v[96:99], v[132:135], v[56:59]\n\t"
        "s_waitcnt lgkmcnt(7)\n\t"
        "v_mfma_f32_16x16x32_bf16 v[52:55], v[96:99], v[128:131], v[52:55]\n\t"
        "v_mfma_f32_16x16x32_bf16 v[48:51], v[96:99], v[156:159], v[48:51]\n\t"
        "ds_read_b64_tr_b16 v[96:97], %[paF] offset:192\n\t"
        "buffer_load_dwordx4 a[140:143], %[vl16], %[rws], %[t0] offen offset:3072 sc1\n\t"
        "v_mfma_f32_16x16x32_bf16 v[76:79], v[100:103], v[148:151], v[76:79]\n\t"
        "ds_read_b64_tr_b16 v[98:99], %[paF] offset:200\n\t"
        "v_mfma_f32_16x16x32_bf16 v[72:75], v[100:103], v[144:147], v[72:75]\n\t"
        "ds_read_b128 v[148:151], %[pbF] offset:3328\n\t"
        "v_mfma_f32_16x16x32_bf16 v[68:71], v[100:103], v[140:143], v[68:71]\n\t"
        "s_add_u32 %[t0], %[w2o], 36864\n\t"
        "buffer_load_dwordx4 a[144:147], %[vl16], %[rws], %[t0] offen offset:0 sc1\n\t"
        "v_mfma_f32_16x16x32_bf16 v[64:67], v[100:103], v[136:139], v[64:67]\n\t"
        "v_mfma_f32_16x16x32_bf16 v[60:63], v[100:103], v[132:135], v[60:63]\n\t"
        "v_mfma_f32_16x16x32_bf16 v[56:59], v[100:103], v[128:131], v[56:59]\n\t"
        "buffer_load_dwordx4 a[148:151], %[vl16], %[rws], %[t0] offen offset:1024 sc1\n\t"
        "v_mfma_f32_16x16x32_bf16 v[52:55], v[100:103], v[156:159], v[52:55]\n\t"
        "ds_read_b64_tr_b16 v[100:101], %[paF] offset:704\n\t"
        "v_mfma_f32_16x16x32_bf16 v[76:79], v[104:107], v[144:147], v[76:79]\n\t"
        "ds_read_b128 v[144:147], %[pbF] offset:2816\n\t"
        "v_mfma_f32_16x16x32_bf16 v[72:75], v[104:107], v[140:143], v[72:75]\n\t"
        "ds_read_b64_tr_b16 v[102:103], %[paF] offset:712\n\t"
        "v_mfma_f32_16x16x32_bf16 v[68:71], v[104:107], v[136:139], v[68:71]\n\t"
        "buffer_load_dwordx4 a[152:155], %[vl16], %[rws], %[t0] offen offset:2048 sc1\n\t"
        "v_mfma_f32_16x16x32_bf16 v[64:67], v[104:107], v[132:135], v[64:67]\n\t"
        "v_mfma_f32_16x16x32_bf16 v[60:63], v[104:107], v[128:131], v[60:63]\n\t"
        "v_mfma_f32_16x16x32_bf16 v[56:59], v[104:107], v[156:159], v[56:59]\n\t"
        "ds_read_b64_tr_b16 v[104:105], %[paF] offset:1216\n\t"
        "buffer_load_dwordx4 a[156:159], %[vl16], %[rws], %[t0] offen offset:3072 sc1\n\t"
        "v_mfma_f32_16x16x32_bf16 v[76:79], v[108:111], v[140:143], v[76:79]\n\t"
        "ds_read_b128 v[140:143], %[pbF] offset:2304\n\t"
        "v_mfma_f32_16x16x32_bf16 v[72:75], v[108:111], v[136:139], v[72:75]\n\t"
        "ds_read_b64_tr_b16 v[106:107], %[paF] offset:1224\n\t"
        "v_mfma_f32_16x16x32_bf16 v[68:71], v[108:111], v[132:135], v[68:71]\n\t"
        "s_add_u32 %[t0], %[w2o], 40960\n\t"
        "buffer_load_dwordx4 a[160:163], %[vl16], %[rws], %[t0] offen offset:0 sc1\n\t"
        "v_mfma_f32_16x16x32_bf16 v[64:67], v[108:111], v[128:131], v[64:67]\n\t"
        "v_mfma_f32_16x16x32_bf16 v[60:63], v[108:111], v[156:159], v[60:63]\n\t"
        "ds_read_b64_tr_b16 v[108:109], %[paF] offset:1728\n\t"
        "v_mfma_f32_16x16x32_bf16 v[76:79], v[112:115], v[136:139], v[76:79]\n\t"
        "ds_read_b128 v[136:139], %[pbF] offset:1792\n\t"
        "v_mfma_f32_16x16x32_bf16 v[72:75], v[112:115], v[132:135], v[72:75]\n\t"
        "ds_read_b64_tr_b16 v[110:111], %[paF] offset:1736\n\t"
        "buffer_load_dwordx4 a[164:167], %[vl16], %[rws], %[t0] offen offset:1024 sc1\n\t"
        "v_mfma_f32_16x16x32_bf16 v[68:71], v[112:115], v[128:131], v[68:71]\n\t"
        "v_mfma_f32_16x16x32_bf16 v[64:67], v[112:115], v[156:159], v[64:67]\n\t"
        "ds_read_b64_tr_b16 v[112:113], %[paF] offset:2240\n\t"
        "s_waitcnt lgkmcnt(15)\n\t"
        "v_mfma_f32_16x16x32_bf16 v[76:79], v[116:119], v[132:135], v[76:79]\n\t"
        "ds_read_b128 v[132:135], %[pbF] offset:1280\n\t"
        "buffer_load_dwordx4 a[168:171], %[vl16], %[rws], %[t0] offen offset:2048 sc1\n\t"
        "v_mfma_f32_16x16x32_bf16 v[72:75], v[116:119], v[128:131], v[72:75]\n\t"
        "ds_read_b64_tr_b16 v[114:115], %[paF] offset:2248\n\t"
        "v_mfma_f32_16x16x32_bf16 v[68:71], v[116:119], v[156:159], v[68:71]\n\t"
        "ds_read_b64_tr_b16 v[116:117], %[paF] offset:2752\n\t"
        "v_mfma_f32_16x16x32_bf16 v[76:79], v[120:123], v[128:131], v[76:79]\n\t"
        "ds_read_b128 v[128:131], %[pbF] offset:768\n\t"
        "buffer_load_dwordx4 a[172:175], %[vl16], %[rws], %[t0] offen offset:3072 sc1\n\t"
        "v_mfma_f32_16x16x32_bf16 v[72:75], v[120:123], v[156:159], v[72:75]\n\t"
        "ds_read_b64_tr_b16 v[118:119], %[paF] offset:2760\n\t"
        "v_mfma_f32_16x16x32_bf16 v[76:79], v[124:127], v[156:159], v[76:79]\n\t"
        "ds_read_b64_tr_b16 v[120:121], %[paF] offset:3264\n\t"
        "s_waitcnt lgkmcnt(15)\n\t"
        "v_mfma_f32_16x16x32_bf16 v[76:79], v[96:99], v[152:155], v[76:79]\n\t"
        "ds_read_b64_tr_b16 v[122:123], %[paF] offset:3272\n\t"
        "v_mfma_f32_16x16x32_bf16 v[72:75], v[96:99], v[148:151], v[72:75]\n\t"
        "ds_read_b64_tr_b16 v[124:125], %[paF] offset:3776\n\t"
        "s_add_u32 %[t0], %[w2o], 45056\n\t"
        "buffer_load_dwordx4 a[176:179], %[vl16], %[rws], %[t0] offen offset:0 sc1\n\t"
        "s_waitcnt lgkmcnt(15)\n\t"
        "v_mfma_f32_16x16x32_bf16 v[68:71], v[96:99], v[144:147], v[68:71]\n\t"
        "ds_read_b64_tr_b16 v[126:127], %[paF] offset:3784\n\t"
        "s_waitcnt lgkmcnt(14)\n\t"
        "v_mfma_f32_16x16x32_bf16 v[64:67], v[96:99], v[140:143], v[64:67]\n\t"
        "ds_read_b128 v[152:155], %[pbF] offset:3776\n\t"
        "s_waitcnt lgkmcnt(12)\n\t"
        "v_mfma_f32_16x16x32_bf16 v[60:63], v[96:99], v[136:139], v[60:63]\n\t"
        "ds_read_b128 v[156:159], %[pbF] offset:192\n\t"
        "buffer_load_dwordx4 a[180:183], %[vl16], %[rws], %[t0] offen offset:1024 sc1\n\t"
        "s_waitcnt lgkmcnt(10)\n\t"
        "v_mfma_f32_16x16x32_bf16 v[56:59], v[96:99], v[132:135], v[56:59]\n\t"
        "s_waitcnt lgkmcnt(7)\n\t"
        "v_mfma_f32_16x16x32_bf16 v[52:55], v[96:99], v[128:131], v[52:55]\n\t"
        "v_mfma_f32_16x16x32_bf16 v[48:51], v[96:99], v[160:163], v[48:51]\n\t"
        "ds_read_b64_tr_b16 v[96:97], %[paF] offset:256\n\t"
        "buffer_load_dwordx4 a[184:187], %[vl16], %[rws], %[t0] offen offset:2048 sc1\n\t"
        "v_mfma_f32_16x16x32_bf16 v[76:79], v[100:103], v[148:151], v[76:79]\n\t"
        "ds_read_b64_tr_b16 v[98:99], %[paF] offset:264\n\t"
        "v_mfma_f32_16x16x32_bf16 v[72:75], v[100:103], v[144:147], v[72:75]\n\t"
        "ds_read_b128 v[148:151], %[pbF] offset:3264\n\t"
        "v_mfma_f32_16x16x32_bf16 v[68:71], v[100:103], v[140:143], v[68:71]\n\t"
        "v_mfma_f32_16x16x32_bf16 v[64:67], v[100:103], v[136:139], v[64:67]\n\t"
        "buffer_load_dwordx4 a[188:191], %[vl16], %[rws], %[t0] offen offset:3072 sc1\n\t"
        "v_mfma_f32_16x16x32_bf16 v[60:63], v[100:103], v[132:135], v[60:63]\n\t"
        "v_mfma_f32_16x16x32_bf16 v[56:59], v[100:103], v[128:131], v[56:59]\n\t"
        "v_mfma_f32_16x16x32_bf16 v[52:55], v[100:103], v[160:163], v[52:55]\n\t"
        "ds_read_b64_tr_b16 v[100:101], %[paF] offset:768\n\t"
        "s_add_u32 %[t0], %[w2o], 49152\n\t"
        "buffer_load_dwordx4 a[192:195], %[vl16], %[rws], %[t0] offen offset:0 sc1\n\t"
        "v_mfma_f32_16x16x32_bf16 v[76:79], v[104:107], v[144:147], v[76:79]\n\t"
        "ds_read_b128 v[144:147], %[pbF] offset:2752\n\t"
        "v_mfma_f32_16x16x32_bf16 v[72:75], v[104:107], v[140:143], v[72:75]\n\t"
        "ds_read_b64_tr_b16 v[102:103], %[paF] offset:776\n\t"
        "buffer_load_dwordx2 v[204:205], %[pollh], %[rws], 0 offen sc1\n\t"
        "v_mfma_f32_16x16x32_bf16 v[68:71], v[104:107], v[136:139], v[68:71]\n\t"
        "buffer_load_dwordx4 a[196:199], %[vl16], %[rws], %[t0] offen offset:1024 sc1\n\t"
        "v_mfma_f32_16x16x32_bf16 v[64:67], v[104:107], v[132:135], v[64:67]\n\t"
        "v_mfma_f32_16x16x32_bf16 v[60:63], v[104:107], v[128:131], v[60:63]\n\t"
        "v_mfma_f32_16x16x32_bf16 v[56:59], v[104:107], v[160:163], v[56:59]\n\t"
        "ds_read_b64_tr_b16 v[104:105], %[paF] offset:1280\n\t"
        "v_mfma_f32_16x16x32_bf16 v[76:79], v[108:111], v[140:143], v[76:79]\n\t"
        "ds_read_b128 v[140:143], %[pbF] offset:2240\n\t"
        "buffer_load_dwordx4 a[200:203], %[vl16], %[rws], %[t0] offen offset:2048 sc1\n\t"
        "v_mfma_f32_16x16x32_bf16 v[72:75], v[108:111], v[136:139], v[72:75]\n\t"
        "ds_read_b64_tr_b16 v[106:107], %[paF] offset:1288\n\t"
        "v_mfma_f32_16x16x32_bf16 v[68:71], v[108:111], v[132:135], v[68:71]\n\t"
        "v_mfma_f32_16x16x32_bf16 v[64:67], v[108:111], v[128:131], v[64:67]\n\t"
        "buffer_load_dwordx4 a[204:207], %[vl16], %[rws], %[t0] offen offset:3072 sc1\n\t"
        "v_mfma_f32_16x16x32_bf16 v[60:63], v[108:111], v[160:163], v[60:63]\n\t"
        "ds_read_b64_tr_b16 v[108:109], %[paF] offset:1792\n\t"
        "v_mfma_f32_16x16x32_bf16 v[76:79], v[112:115], v[136:139], v[76:79]\n\t"
        "ds_read_b128 v[136:139], %[pbF] offset:1728\n\t"
        "v_mfma_f32_16x16x32_bf16 v[72:75], v[112:115], v[132:135], v[72:75]\n\t"
        "ds_read_b64_tr_b16 v[110:111], %[paF] offset:1800\n\t"
        "s_add_u32 %[t0], %[w2o], 53248\n\t"
        "buffer_load_dwordx4 a[208:211], %[vl16], %[rws], %[t0] offen offset:0 sc1\n\t"
        "v_mfma_f32_16x16x32_bf16 v[68:71], v[112:115], v[128:131], v[68:71]\n\t"
        "v_mfma_f32_16x16x32_bf16 v[64:67], v[112:115], v[160:163], v[64:67]\n\t"
        "ds_read_b64_tr_b16 v[112:113], %[paF] offset:2304\n\t"
        "s_waitcnt lgkmcnt(15)\n\t"
        "v_mfma_f32_16x16x32_bf16 v[76:79], v[116:119], v[132:135], v[76:79]\n\t"
        "ds_read_b128 v[132:135], %[pbF] offset:1216\n\t"
        "v_mfma_f32_16x16x32_bf16 v[72:75], v[116:119], v[128:131], v[72:75]\n\t"
        "ds_read_b64_tr_b16 v[114:115], %[paF] offset:2312\n\t"
        "buffer_load_dwordx4 a[212:215], %[vl16], %[rws], %[t0] offen offset:1024 sc1\n\t"
        "v_mfma_f32_16x16x32_bf16 v[68:71], v[116:119], v[160:163], v[68:71]\n\t"
        "ds_read_b64_tr_b16 v[116:117], %[paF] offset:2816\n\t"
        "v_mfma_f32_16x16x32_bf16 v[76:79], v[120:123], v[128:131], v[76:79]\n\t"
        "ds_read_b128 v[128:131], %[pbF] offset:704\n\t"
        "v_mfma_f32_16x16x32_bf16 v[72:75], v[120:123], v[160:163], v[72:75]\n\t"
        "ds_read_b64_tr_b16 v[118:119], %[paF] offset:2824\n\t"
        "buffer_load_dwordx4 a[216:219], %[vl16], %[rws], %[t0] offen offset:2048 sc1\n\t"
        "v_mfma_f32_16x16x32_bf16 v[76:79], v[124:127], v[160:163], v[76:79]\n\t"
        "ds_read_b64_tr_b16 v[120:121], %[paF] offset:3328\n\t"
        "s_waitcnt lgkmcnt(15)\n\t"
        "v_mfma_f32_16x16x32_bf16 v[76:79], v[96:99], v[152:155], v[76:79]\n\t"
        "ds_read_b64_tr_b16 v[122:123], %[paF] offset:3336\n\t"
        "v_mfma_f32_16x16x32_bf16 v[72:75], v[96:99], v[148:151], v[72:75]\n\t"
        "ds_read_b64_tr_b16 v[124:125], %[paF] offset:3840\n\t"
        "buffer_load_dwordx4 a[220:223], %[vl16], %[rws], %[t0] offen offset:3072 sc1\n\t"
        "s_waitcnt lgkmcnt(15)\n\t"
        "v_mfma_f32_16x16x32_bf16 v[68:71], v[96:99], v[144:147], v[68:71]\n\t"
        "ds_read_b64_tr_b16 v[126:127], %[paF] offset:3848\n\t"
        "s_waitcnt lgkmcnt(14)\n\t"
        "v_mfma_f32_16x16x32_bf16 v[64:67], v[96:99], v[140:143], v[64:67]\n\t"
        "s_waitcnt lgkmcnt(11)\n\t"
        "v_mfma_f32_16x16x32_bf16 v[60:63], v[96:99], v[136:139], v[60:63]\n\t"
        "s_waitcnt lgkmcnt(8)\n\t"
        "v_mfma_f32_16x16x32_bf16 v[56:59], v[96:99], v[132:135], v[56:59]\n\t"
        "s_add_u32 %[t0], %[w2o], 57344\n\t"
        "buffer_load_dwordx4 a[224:227], %[vl16], %[rws], %[t0] offen offset:0 sc1\n\t"
        "s_waitcnt lgkmcnt(5)\n\t"
        "v_mfma_f32_16x16x32_bf16 v[52:55], v[96:99], v[128:131], v[52:55]\n\t"
        "v_mfma_f32_16x16x32_bf16 v[48:51], v[96:99], v[156:159], v[48:51]\n\t"
        "v_mfma_f32_16x16x32_bf16 v[76:79], v[100:103], v[148:151], v[76:79]\n\t"
        "buffer_load_dwordx4 a[228:231], %[vl16], %[rws], %[t0] offen offset:1024 sc1\n\t"
        "s_waitcnt vmcnt(9)\n\t"
        "v_cmp_ne_u32 vcc, %[elo], v204\n\t"
        "v_cmp_ne_u32 %[tm], %[ehi], v205\n\t"
        "s_or_b64 vcc, vcc, %[tm]\n\t"
        "s_cbranch_vccz Lok1_%=\n\t"
        "s_mov_b32 %[t1], 0\n\t"
        "Lsp1_%=:\n\t"
        "buffer_load_dwordx2 v[204:205], %[pollh], %[rws], 0 offen sc1\n\t"
        "s_waitcnt vmcnt(0)\n\t"
        "v_cmp_ne_u32 vcc, %[elo], v204\n\t"
        "v_cmp_ne_u32 %[tm], %[ehi], v205\n\t"
        "s_or_b64 vcc, vcc, %[tm]\n\t"
        "s_cbranch_vccz Lok1_%=\n\t"
        "s_sleep 2\n\t"
        "s_add_u32 %[t1], %[t1], 1\n\t"
        "s_cmp_lt_u32 %[t1], 0x40000\n\t"
        "s_cbranch_scc1 Lsp1_%=\n\t"
        "Lok1_%=:\n\t"
        "v_mfma_f32_16x16x32_bf16 v[72:75], v[100:103], v[144:147], v[72:75]\n\t"
        "v_mfma_f32_16x16x32_bf16 v[68:71], v[100:103], v[140:143], v[68:71]\n\t"
        "v_mfma_f32_16x16x32_bf16 v[64:67], v[100:103], v[136:139], v[64:67]\n\t"
        "buffer_load_dwordx4 a[232:235], %[vl16], %[rws], %[t0] offen offset:2048 sc1\n\t"
        "s_add_u32 %[t1], %[hoff0], 0\n\t"
        "buffer_load_dwordx4 v[0:3], %[vl16], %[rws], %[t1] offen offset:0 sc1\n\t"
        "v_mfma_f32_16x16x32_bf16 v[60:63], v[100:103], v[132:135], v[60:63]\n\t"
        "v_mfma_f32_16x16x32_bf16 v[56:59], v[100:103], v[128:131], v[56:59]\n\t"
        "v_mfma_f32_16x16x32_bf16 v[52:55], v[100:103], v[156:159], v[52:55]\n\t"
        "buffer_load_dwordx4 v[4:7], %[vl16], %[rws], %[t1] offen offset:1024 sc1\n\t"
        "v_mfma_f32_16x16x32_bf16 v[76:79], v[104:107], v[144:147], v[76:79]\n\t"
        "buffer_load_dwordx4 a[236:239], %[vl16], %[rws], %[t0] offen offset:3072 sc1\n\t"
        "v_mfma_f32_16x16x32_bf16 v[72:75], v[104:107], v[140:143], v[72:75]\n\t"
        "v_mfma_f32_16x16x32_bf16 v[68:71], v[104:107], v[136:139], v[68:71]\n\t"
        "buffer_load_dwordx4 v[8:11], %[vl16], %[rws], %[t1] offen offset:2048 sc1\n\t"
        "v_mfma_f32_16x16x32_bf16 v[64:67], v[104:107], v[132:135], v[64:67]\n\t"
        "v_mfma_f32_16x16x32_bf16 v[60:63], v[104:107], v[128:131], v[60:63]\n\t"
        "v_mfma_f32_16x16x32_bf16 v[56:59], v[104:107], v[156:159], v[56:59]\n\t"
        "buffer_load_dwordx4 v[12:15], %[vl16], %[rws], %[t1] offen offset:3072 sc1\n\t"
        "v_mfma_f32_16x16x32_bf16 v[76:79], v[108:111], v[140:143], v[76:79]\n\t"
        "v_mfma_f32_16x16x32_bf16 v[72:75], v[108:111], v[136:139], v[72:75]\n\t"
        "v_mfma_f32_16x16x32_bf16 v[68:71], v[108:111], v[132:135], v[68:71]\n\t"
        "s_add_u32 %[t1], %[hoff0], 4096\n\t"
        "buffer_load_dwordx4 v[16:19], %[vl16], %[rws], %[t1] offen offset:0 sc1\n\t"
        "v_mfma_f32_16x16x32_bf16 v[64:67], v[108:111], v[128:131], v[64:67]\n\t"
        "v_mfma_f32_16x16x32_bf16 v[60:63], v[108:111], v[156:159], v[60:63]\n\t"
        "v_mfma_f32_16x16x32_bf16 v[76:79], v[112:115], v[136:139], v[76:79]\n\t"
        "buffer_load_dwordx4 v[20:23], %[vl16], %[rws], %[t1] offen offset:1024 sc1\n\t"
        "v_mfma_f32_16x16x32_bf16 v[72:75], v[112:115], v[132:135], v[72:75]\n\t"
        "v_mfma_f32_16x16x32_bf16 v[68:71], v[112:115], v[128:131], v[68:71]\n\t"
        "v_mfma_f32_16x16x32_bf16 v[64:67], v[112:115], v[156:159], v[64:67]\n\t"
        "buffer_load_dwordx4 v[24:27], %[vl16], %[rws], %[t1] offen offset:2048 sc1\n\t"
        "s_waitcnt lgkmcnt(4)\n\t"
        "v_mfma_f32_16x16x32_bf16 v[76:79], v[116:119], v[132:135], v[76:79]\n\t"
        "v_mfma_f32_16x16x32_bf16 v[72:75], v[116:119], v[128:131], v[72:75]\n\t"
        "v_mfma_f32_16x16x32_bf16 v[68:71], v[116:119], v[156:159], v[68:71]\n\t"
        "buffer_load_dwordx4 v[28:31], %[vl16], %[rws], %[t1] offen offset:3072 sc1\n\t"
        "s_waitcnt lgkmcnt(2)\n\t"
        "v_mfma_f32_16x16x32_bf16 v[76:79], v[120:123], v[128:131], v[76:79]\n\t"
        "v_mfma_f32_16x16x32_bf16 v[72:75], v[120:123], v[156:159], v[72:75]\n\t"
        "s_waitcnt lgkmcnt(0)\n\t"
        "v_mfma_f32_16x16x32_bf16 v[76:79], v[124:127], v[156:159], v[76:79]\n\t"
        "s_add_u32 %[t1], %[hoff0], 8192\n\t"
        "buffer_load_dwordx4 v[32:35], %[vl16], %[rws], %[t1] offen offset:0 sc1\n\t"
        "s_nop 7\n\t"
        "ds_write_b128 %[aEx], v[68:71] offset:5120\n\t"
        "ds_write_b128 %[aEx], v[72:75] offset:6144\n\t"
        "ds_write_b128 %[aEx], v[76:79] offset:7168\n\t"
        "buffer_load_dwordx4 v[36:39], %[vl16], %[rws], %[t1] offen offset:1024 sc1\n\t"
        "buffer_load_dwordx4 v[40:43], %[vl16], %[rws], %[t1] offen offset:2048 sc1\n\t"
        "buffer_load_dwordx4 v[44:47], %[vl16], %[rws], %[t1] offen offset:3072 sc1\n\t"
        "s_add_u32 %[t1], %[hoff1], 0\n\t"
        "buffer_load_dwordx4 v[96:99], %[vl16], %[rws], %[t1] offen offset:0 sc1\n\t"
        "buffer_load_dwordx4 v[100:103], %[vl16], %[rws], %[t1] offen offset:1024 sc1\n\t"
        "buffer_load_dwordx4 v[104:107], %[vl16], %[rws], %[t1] offen offset:2048 sc1\n\t"
        "v_bfe_u32 v193, %[vl16], 8, 1\n\t"
        "v_mul_u32_u24 v192, 0x1f8, v193\n\t"
        "v_add_u32 v192, v192, %[oY]\n\t"
        "s_waitcnt lgkmcnt(0)\n\t"
        "s_barrier\n\t"
        "ds_read_b128 v[144:147], %[aEx] offset:0\n\t"
        "ds_read_b128 v[148:151], %[aEx] offset:1024\n\t"
        "ds_read_b128 v[152:155], %[aEx] offset:2048\n\t"
        "ds_read_b128 v[156:159], %[aEx] offset:3072\n\t"
        "ds_read_b128 v[160:163], %[aEx] offset:4096\n\t"
        "buffer_load_dwordx4 v[108:111], %[vl16], %[rws], %[t1] offen offset:3072 sc1\n\t"
        "s_waitcnt lgkmcnt(4)\n\t"
        "v_pk_add_f32 v[48:49], v[48:49], v[144:145]\n\t"
        "v_pk_add_f32 v[50:51], v[50:51], v[146:147]\n\t"
        "v_cvt_pk_bf16_f32 v182, v48, v49\n\t"
        "v_cvt_pk_bf16_f32 v183, v50, v51\n\t"
        "s_add_u32 %[t1], %[hoff1], 4096\n\t"
        "buffer_load_dwordx4 v[112:115], %[vl16], %[rws], %[t1] offen offset:0 sc1\n\t"
        "s_waitcnt lgkmcnt(3)\n\t"
        "v_pk_add_f32 v[52:53], v[52:53], v[148:149]\n\t"
        "v_pk_add_f32 v[54:55], v[54:55], v[150:151]\n\t"
        "v_cvt_pk_bf16_f32 v184, v52, v53\n\t"
        "v_cvt_pk_bf16_f32 v185, v54, v55\n\t"
        "s_nop 1\n\t"
        "v_permlane16_swap_b32 v182, v184\n\t"
        "v_permlane16_swap_b32 v183, v185\n\t"
        "buffer_store_dwordx4 v[182:185], v192, %[rws], 0 offen offset:0 sc1\n\t"
        "s_waitcnt lgkmcnt(2)\n\t"
        "v_pk_add_f32 v[56:57], v[56:57], v[152:153]\n\t"
        "v_pk_add_f32 v[58:59], v[58:59], v[154:155]\n\t"
        "v_cvt_pk_bf16_f32 v186, v56, v57\n\t"
        "v_cvt_pk_bf16_f32 v187, v58, v59\n\t"
        "s_waitcnt lgkmcnt(1)\n\t"
        "v_pk_add_f32 v[60:61], v[60:61], v[156:157]\n\t"
        "v_pk_add_f32 v[62:63], v[62:63], v[158:159]\n\t"
        "v_cvt_pk_bf16_f32 v188, v60, v61\n\t"
        "v_cvt_pk_bf16_f32 v189, v62, v63\n\t"
        "s_nop 1\n\t"
        "v_permlane16_swap_b32 v186, v188\n\t"
        "v_permlane16_swap_b32 v187, v189\n\t"
        "buffer_store_dwordx4 v[186:189], v192, %[rws], 0 offen offset:1024 sc1\n\t"
        "s_waitcnt lgkmcnt(0)\n\t"
        "v_pk_add_f32 v[64:65], v[64:65], v[160:161]\n\t"
        "v_pk_add_f32 v[66:67], v[66:67], v[162:163]\n\t"
        "v_cvt_pk_bf16_f32 v190, v64, v65\n\t"
        "v_cvt_pk_bf16_f32 v191, v66, v67\n\t"
        "buffer_store_dwordx2 v[190:191], %[oY], %[rws], 0 offen offset:2048 sc1\n\t"
        "s_branch Ljoin_%=\n\t"
        "Lodd_%=:\n\t"
        "s_cmp_eq_u32 %[par], 0\n\t"
        "s_cbranch_scc0 Lodd1_%=\n\t"
        "s_waitcnt lgkmcnt(0)\n\t"
        "v_pk_fma_f32 v[180:181], v[172:173], %[st0], v[164:165] op_sel:[0,1,0] op_sel_hi:[1,1,1]\n\t"
        "v_pk_fma_f32 v[182:183], v[174:175], %[st0], v[166:167] op_sel:[0,1,0] op_sel_hi:[1,1,1]\n\t"
        "v_pk_fma_f32 v[184:185], v[176:177], %[st0], v[168:169] op_sel:[0,1,0] op_sel_hi:[1,1,1]\n\t"
        "v_pk_fma_f32 v[186:187], v[178:179], %[st0], v[170:171] op_sel:[0,1,0] op_sel_hi:[1,1,1]\n\t"
        "v_pk_fma_f32 v[180:181], v[0:1], %[st0], v[180:181] op_sel:[0,0,0] op_sel_hi:[1,0,1]\n\t"
        "v_pk_fma_f32 v[182:183], v[2:3], %[st0], v[182:183] op_sel:[0,0,0] op_sel_hi:[1,0,1]\n\t"
        "v_pk_fma_f32 v[184:185], v[4:5], %[st0], v[184:185] op_sel:[0,0,0] op_sel_hi:[1,0,1]\n\t"
        "v_pk_fma_f32 v[186:187], v[6:7], %[st0], v[186:187] op_sel:[0,0,0] op_sel_hi:[1,0,1]\n\t"
        "v_pk_mul_f32 v[188:189], v[180:181], v[180:181]\n\t"
        "v_pk_mul_f32 v[190:191], v[182:183], v[182:183]\n\t"
        "v_pk_mul_f32 v[192:193], v[184:185], v[184:185]\n\t"
        "v_pk_mul_f32 v[194:195], v[186:187], v[186:187]\n\t"
        "v_pk_fma_f32 v[188:189], v[188:189], %[cc], %[cc] op_sel:[0,0,1] op_sel_hi:[1,0,1]\n\t"
        "v_pk_fma_f32 v[190:191], v[190:191], %[cc], %[cc] op_sel:[0,0,1] op_sel_hi:[1,0,1]\n\t"
        "v_pk_fma_f32 v[192:193], v[192:193], %[cc], %[cc] op_sel:[0,0,1] op_sel_hi:[1,0,1]\n\t"
        "v_pk_fma_f32 v[194:195], v[194:195], %[cc], %[cc] op_sel:[0,0,1] op_sel_hi:[1,0,1]\n\t"
        "v_pk_mul_f32 v[188:189], v[188:189], v[180:181]\n\t"
        "v_pk_mul_f32 v[190:191], v[190:191], v[182:183]\n\t"
        "v_pk_mul_f32 v[192:193], v[192:193], v[184:185]\n\t"
        "v_pk_mul_f32 v[194:195], v[194:195], v[186:187]\n\t"
        "v_exp_f32 v188, v188\n\t"
        "v_exp_f32 v189, v189\n\t"
        "v_exp_f32 v190, v190\n\t"
        "s_add_u32 %[t0], %[w2o], 0\n\t"
        "buffer_load_dwordx4 a[0:3], %[vl16], %[rws], %[t0] offen offset:0 sc1\n\t"
        "v_exp_f32 v191, v191\n\t"
        "v_exp_f32 v192, v192\n\t"
        "v_exp_f32 v193, v193\n\t"
        "v_exp_f32 v194, v194\n\t"
        "v_exp_f32 v195, v195\n\t"
        "v_pk_add_f32 v[188:189], v[188:189], 1.0 op_sel_hi:[1,0]\n\t"
        "v_pk_add_f32 v[190:191], v[190:191], 1.0 op_sel_hi:[1,0]\n\t"
        "v_pk_add_f32 v[192:193], v[192:193], 1.0 op_sel_hi:[1,0]\n\t"
        "v_pk_add_f32 v[194:195], v[194:195], 1.0 op_sel_hi:[1,0]\n\t"
        "v_rcp_f32 v188, v188\n\t"
        "v_rcp_f32 v189, v189\n\t"
        "v_rcp_f32 v190, v190\n\t"
        "buffer_load_dwordx4 a[4:7], %[vl16], %[rws], %[t0] offen offset:1024 sc1\n\t"
        "v_rcp_f32 v191, v191\n\t"
        "v_rcp_f32 v192, v192\n\t"
        "v_rcp_f32 v193, v193\n\t"
        "v_rcp_f32 v194, v194\n\t"
        "v_rcp_f32 v195, v195\n\t"
        "v_pk_mul_f32 v[188:189], v[188:189], v[180:181]\n\t"
        "v_pk_mul_f32 v[190:191], v[190:191], v[182:183]\n\t"
        "v_pk_mul_f32 v[192:193], v[192:193], v[184:185]\n\t"
        "v_pk_mul_f32 v[194:195], v[194:195], v[186:187]\n\t"
        "v_cvt_pk_bf16_f32 v196, v188, v189\n\t"
        "v_cvt_pk_bf16_f32 v197, v190, v191\n\t"
        "v_cvt_pk_bf16_f32 v198, v192, v193\n\t"
        "v_cvt_pk_bf16_f32 v199, v194, v195\n\t"
        "buffer_store_dwordx4 v[196:199], %[vl16], %[rws], %[hs0] offen sc1\n\t"
        "v_mov_b32 v136, 0\n\t"
        "v_mov_b32 v137, 0\n\t"
        "v_readlane_b32 %[t0], %[ddF], %[w]\n\t"
        "v_lshlrev_b32 v128, 16, %[pref]\n\t"
        "s_lshl_b32 %[t0], %[t0], 16\n\t"
        "v_add_f32 v128, %[t0], v128\n\t"
        "s_mov_b32 vcc_lo, 0x80000000\n\t"
        "s_mov_b32 vcc_hi, 0x20004000\n\t"
        "v_cvt_pk_bf16_f32 v128, v128, v128\n\t"
        "v_cndmask_b32 v128, %[pref], v128, vcc\n\t"
        "ds_write_b16 %[aPre], v128\n\t"
        "ds_write_b64 %[aZ], v[136:137]\n\t"
        "v_pk_fma_f32 v[180:181], v[172:173], %[st1], v[164:165] op_sel:[0,1,0] op_sel_hi:[1,1,1]\n\t"
        "v_pk_fma_f32 v[182:183], v[174:175], %[st1], v[166:167] op_sel:[0,1,0] op_sel_hi:[1,1,1]\n\t"
        "v_pk_fma_f32 v[184:185], v[176:177], %[st1], v[168:169] op_sel:[0,1,0] op_sel_hi:[1,1,1]\n\t"
        "v_pk_fma_f32 v[186:187], v[178:179], %[st1], v[170:171] op_sel:[0,1,0] op_sel_hi:[1,1,1]\n\t"
        "v_pk_fma_f32 v[180:181], v[8:9], %[st1], v[180:181] op_sel:[0,0,0] op_sel_hi:[1,0,1]\n\t"
        "v_pk_fma_f32 v[182:183], v[10:11], %[st1], v[182:183] op_sel:[0,0,0] op_sel_hi:[1,0,1]\n\t"
        "v_pk_fma_f32 v[184:185], v[12:13], %[st1], v[184:185] op_sel:[0,0,0] op_sel_hi:[1,0,1]\n\t"
        "v_pk_fma_f32 v[186:187], v[14:15], %[st1], v[186:187] op_sel:[0,0,0] op_sel_hi:[1,0,1]\n\t"
        "v_pk_mul_f32 v[188:189], v[180:181], v[180:181]\n\t"
        "v_pk_mul_f32 v[190:191], v[182:183], v[182:183]\n\t"
        "v_pk_mul_f32 v[192:193], v[184:185], v[184:185]\n\t"
        "v_pk_mul_f32 v[194:195], v[186:187], v[186:187]\n\t"
        "v_pk_fma_f32 v[188:189], v[188:189], %[cc], %[cc] op_sel:[0,0,1] op_sel_hi:[1,0,1]\n\t"
        "v_pk_fma_f32 v[190:191], v[190:191], %[cc], %[cc] op_sel:[0,0,1] op_sel_hi:[1,0,1]\n\t"
        "v_pk_fma_f32 v[192:193], v[192:193], %[cc], %[cc] op_sel:[0,0,1] op_sel_hi:[1,0,1]\n\t"
        "v_pk_fma_f32 v[194:195], v[194:195], %[cc], %[cc] op_sel:[0,0,1] op_sel_hi:[1,0,1]\n\t"
        "v_pk_mul_f32 v[188:189], v[188:189], v[180:181]\n\t"
        "v_pk_mul_f32 v[190:191], v[190:191], v[182:183]\n\t"
        "v_pk_mul_f32 v[192:193], v[192:193], v[184:185]\n\t"
        "v_pk_mul_f32 v[194:195], v[194:195], v[186:187]\n\t"
        "ds_read_b128 v[164:167], %[aC1] offset:0\n\t"
        "ds_read_b128 v[172:175], %[aC1] offset:384\n\t"
        "ds_read_b128 v[168:171], %[aC1] offset:32\n\t"
        "ds_read_b128 v[176:179], %[aC1] offset:416\n\t"
        "v_exp_f32 v188, v188\n\t"
        "s_waitcnt vmcnt(10)\n\t"
        "v_exp_f32 v189, v189\n\t"
        "v_readlane_b32 %[t0], %[ddF], 0\n\t"
        "v_lshlrev_b32 v128, 16, v96\n\t"
        "s_lshl_b32 %[t0], %[t0], 16\n\t"
        "v_add_f32 v128, %[t0], v128\n\t"
        "v_exp_f32 v190, v190\n\t"
        "s_add_u32 %[t0], %[w2o], 0\n\t"
        "buffer_load_dwordx4 a[8:11], %[vl16], %[rws], %[t0] offen offset:2048 sc1\n\t"
        "v_and_b32 v129, 0xffff0000, v96\n\t"
        "s_cmp_eq_u32 %[w], 0\n\t"
        "v_cvt_pk_bf16_f32 v128, v128, v129\n\t"
        "s_cselect_b64 vcc, 1, 0\n\t"
        "v_cndmask_b32 v96, v96, v128, vcc\n\t"
        "v_exp_f32 v191, v191\n\t"
        "ds_write_b128 %[aFc0], v[96:99] offset:0\n\t"
        "v_exp_f32 v192, v192\n\t"
        "v_alignbit_b32 v128, v100, v96, 16\n\t"
        "v_exp_f32 v193, v193\n\t"
        "v_exp_f32 v194, v194\n\t"
        "v_exp_f32 v195, v195\n\t"
        "v_pk_add_f32 v[188:189], v[188:189], 1.0 op_sel_hi:[1,0]\n\t"
        "v_pk_add_f32 v[190:191], v[190:191], 1.0 op_sel_hi:[1,0]\n\t"
        "v_pk_add_f32 v[192:193], v[192:193], 1.0 op_sel_hi:[1,0]\n\t"
        "v_pk_add_f32 v[194:195], v[194:195], 1.0 op_sel_hi:[1,0]\n\t"
        "v_alignbit_b32 v129, v101, v97, 16\n\t"
        "v_alignbit_b32 v130, v102, v98, 16\n\t"
        "v_rcp_f32 v188, v188\n\t"
        "v_alignbit_b32 v131, v103, v99, 16\n\t"
        "ds_write_b128 %[aFc0], v[100:103] offset:8320\n\t"
        "v_rcp_f32 v189, v189\n\t"
        "v_alignbit_b32 v132, v101, v100, 16\n\t"
        "v_alignbit_b32 v133, v102, v101, 16\n\t"
        "v_rcp_f32 v190, v190\n\t"
        "buffer_load_dwordx4 a[12:15], %[vl16], %[rws], %[t0] offen offset:3072 sc1\n\t"
        "v_alignbit_b32 v134, v103, v102, 16\n\t"
        "v_alignbit_b32 v135, v104, v103, 16\n\t"
        "v_rcp_f32 v191, v191\n\t"
        "ds_write_b128 %[aFc0], v[128:131] offset:4160\n\t"
        "ds_write_b128 %[aFc0], v[132:135] offset:12480\n\t"
        "v_rcp_f32 v192, v192\n\t"
        "v_rcp_f32 v193, v193\n\t"
        "v_rcp_f32 v194, v194\n\t"
        "v_rcp_f32 v195, v195\n\t"
        "v_pk_mul_f32 v[188:189], v[188:189], v[180:181]\n\t"
        "v_pk_mul_f32 v[190:191], v[190:191], v[182:183]\n\t"
        "v_pk_mul_f32 v[192:193], v[192:193], v[184:185]\n\t"
        "v_pk_mul_f32 v[194:195], v[194:195], v[186:187]\n\t"
        "v_cvt_pk_bf16_f32 v196, v188, v189\n\t"
        "v_cvt_pk_bf16_f32 v197, v190, v191\n\t"
        "v_cvt_pk_bf16_f32 v198, v192, v193\n\t"
        "v_cvt_pk_bf16_f32 v199, v194, v195\n\t"
        "s_add_u32 %[t1], %[hs0], 0xc000\n\t"
        "buffer_store_dwordx4 v[196:199], %[vl16], %[rws], %[t1] offen sc1\n\t"
        "s_waitcnt lgkmcnt(4)\n\t"
        "v_pk_fma_f32 v[180:181], v[172:173], %[st0], v[164:165] op_sel:[0,1,0] op_sel_hi:[1,1,1]\n\t"
        "v_pk_fma_f32 v[182:183], v[174:175], %[st0], v[166:167] op_sel:[0,1,0] op_sel_hi:[1,1,1]\n\t"
        "v_pk_fma_f32 v[184:185], v[176:177], %[st0], v[168:169] op_sel:[0,1,0] op_sel_hi:[1,1,1]\n\t"
        "v_pk_fma_f32 v[186:187], v[178:179], %[st0], v[170:171] op_sel:[0,1,0] op_sel_hi:[1,1,1]\n\t"
        "v_pk_fma_f32 v[180:181], v[16:17], %[st0], v[180:181] op_sel:[0,0,0] op_sel_hi:[1,0,1]\n\t"
        "v_pk_fma_f32 v[182:183], v[18:19], %[st0], v[182:183] op_sel:[0,0,0] op_sel_hi:[1,0,1]\n\t"
        "v_pk_fma_f32 v[184:185], v[20:21], %[st0], v[184:185] op_sel:[0,0,0] op_sel_hi:[1,0,1]\n\t"
        "v_pk_fma_f32 v[186:187], v[22:23], %[st0], v[186:187] op_sel:[0,0,0] op_sel_hi:[1,0,1]\n\t"
        "v_pk_mul_f32 v[188:189], v[180:181], v[180:181]\n\t"
        "v_pk_mul_f32 v[190:191], v[182:183], v[182:183]\n\t"
        "v_pk_mul_f32 v[192:193], v[184:185], v[184:185]\n\t"
        "v_pk_mul_f32 v[194:195], v[186:187], v[186:187]\n\t"
        "v_pk_fma_f32 v[188:189], v[188:189], %[cc], %[cc] op_sel:[0,0,1] op_sel_hi:[1,0,1]\n\t"
        "v_pk_fma_f32 v[190:191], v[190:191], %[cc], %[cc] op_sel:[0,0,1] op_sel_hi:[1,0,1]\n\t"
        "v_pk_fma_f32 v[192:193], v[192:193], %[cc], %[cc] op_sel:[0,0,1] op_sel_hi:[1,0,1]\n\t"
        "v_pk_fma_f32 v[194:195], v[194:195], %[cc], %[cc] op_sel:[0,0,1] op_sel_hi:[1,0,1]\n\t"
        "v_pk_mul_f32 v[188:189], v[188:189], v[180:181]\n\t"
        "v_pk_mul_f32 v[190:191], v[190:191], v[182:183]\n\t"
        "v_pk_mul_f32 v[192:193], v[192:193], v[184:185]\n\t"
        "v_pk_mul_f32 v[194:195], v[194:195], v[186:187]\n\t"
        "v_exp_f32 v188, v188\n\t"
        "s_waitcnt vmcnt(10)\n\t"
        "v_exp_f32 v189, v189\n\t"
        "v_readlane_b32 %[t0], %[ddF], 1\n\t"
        "v_lshlrev_b32 v128, 16, v106\n\t"
        "s_lshl_b32 %[t0], %[t0], 16\n\t"
        "v_add_f32 v128, %[t0], v128\n\t"
        "v_exp_f32 v190, v190\n\t"
        "s_add_u32 %[t0], %[w2o], 4096\n\t"
        "buffer_load_dwordx4 a[16:19], %[vl16], %[rws], %[t0] offen offset:0 sc1\n\t"
        "v_and_b32 v129, 0xffff0000, v106\n\t"
        "s_cmp_eq_u32 %[w], 0\n\t"
        "v_cvt_pk_bf16_f32 v128, v128, v129\n\t"
        "s_cselect_b64 vcc, 1, 0\n\t"
        "v_cndmask_b32 v106, v106, v128, vcc\n\t"
        "v_exp_f32 v191, v191\n\t"
        "ds_write_b128 %[aFc1], v[106:109] offset:0\n\t"
        "v_exp_f32 v192, v192\n\t"
        "v_alignbit_b32 v128, v110, v106, 16\n\t"
        "v_exp_f32 v193, v193\n\t"
        "v_exp_f32 v194, v194\n\t"
        "v_exp_f32 v195, v195\n\t"
        "v_pk_add_f32 v[188:189], v[188:189], 1.0 op_sel_hi:[1,0]\n\t"
        "v_pk_add_f32 v[190:191], v[190:191], 1.0 op_sel_hi:[1,0]\n\t"
        "v_pk_add_f32 v[192:193], v[192:193], 1.0 op_sel_hi:[1,0]\n\t"
        "v_pk_add_f32 v[194:195], v[194:195], 1.0 op_sel_hi:[1,0]\n\t"
        "v_alignbit_b32 v129, v111, v107, 16\n\t"
        "v_alignbit_b32 v130, v112, v108, 16\n\t"
        "v_rcp_f32 v188, v188\n\t"
        "v_alignbit_b32 v131, v113, v109, 16\n\t"
        "ds_write_b128 %[aFc1], v[110:113] offset:8320\n\t"
        "v_rcp_f32 v189, v189\n\t"
        "v_alignbit_b32 v132, v111, v110, 16\n\t"
        "v_alignbit_b32 v133, v112, v111, 16\n\t"
        "v_rcp_f32 v190, v190\n\t"
        "buffer_load_dwordx4 a[20:23], %[vl16], %[rws], %[t0] offen offset:1024 sc1\n\t"
        "v_alignbit_b32 v134, v113, v112, 16\n\t"
        "v_alignbit_b32 v135, v114, v113, 16\n\t"
        "v_rcp_f32 v191, v191\n\t"
        "ds_write_b128 %[aFc1], v[128:131] offset:4160\n\t"
        "ds_write_b128 %[aFc1], v[132:135] offset:12480\n\t"
        "v_rcp_f32 v192, v192\n\t"
        "v_rcp_f32 v193, v193\n\t"
        "v_rcp_f32 v194, v194\n\t"
        "v_rcp_f32 v195, v195\n\t"
        "v_pk_mul_f32 v[188:189], v[188:189], v[180:181]\n\t"
        "v_pk_mul_f32 v[190:191], v[190:191], v[182:183]\n\t"
        "v_pk_mul_f32 v[192:193], v[192:193], v[184:185]\n\t"
        "v_pk_mul_f32 v[194:195], v[194:195], v[186:187]\n\t"
        "v_cvt_pk_bf16_f32 v196, v188, v189\n\t"
        "v_cvt_pk_bf16_f32 v197, v190, v191\n\t"
        "v_cvt_pk_bf16_f32 v198, v192, v193\n\t"
        "v_cvt_pk_bf16_f32 v199, v194, v195\n\t"
        "buffer_store_dwordx4 v[196:199], %[vl16], %[rws], %[hs1] offen sc1\n\t"
        "v_pk_fma_f32 v[180:181], v[172:173], %[st1], v[164:165] op_sel:[0,1,0] op_sel_hi:[1,1,1]\n\t"
        "v_pk_fma_f32 v[182:183], v[174:175], %[st1], v[166:167] op_sel:[0,1,0] op_sel_hi:[1,1,1]\n\t"
        "v_pk_fma_f32 v[184:185], v[176:177], %[st1], v[168:169] op_sel:[0,1,0] op_sel_hi:[1,1,1]\n\t"
        "v_pk_fma_f32 v[186:187], v[178:179], %[st1], v[170:171] op_sel:[0,1,0] op_sel_hi:[1,1,1]\n\t"
        "v_pk_fma_f32 v[180:181], v[24:25], %[st1], v[180:181] op_sel:[0,0,0] op_sel_hi:[1,0,1]\n\t"
        "v_pk_fma_f32 v[182:183], v[26:27], %[st1], v[182:183] op_sel:[0,0,0] op_sel_hi:[1,0,1]\n\t"
        "v_pk_fma_f32 v[184:185], v[28:29], %[st1], v[184:185] op_sel:[0,0,0] op_sel_hi:[1,0,1]\n\t"
        "v_pk_fma_f32 v[186:187], v[30:31], %[st1], v[186:187] op_sel:[0,0,0] op_sel_hi:[1,0,1]\n\t"
        "v_pk_mul_f32 v[188:189], v[180:181], v[180:181]\n\t"
        "v_pk_mul_f32 v[190:191], v[182:183], v[182:183]\n\t"
        "v_pk_mul_f32 v[192:193], v[184:185], v[184:185]\n\t"
        "v_pk_mul_f32 v[194:195], v[186:187], v[186:187]\n\t"
        "v_pk_fma_f32 v[188:189], v[188:189], %[cc], %[cc] op_sel:[0,0,1] op_sel_hi:[1,0,1]\n\t"
        "v_pk_fma_f32 v[190:191], v[190:191], %[cc], %[cc] op_sel:[0,0,1] op_sel_hi:[1,0,1]\n\t"
        "v_pk_fma_f32 v[192:193], v[192:193], %[cc], %[cc] op_sel:[0,0,1] op_sel_hi:[1,0,1]\n\t"
        "v_pk_fma_f32 v[194:195], v[194:195], %[cc], %[cc] op_sel:[0,0,1] op_sel_hi:[1,0,1]\n\t"
        "v_pk_mul_f32 v[188:189], v[188:189], v[180:181]\n\t"
        "v_pk_mul_f32 v[190:191], v[190:191], v[182:183]\n\t"
        "v_pk_mul_f32 v[192:193], v[192:193], v[184:185]\n\t"
        "v_pk_mul_f32 v[194:195], v[194:195], v[186:187]\n\t"
        "ds_read_b128 v[164:167], %[aC2] offset:0\n\t"
        "ds_read_b128 v[172:175], %[aC2] offset:384\n\t"
        "ds_read_b128 v[168:171], %[aC2] offset:32\n\t"
        "ds_read_b128 v[176:179], %[aC2] offset:416\n\t"
        "v_exp_f32 v188, v188\n\t"
        "s_waitcnt vmcnt(10)\n\t"
        "v_exp_f32 v189, v189\n\t"
        "v_readlane_b32 %[t0], %[ddF], 2\n\t"
        "v_lshlrev_b32 v128, 16, v116\n\t"
        "s_lshl_b32 %[t0], %[t0], 16\n\t"
        "v_add_f32 v128, %[t0], v128\n\t"
        "v_exp_f32 v190, v190\n\t"
        "s_add_u32 %[t0], %[w2o], 4096\n\t"
        "buffer_load_dwordx4 a[24:27], %[vl16], %[rws], %[t0] offen offset:2048 sc1\n\t"
        "v_and_b32 v129, 0xffff0000, v116\n\t"
        "s_cmp_eq_u32 %[w], 0\n\t"
        "v_cvt_pk_bf16_f32 v128, v128, v129\n\t"
        "s_cselect_b64 vcc, 1, 0\n\t"
        "v_cndmask_b32 v116, v116, v128, vcc\n\t"
        "v_exp_f32 v191, v191\n\t"
        "ds_write_b128 %[aFc2], v[116:119] offset:0\n\t"
        "v_exp_f32 v192, v192\n\t"
        "v_alignbit_b32 v128, v120, v116, 16\n\t"
        "v_exp_f32 v193, v193\n\t"
        "v_exp_f32 v194, v194\n\t"
        "v_exp_f32 v195, v195\n\t"
        "v_pk_add_f32 v[188:189], v[188:189], 1.0 op_sel_hi:[1,0]\n\t"
        "v_pk_add_f32 v[190:191], v[190:191], 1.0 op_sel_hi:[1,0]\n\t"
        "v_pk_add_f32 v[192:193], v[192:193], 1.0 op_sel_hi:[1,0]\n\t"
        "v_pk_add_f32 v[194:195], v[194:195], 1.0 op_sel_hi:[1,0]\n\t"
        "v_alignbit_b32 v129, v121, v117, 16\n\t"
        "v_alignbit_b32 v130, v122, v118, 16\n\t"
        "v_rcp_f32 v188, v188\n\t"
        "v_alignbit_b32 v131, v123, v119, 16\n\t"
        "ds_write_b128 %[aFc2], v[120:123] offset:8320\n\t"
        "v_rcp_f32 v189, v189\n\t"
        "v_alignbit_b32 v132, v121, v120, 16\n\t"
        "v_alignbit_b32 v133, v122, v121, 16\n\t"
        "v_rcp_f32 v190, v190\n\t"
        "buffer_load_dwordx4 a[28:31], %[vl16], %[rws], %[t0] offen offset:3072 sc1\n\t"
        "v_alignbit_b32 v134, v123, v122, 16\n\t"
        "v_alignbit_b32 v135, v124, v123, 16\n\t"
        "v_rcp_f32 v191, v191\n\t"
        "ds_write_b128 %[aFc2], v[128:131] offset:4160\n\t"
        "ds_write_b128 %[aFc2], v[132:135] offset:12480\n\t"
        "v_rcp_f32 v192, v192\n\t"
        "v_rcp_f32 v193, v193\n\t"
        "v_rcp_f32 v194, v194\n\t"
        "v_rcp_f32 v195, v195\n\t"
        "v_pk_mul_f32 v[188:189], v[188:189], v[180:181]\n\t"
        "v_pk_mul_f32 v[190:191], v[190:191], v[182:183]\n\t"
        "v_pk_mul_f32 v[192:193], v[192:193], v[184:185]\n\t"
        "v_pk_mul_f32 v[194:195], v[194:195], v[186:187]\n\t"
        "v_cvt_pk_bf16_f32 v196, v188, v189\n\t"
        "v_cvt_pk_bf16_f32 v197, v190, v191\n\t"
        "v_cvt_pk_bf16_f32 v198, v192, v193\n\t"
        "v_cvt_pk_bf16_f32 v199, v194, v195\n\t"
        "s_add_u32 %[t1], %[hs1], 0xc000\n\t"
        "buffer_store_dwordx4 v[196:199], %[vl16], %[rws], %[t1] offen sc1\n\t"
        "s_waitcnt lgkmcnt(4)\n\t"
        "v_pk_fma_f32 v[180:181], v[172:173], %[st0], v[164:165] op_sel:[0,1,0] op_sel_hi:[1,1,1]\n\t"
        "v_pk_fma_f32 v[182:183], v[174:175], %[st0], v[166:167] op_sel:[0,1,0] op_sel_hi:[1,1,1]\n\t"
        "v_pk_fma_f32 v[184:185], v[176:177], %[st0], v[168:169] op_sel:[0,1,0] op_sel_hi:[1,1,1]\n\t"
        "v_pk_fma_f32 v[186:187], v[178:179], %[st0], v[170:171] op_sel:[0,1,0] op_sel_hi:[1,1,1]\n\t"
        "v_pk_fma_f32 v[180:181], v[32:33], %[st0], v[180:181] op_sel:[0,0,0] op_sel_hi:[1,0,1]\n\t"
        "v_pk_fma_f32 v[182:183], v[34:35], %[st0], v[182:183] op_sel:[0,0,0] op_sel_hi:[1,0,1]\n\t"
        "v_pk_fma_f32 v[184:185], v[36:37], %[st0], v[184:185] op_sel:[0,0,0] op_sel_hi:[1,0,1]\n\t"
        "v_pk_fma_f32 v[186:187], v[38:39], %[st0], v[186:187] op_sel:[0,0,0] op_sel_hi:[1,0,1]\n\t"
        "v_pk_mul_f32 v[188:189], v[180:181], v[180:181]\n\t"
        "v_pk_mul_f32 v[190:191], v[182:183], v[182:183]\n\t"
        "v_pk_mul_f32 v[192:193], v[184:185], v[184:185]\n\t"
        "v_pk_mul_f32 v[194:195], v[186:187], v[186:187]\n\t"
        "v_pk_fma_f32 v[188:189], v[188:189], %[cc], %[cc] op_sel:[0,0,1] op_sel_hi:[1,0,1]\n\t"
        "v_pk_fma_f32 v[190:191], v[190:191], %[cc], %[cc] op_sel:[0,0,1] op_sel_hi:[1,0,1]\n\t"
        "v_pk_fma_f32 v[192:193], v[192:193], %[cc], %[cc] op_sel:[0,0,1] op_sel_hi:[1,0,1]\n\t"
        "v_pk_fma_f32 v[194:195], v[194:195], %[cc], %[cc] op_sel:[0,0,1] op_sel_hi:[1,0,1]\n\t"
        "v_pk_mul_f32 v[188:189], v[188:189], v[180:181]\n\t"
        "v_pk_mul_f32 v[190:191], v[190:191], v[182:183]\n\t"
        "v_pk_mul_f32 v[192:193], v[192:193], v[184:185]\n\t"
        "v_pk_mul_f32 v[194:195], v[194:195], v[186:187]\n\t"
        "v_exp_f32 v188, v188\n\t"
        "v_exp_f32 v189, v189\n\t"
        "v_exp_f32 v190, v190\n\t"
        "s_add_u32 %[t0], %[w2o], 8192\n\t"
        "buffer_load_dwordx4 a[32:35], %[vl16], %[rws], %[t0] offen offset:0 sc1\n\t"
        "v_exp_f32 v191, v191\n\t"
        "v_exp_f32 v192, v192\n\t"
        "v_exp_f32 v193, v193\n\t"
        "v_exp_f32 v194, v194\n\t"
        "v_exp_f32 v195, v195\n\t"
        "v_pk_add_f32 v[188:189], v[188:189], 1.0 op_sel_hi:[1,0]\n\t"
        "v_pk_add_f32 v[190:191], v[190:191], 1.0 op_sel_hi:[1,0]\n\t"
        "v_pk_add_f32 v[192:193], v[192:193], 1.0 op_sel_hi:[1,0]\n\t"
        "v_pk_add_f32 v[194:195], v[194:195], 1.0 op_sel_hi:[1,0]\n\t"
        "v_rcp_f32 v188, v188\n\t"
        "v_rcp_f32 v189, v189\n\t"
        "v_rcp_f32 v190, v190\n\t"
        "buffer_load_dwordx4 a[36:39], %[vl16], %[rws], %[t0] offen offset:1024 sc1\n\t"
        "v_rcp_f32 v191, v191\n\t"
        "v_rcp_f32 v192, v192\n\t"
        "v_rcp_f32 v193, v193\n\t"
        "v_rcp_f32 v194, v194\n\t"
        "v_rcp_f32 v195, v195\n\t"
        "v_pk_mul_f32 v[188:189], v[188:189], v[180:181]\n\t"
        "v_pk_mul_f32 v[190:191], v[190:191], v[182:183]\n\t"
        "v_pk_mul_f32 v[192:193], v[192:193], v[184:185]\n\t"
        "v_pk_mul_f32 v[194:195], v[194:195], v[186:187]\n\t"
        "v_cvt_pk_bf16_f32 v196, v188, v189\n\t"
        "v_cvt_pk_bf16_f32 v197, v190, v191\n\t"
        "v_cvt_pk_bf16_f32 v198, v192, v193\n\t"
        "v_cvt_pk_bf16_f32 v199, v194, v195\n\t"
        "buffer_store_dwordx4 v[196:199], %[vl16], %[rws], %[hs2] offen sc1\n\t"
        "v_pk_fma_f32 v[180:181], v[172:173], %[st1], v[164:165] op_sel:[0,1,0] op_sel_hi:[1,1,1]\n\t"
        "v_pk_fma_f32 v[182:183], v[174:175], %[st1], v[166:167] op_sel:[0,1,0] op_sel_hi:[1,1,1]\n\t"
        "v_pk_fma_f32 v[184:185], v[176:177], %[st1], v[168:169] op_sel:[0,1,0] op_sel_hi:[1,1,1]\n\t"
        "v_pk_fma_f32 v[186:187], v[178:179], %[st1], v[170:171] op_sel:[0,1,0] op_sel_hi:[1,1,1]\n\t"
        "v_pk_fma_f32 v[180:181], v[40:41], %[st1], v[180:181] op_sel:[0,0,0] op_sel_hi:[1,0,1]\n\t"
        "v_pk_fma_f32 v[182:183], v[42:43], %[st1], v[182:183] op_sel:[0,0,0] op_sel_hi:[1,0,1]\n\t"
        "v_pk_fma_f32 v[184:185], v[44:45], %[st1], v[184:185] op_sel:[0,0,0] op_sel_hi:[1,0,1]\n\t"
        "v_pk_fma_f32 v[186:187], v[46:47], %[st1], v[186:187] op_sel:[0,0,0] op_sel_hi:[1,0,1]\n\t"
        "v_pk_mul_f32 v[188:189], v[180:181], v[180:181]\n\t"
        "v_pk_mul_f32 v[190:191], v[182:183], v[182:183]\n\t"
        "v_pk_mul_f32 v[192:193], v[184:185], v[184:185]\n\t"
        "v_pk_mul_f32 v[194:195], v[186:187], v[186:187]\n\t"
        "v_pk_fma_f32 v[188:189], v[188:189], %[cc], %[cc] op_sel:[0,0,1] op_sel_hi:[1,0,1]\n\t"
        "v_pk_fma_f32 v[190:191], v[190:191], %[cc], %[cc] op_sel:[0,0,1] op_sel_hi:[1,0,1]\n\t"
        "v_pk_fma_f32 v[192:193], v[192:193], %[cc], %[cc] op_sel:[0,0,1] op_sel_hi:[1,0,1]\n\t"
        "v_pk_fma_f32 v[194:195], v[194:195], %[cc], %[cc] op_sel:[0,0,1] op_sel_hi:[1,0,1]\n\t"
        "v_pk_mul_f32 v[188:189], v[188:189], v[180:181]\n\t"
        "v_pk_mul_f32 v[190:191], v[190:191], v[182:183]\n\t"
        "v_pk_mul_f32 v[192:193], v[192:193], v[184:185]\n\t"
        "v_pk_mul_f32 v[194:195], v[194:195], v[186:187]\n\t"
        "v_exp_f32 v188, v188\n\t"
        "v_exp_f32 v189, v189\n\t"
        "v_exp_f32 v190, v190\n\t"
        "buffer_load_dwordx4 a[40:43], %[vl16], %[rws], %[t0] offen offset:2048 sc1\n\t"
        "v_exp_f32 v191, v191\n\t"
        "v_exp_f32 v192, v192\n\t"
        "v_exp_f32 v193, v193\n\t"
        "v_exp_f32 v194, v194\n\t"
        "v_exp_f32 v195, v195\n\t"
        "v_pk_add_f32 v[188:189], v[188:189], 1.0 op_sel_hi:[1,0]\n\t"
        "v_pk_add_f32 v[190:191], v[190:191], 1.0 op_sel_hi:[1,0]\n\t"
        "v_pk_add_f32 v[192:193], v[192:193], 1.0 op_sel_hi:[1,0]\n\t"
        "v_pk_add_f32 v[194:195], v[194:195], 1.0 op_sel_hi:[1,0]\n\t"
        "v_rcp_f32 v188, v188\n\t"
        "v_rcp_f32 v189, v189\n\t"
        "v_rcp_f32 v190, v190\n\t"
        "buffer_load_dwordx4 a[44:47], %[vl16], %[rws], %[t0] offen offset:3072 sc1\n\t"
        "v_rcp_f32 v191, v191\n\t"
        "v_rcp_f32 v192, v192\n\t"
        "v_rcp_f32 v193, v193\n\t"
        "v_rcp_f32 v194, v194\n\t"
        "v_rcp_f32 v195, v195\n\t"
        "v_pk_mul_f32 v[188:189], v[188:189], v[180:181]\n\t"
        "v_pk_mul_f32 v[190:191], v[190:191], v[182:183]\n\t"
        "v_pk_mul_f32 v[192:193], v[192:193], v[184:185]\n\t"
        "v_pk_mul_f32 v[194:195], v[194:195], v[186:187]\n\t"
        "v_cvt_pk_bf16_f32 v196, v188, v189\n\t"
        "v_cvt_pk_bf16_f32 v197, v190, v191\n\t"
        "v_cvt_pk_bf16_f32 v198, v192, v193\n\t"
        "v_cvt_pk_bf16_f32 v199, v194, v195\n\t"
        "s_add_u32 %[t1], %[hs2], 0xc000\n\t"
        "buffer_store_dwordx4 v[196:199], %[vl16], %[rws], %[t1] offen sc1\n\t"
        "s_waitcnt lgkmcnt(0)\n\t"
        "s_barrier\n\t"
        "ds_read_b128 v[152:155], %[pbF] offset:4032\n\t"
        "ds_read_b64_tr_b16 v[96:97], %[paF] offset:0\n\t"
        "ds_read_b64_tr_b16 v[98:99], %[paF] offset:8\n\t"
        "ds_read_b128 v[148:151], %[pbF] offset:3520\n\t"
        "ds_read_b128 v[144:147], %[pbF] offset:3008\n\t"
        "ds_read_b128 v[140:143], %[pbF] offset:2496\n\t"
        "ds_read_b128 v[136:139], %[pbF] offset:1984\n\t"
        "ds_read_b128 v[132:135], %[pbF] offset:1472\n\t"
        "ds_read_b128 v[128:131], %[pbF] offset:960\n\t"
        "ds_read_b128 v[156:159], %[pbF] offset:448\n\t"
        "ds_read_b64_tr_b16 v[100:101], %[paF] offset:512\n\t"
        "ds_read_b64_tr_b16 v[102:103], %[paF] offset:520\n\t"
        "ds_read_b64_tr_b16 v[104:105], %[paF] offset:1024\n\t"
        "ds_read_b64_tr_b16 v[106:107], %[paF] offset:1032\n\t"
        "ds_read_b64_tr_b16 v[108:109], %[paF] offset:1536\n\t"
        "ds_read_b64_tr_b16 v[110:111], %[paF] offset:1544\n\t"
        "ds_read_b64_tr_b16 v[112:113], %[paF] offset:2048\n\t"
        "ds_read_b64_tr_b16 v[114:115], %[paF] offset:2056\n\t"
        "ds_read_b64_tr_b16 v[116:117], %[paF] offset:2560\n\t"
        "ds_read_b64_tr_b16 v[118:119], %[paF] offset:2568\n\t"
        "ds_read_b64_tr_b16 v[120:121], %[paF] offset:3072\n\t"
        "ds_read_b64_tr_b16 v[122:123], %[paF] offset:3080\n\t"
        "ds_read_b64_tr_b16 v[124:125], %[paF] offset:3584\n\t"
        "ds_read_b64_tr_b16 v[126:127], %[paF] offset:3592\n\t"
        "ds_read_b128 v[160:163], %[pbF] offset:384\n\t"
        "ds_read_b64 a[246:247], %[aQ0] offset:61440\n\t"
        "ds_read_b64 a[248:249], %[aQ1] offset:61440\n\t"
        "ds_read_b64 a[250:251], %[aQ2] offset:61440\n\t"
        "ds_read_b64 a[252:253], %[aQ3] offset:61440\n\t"
        "ds_read_b64 a[254:255], %[aQ4] offset:61440\n\t"
        "s_waitcnt lgkmcnt(15)\n\t"
        "v_mfma_f32_16x16x32_bf16 v[76:79], v[96:99], v[152:155], 0\n\t"
        "ds_read_b128 v[152:155], %[pbF] offset:3968\n\t"
        "v_mfma_f32_16x16x32_bf16 v[72:75], v[96:99], v[148:151], 0\n\t"
        "v_mfma_f32_16x16x32_bf16 v[68:71], v[96:99], v[144:147], 0\n\t"
        "s_add_u32 %[t0], %[w2o], 12288\n\t"
        "buffer_load_dwordx4 a[48:51], %[vl16], %[rws], %[t0] offen offset:0 sc1\n\t"
        "v_mfma_f32_16x16x32_bf16 v[64:67], v[96:99], v[140:143], 0\n\t"
        "v_mfma_f32_16x16x32_bf16 v[60:63], v[96:99], v[136:139], 0\n\t"
        "v_mfma_f32_16x16x32_bf16 v[56:59], v[96:99], v[132:135], 0\n\t"
        "buffer_load_dwordx4 a[52:55], %[vl16], %[rws], %[t0] offen offset:1024 sc1\n\t"
        "v_mfma_f32_16x16x32_bf16 v[52:55], v[96:99], v[128:131], 0\n\t"
        "v_mfma_f32_16x16x32_bf16 v[48:51], v[96:99], v[156:159], 0\n\t"
        "ds_read_b64_tr_b16 v[96:97], %[paF] offset:64\n\t"
        "v_mfma_f32_16x16x32_bf16 v[76:79], v[100:103], v[148:151], v[76:79]\n\t"
        "ds_read_b64_tr_b16 v[98:99], %[paF] offset:72\n\t"
        "v_mfma_f32_16x16x32_bf16 v[72:75], v[100:103], v[144:147], v[72:75]\n\t"
        "ds_read_b128 v[148:151], %[pbF] offset:3456\n\t"
        "buffer_load_dwordx4 a[56:59], %[vl16], %[rws], %[t0] offen offset:2048 sc1\n\t"
        "v_mfma_f32_16x16x32_bf16 v[68:71], v[100:103], v[140:143], v[68:71]\n\t"
        "v_mfma_f32_16x16x32_bf16 v[64:67], v[100:103], v[136:139], v[64:67]\n\t"
        "v_mfma_f32_16x16x32_bf16 v[60:63], v[100:103], v[132:135], v[60:63]\n\t"
        "buffer_load_dwordx4 a[60:63], %[vl16], %[rws], %[t0] offen offset:3072 sc1\n\t"
        "v_mfma_f32_16x16x32_bf16 v[56:59], v[100:103], v[128:131], v[56:59]\n\t"
        "v_mfma_f32_16x16x32_bf16 v[52:55], v[100:103], v[156:159], v[52:55]\n\t"
        "ds_read_b64_tr_b16 v[100:101], %[paF] offset:576\n\t"
        "v_mfma_f32_16x16x32_bf16 v[76:79], v[104:107], v[144:147], v[76:79]\n\t"
        "ds_read_b128 v[144:147], %[pbF] offset:2944\n\t"
        "s_add_u32 %[t0], %[w2o], 16384\n\t"
        "buffer_load_dwordx4 a[64:67], %[vl16], %[rws], %[t0] offen offset:0 sc1\n\t"
        "v_mfma_f32_16x16x32_bf16 v[72:75], v[104:107], v[140:143], v[72:75]\n\t"
        "ds_read_b64_tr_b16 v[102:103], %[paF] offset:584\n\t"
        "v_mfma_f32_16x16x32_bf16 v[68:71], v[104:107], v[136:139], v[68:71]\n\t"
        "v_mfma_f32_16x16x32_bf16 v[64:67], v[104:107], v[132:135], v[64:67]\n\t"
        "v_mfma_f32_16x16x32_bf16 v[60:63], v[104:107], v[128:131], v[60:63]\n\t"
        "buffer_load_dwordx4 a[68:71], %[vl16], %[rws], %[t0] offen offset:1024 sc1\n\t"
        "v_mfma_f32_16x16x32_bf16 v[56:59], v[104:107], v[156:159], v[56:59]\n\t"
        "ds_read_b64_tr_b16 v[104:105], %[paF] offset:1088\n\t"
        "s_waitcnt lgkmcnt(15)\n\t"
        "v_mfma_f32_16x16x32_bf16 v[76:79], v[108:111], v[140:143], v[76:79]\n\t"
        "ds_read_b128 v[140:143], %[pbF] offset:2432\n\t"
        "v_mfma_f32_16x16x32_bf16 v[72:75], v[108:111], v[136:139], v[72:75]\n\t"
        "ds_read_b64_tr_b16 v[106:107], %[paF] offset:1096\n\t"
        "buffer_load_dwordx4 a[72:75], %[vl16], %[rws], %[t0] offen offset:2048 sc1\n\t"
        "v_mfma_f32_16x16x32_bf16 v[68:71], v[108:111], v[132:135], v[68:71]\n\t"
        "v_mfma_f32_16x16x32_bf16 v[64:67], v[108:111], v[128:131], v[64:67]\n\t"
        "s_waitcnt vmcnt(7)\n\t"
        "s_barrier\n\t"
        "buffer_store_dwordx2 %[evv], %[vfH], %[rws], 0 offen sc1\n\t"
        "v_mfma_f32_16x16x32_bf16 v[60:63], v[108:111], v[156:159], v[60:63]\n\t"
        "ds_read_b64_tr_b16 v[108:109], %[paF] offset:1600\n\t"
        "buffer_load_dwordx4 a[76:79], %[vl16], %[rws], %[t0] offen offset:3072 sc1\n\t"
        "v_mfma_f32_16x16x32_bf16 v[76:79], v[112:115], v[136:139], v[76:79]\n\t"
        "ds_read_b128 v[136:139], %[pbF] offset:1920\n\t"
        "v_mfma_f32_16x16x32_bf16 v[72:75], v[112:115], v[132:135], v[72:75]\n\t"
        "ds_read_b64_tr_b16 v[110:111], %[paF] offset:1608\n\t"
        "v_mfma_f32_16x16x32_bf16 v[68:71], v[112:115], v[128:131], v[68:71]\n\t"
        "v_mfma_f32_16x16x32_bf16 v[64:67], v[112:115], v[156:159], v[64:67]\n\t"
        "ds_read_b64_tr_b16 v[112:113], %[paF] offset:2112\n\t"
        "s_add_u32 %[t0], %[w2o], 20480\n\t"
        "buffer_load_dwordx4 a[80:83], %[vl16], %[rws], %[t0] offen offset:0 sc1\n\t"
        "v_mfma_f32_16x16x32_bf16 v[76:79], v[116:119], v[132:135], v[76:79]\n\t"
        "ds_read_b128 v[132:135], %[pbF] offset:1408\n\t"
        "v_mfma_f32_16x16x32_bf16 v[72:75], v[116:119], v[128:131], v[72:75]\n\t"
        "ds_read_b64_tr_b16 v[114:115], %[paF] offset:2120\n\t"
        "v_mfma_f32_16x16x32_bf16 v[68:71], v[116:119], v[156:159], v[68:71]\n\t"
        "ds_read_b64_tr_b16 v[116:117], %[paF] offset:2624\n\t"
        "buffer_load_dwordx4 a[84:87], %[vl16], %[rws], %[t0] offen offset:1024 sc1\n\t"
        "v_mfma_f32_16x16x32_bf16 v[76:79], v[120:123], v[128:131], v[76:79]\n\t"
        "ds_read_b128 v[128:131], %[pbF] offset:896\n\t"
        "v_mfma_f32_16x16x32_bf16 v[72:75], v[120:123], v[156:159], v[72:75]\n\t"
        "ds_read_b64_tr_b16 v[118:119], %[paF] offset:2632\n\t"
        "s_waitcnt lgkmcnt(15)\n\t"
        "v_mfma_f32_16x16x32_bf16 v[76:79], v[124:127], v[156:159], v[76:79]\n\t"
        "ds_read_b64_tr_b16 v[120:121], %[paF] offset:3136\n\t"
        "buffer_load_dwordx4 a[88:91], %[vl16], %[rws], %[t0] offen offset:2048 sc1\n\t"
        "v_mfma_f32_16x16x32_bf16 v[76:79], v[96:99], v[152:155], v[76:79]\n\t"
        "ds_read_b64_tr_b16 v[122:123], %[paF] offset:3144\n\t"
        "v_mfma_f32_16x16x32_bf16 v[72:75], v[96:99], v[148:151], v[72:75]\n\t"
        "ds_read_b64_tr_b16 v[124:125], %[paF] offset:3648\n\t"
        "s_waitcnt lgkmcnt(15)\n\t"
        "v_mfma_f32_16x16x32_bf16 v[68:71], v[96:99], v[144:147], v[68:71]\n\t"
        "ds_read_b64_tr_b16 v[126:127], %[paF] offset:3656\n\t"
        "s_waitcnt lgkmcnt(14)\n\t"
        "v_mfma_f32_16x16x32_bf16 v[64:67], v[96:99], v[140:143], v[64:67]\n\t"
        "ds_read_b128 v[152:155], %[pbF] offset:3904\n\t"
        "buffer_load_dwordx4 a[92:95], %[vl16], %[rws], %[t0] offen offset:3072 sc1\n\t"
        "s_waitcnt lgkmcnt(12)\n\t"
        "v_mfma_f32_16x16x32_bf16 v[60:63], v[96:99], v[136:139], v[60:63]\n\t"
        "ds_read_b128 v[156:159], %[pbF] offset:320\n\t"
        "s_waitcnt lgkmcnt(10)\n\t"
        "v_mfma_f32_16x16x32_bf16 v[56:59], v[96:99], v[132:135], v[56:59]\n\t"
        "s_waitcnt lgkmcnt(7)\n\t"
        "v_mfma_f32_16x16x32_bf16 v[52:55], v[96:99], v[128:131], v[52:55]\n\t"
        "s_add_u32 %[t0], %[w2o], 24576\n\t"
        "buffer_load_dwordx4 a[96:99], %[vl16], %[rws], %[t0] offen offset:0 sc1\n\t"
        "v_mfma_f32_16x16x32_bf16 v[48:51], v[96:99], v[160:163], v[48:51]\n\t"
        "ds_read_b64_tr_b16 v[96:97], %[paF] offset:128\n\t"
        "v_mfma_f32_16x16x32_bf16 v[76:79], v[100:103], v[148:151], v[76:79]\n\t"
        "ds_read_b64_tr_b16 v[98:99], %[paF] offset:136\n\t"
        "v_mfma_f32_16x16x32_bf16 v[72:75], v[100:103], v[144:147], v[72:75]\n\t"
        "ds_read_b128 v[148:151], %[pbF] offset:3392\n\t"
        "buffer_load_dwordx4 a[100:103], %[vl16], %[rws], %[t0] offen offset:1024 sc1\n\t"
        "v_mfma_f32_16x16x32_bf16 v[68:71], v[100:103], v[140:143], v[68:71]\n\t"
        "v_mfma_f32_16x16x32_bf16 v[64:67], v[100:103], v[136:139], v[64:67]\n\t"
        "v_mfma_f32_16x16x32_bf16 v[60:63], v[100:103], v[132:135], v[60:63]\n\t"
        "v_mfma_f32_16x16x32_bf16 v[56:59], v[100:103], v[128:131], v[56:59]\n\t"
        "buffer_load_dwordx4 a[104:107], %[vl16], %[rws], %[t0] offen offset:2048 sc1\n\t"
        "v_mfma_f32_16x16x32_bf16 v[52:55], v[100:103], v[160:163], v[52:55]\n\t"
        "ds_read_b64_tr_b16 v[100:101], %[paF] offset:640\n\t"
        "v_mfma_f32_16x16x32_bf16 v[76:79], v[104:107], v[144:147], v[76:79]\n\t"
        "ds_read_b128 v[144:147], %[pbF] offset:2880\n\t"
        "v_mfma_f32_16x16x32_bf16 v[72:75], v[104:107], v[140:143], v[72:75]\n\t"
        "ds_read_b64_tr_b16 v[102:103], %[paF] offset:648\n\t"
        "buffer_load_dwordx4 a[108:111], %[vl16], %[rws], %[t0] offen offset:3072 sc1\n\t"
        "v_mfma_f32_16x16x32_bf16 v[68:71], v[104:107], v[136:139], v[68:71]\n\t"
        "v_mfma_f32_16x16x32_bf16 v[64:67], v[104:107], v[132:135], v[64:67]\n\t"
        "v_mfma_f32_16x16x32_bf16 v[60:63], v[104:107], v[128:131], v[60:63]\n\t"
        "s_add_u32 %[t0], %[w2o], 28672\n\t"
        "buffer_load_dwordx4 a[112:115], %[vl16], %[rws], %[t0] offen offset:0 sc1\n\t"
        "v_mfma_f32_16x16x32_bf16 v[56:59], v[104:107], v[160:163], v[56:59]\n\t"
        "ds_read_b64_tr_b16 v[104:105], %[paF] offset:1152\n\t"
        "v_mfma_f32_16x16x32_bf16 v[76:79], v[108:111], v[140:143], v[76:79]\n\t"
        "ds_read_b128 v[140:143], %[pbF] offset:2368\n\t"
        "v_mfma_f32_16x16x32_bf16 v[72:75], v[108:111], v[136:139], v[72:75]\n\t"
        "ds_read_b64_tr_b16 v[106:107], %[paF] offset:1160\n\t"
        "v_mfma_f32_16x16x32_bf16 v[68:71], v[108:111], v[132:135], v[68:71]\n\t"
        "buffer_load_dwordx4 a[116:119], %[vl16], %[rws], %[t0] offen offset:1024 sc1\n\t"
        "v_mfma_f32_16x16x32_bf16 v[64:67], v[108:111], v[128:131], v[64:67]\n\t"
        "v_mfma_f32_16x16x32_bf16 v[60:63], v[108:111], v[160:163], v[60:63]\n\t"
        "ds_read_b64_tr_b16 v[108:109], %[paF] offset:1664\n\t"
        "v_mfma_f32_16x16x32_bf16 v[76:79], v[112:115], v[136:139], v[76:79]\n\t"
        "ds_read_b128 v[136:139], %[pbF] offset:1856\n\t"
        "buffer_load_dwordx4 a[120:123], %[vl16], %[rws], %[t0] offen offset:2048 sc1\n\t"
        "v_mfma_f32_16x16x32_bf16 v[72:75], v[112:115], v[132:135], v[72:75]\n\t"
        "ds_read_b64_tr_b16 v[110:111], %[paF] offset:1672\n\t"
        "v_mfma_f32_16x16x32_bf16 v[68:71], v[112:115], v[128:131], v[68:71]\n\t"
        "v_mfma_f32_16x16x32_bf16 v[64:67], v[112:115], v[160:163], v[64:67]\n\t"
        "ds_read_b64_tr_b16 v[112:113], %[paF] offset:2176\n\t"
        "buffer_load_dwordx4 a[124:127], %[vl16], %[rws], %[t0] offen offset:3072 sc1\n\t"
        "s_waitcnt lgkmcnt(15)\n\t"
        "v_mfma_f32_16x16x32_bf16 v[76:79], v[116:119], v[132:135], v[76:79]\n\t"
        "ds_read_b128 v[132:135], %[pbF] offset:1344\n\t"
        "v_mfma_f32_16x16x32_bf16 v[72:75], v[116:119], v[128:131], v[72:75]\n\t"
        "ds_read_b64_tr_b16 v[114:115], %[paF] offset:2184\n\t"
        "v_mfma_f32_16x16x32_bf16 v[68:71], v[116:119], v[160:163], v[68:71]\n\t"
        "ds_read_b64_tr_b16 v[116:117], %[paF] offset:2688\n\t"
        "v_mfma_f32_16x16x32_bf16 v[76:79], v[120:123], v[128:131], v[76:79]\n\t"
        "ds_read_b128 v[128:131], %[pbF] offset:832\n\t"
        "s_add_u32 %[t0], %[w2o], 32768\n\t"
        "buffer_load_dwordx4 a[128:131], %[vl16], %[rws], %[t0] offen offset:0 sc1\n\t"
        "v_mfma_f32_16x16x32_bf16 v[72:75], v[120:123], v[160:163], v[72:75]\n\t"
        "ds_read_b64_tr_b16 v[118:119], %[paF] offset:2696\n\t"
        "v_mfma_f32_16x16x32_bf16 v[76:79], v[124:127], v[160:163], v[76:79]\n\t"
        "ds_read_b64_tr_b16 v[120:121], %[paF] offset:3200\n\t"
        "s_waitcnt lgkmcnt(15)\n\t"
        "v_mfma_f32_16x16x32_bf16 v[76:79], v[96:99], v[152:155], v[76:79]\n\t"
        "ds_read_b64_tr_b16 v[122:123], %[paF] offset:3208\n\t"
        "buffer_load_dwordx4 a[132:135], %[vl16], %[rws], %[t0] offen offset:1024 sc1\n\t"
        "v_mfma_f32_16x16x32_bf16 v[72:75], v[96:99], v[148:151], v[72:75]\n\t"
        "ds_read_b64_tr_b16 v[124:125], %[paF] offset:3712\n\t"
        "s_waitcnt lgkmcnt(15)\n\t"
        "v_mfma_f32_16x16x32_bf16 v[68:71], v[96:99], v[144:147], v[68:71]\n\t"
        "ds_read_b64_tr_b16 v[126:127], %[paF] offset:3720\n\t"
        "s_waitcnt lgkmcnt(14)\n\t"
        "v_mfma_f32_16x16x32_bf16 v[64:67], v[96:99], v[140:143], v[64:67]\n\t"
        "ds_read_b128 v[152:155], %[pbH] offset:4032\n\t"
        "buffer_load_dwordx4 a[136:139], %[vl16], %[rws], %[t0] offen offset:2048 sc1\n\t"
        "s_waitcnt lgkmcnt(12)\n\t"
        "v_mfma_f32_16x16x32_bf16 v[60:63], v[96:99], v[136:139], v[60:63]\n\t"
        "ds_read_b128 v[160:163], %[pbH] offset:448\n\t"
        "s_waitcnt lgkmcnt(10)\n\t"
        "v_mfma_f32_16x16x32_bf16 v[56:59], v[96:99], v[132:135], v[56:59]\n\t"
        "s_waitcnt lgkmcnt(7)\n\t"
        "v_mfma_f32_16x16x32_bf16 v[52:55], v[96:99], v[128:131], v[52:55]\n\t"
        "v_mfma_f32_16x16x32_bf16 v[48:51], v[96:99], v[156:159], v[48:51]\n\t"
        "ds_read_b64_tr_b16 v[96:97], %[paH] offset:0\n\t"
        "buffer_load_dwordx4 a[140:143], %[vl16], %[rws], %[t0] offen offset:3072 sc1\n\t"
        "v_mfma_f32_16x16x32_bf16 v[76:79], v[100:103], v[148:151], v[76:79]\n\t"
        "ds_read_b64_tr_b16 v[98:99], %[paH] offset:8\n\t"
        "v_mfma_f32_16x16x32_bf16 v[72:75], v[100:103], v[144:147], v[72:75]\n\t"
        "ds_read_b128 v[148:151], %[pbH] offset:3520\n\t"
        "v_mfma_f32_16x16x32_bf16 v[68:71], v[100:103], v[140:143], v[68:71]\n\t"
        "s_add_u32 %[t0], %[w2o], 36864\n\t"
        "buffer_load_dwordx4 a[144:147], %[vl16], %[rws], %[t0] offen offset:0 sc1\n\t"
        "v_mfma_f32_16x16x32_bf16 v[64:67], v[100:103], v[136:139], v[64:67]\n\t"
        "v_mfma_f32_16x16x32_bf16 v[60:63], v[100:103], v[132:135], v[60:63]\n\t"
        "v_mfma_f32_16x16x32_bf16 v[56:59], v[100:103], v[128:131], v[56:59]\n\t"
        "buffer_load_dwordx4 a[148:151], %[vl16], %[rws], %[t0] offen offset:1024 sc1\n\t"
        "v_mfma_f32_16x16x32_bf16 v[52:55], v[100:103], v[156:159], v[52:55]\n\t"
        "ds_read_b64_tr_b16 v[100:101], %[paH] offset:512\n\t"
        "v_mfma_f32_16x16x32_bf16 v[76:79], v[104:107], v[144:147], v[76:79]\n\t"
        "ds_read_b128 v[144:147], %[pbH] offset:3008\n\t"
        "v_mfma_f32_16x16x32_bf16 v[72:75], v[104:107], v[140:143], v[72:75]\n\t"
        "ds_read_b64_tr_b16 v[102:103], %[paH] offset:520\n\t"
        "v_mfma_f32_16x16x32_bf16 v[68:71], v[104:107], v[136:139], v[68:71]\n\t"
        "buffer_load_dwordx4 a[152:155], %[vl16], %[rws], %[t0] offen offset:2048 sc1\n\t"
        "v_mfma_f32_16x16x32_bf16 v[64:67], v[104:107], v[132:135], v[64:67]\n\t"
        "v_mfma_f32_16x16x32_bf16 v[60:63], v[104:107], v[128:131], v[60:63]\n\t"
        "v_mfma_f32_16x16x32_bf16 v[56:59], v[104:107], v[156:159], v[56:59]\n\t"
        "ds_read_b64_tr_b16 v[104:105], %[paH] offset:1024\n\t"
        "buffer_load_dwordx4 a[156:159], %[vl16], %[rws], %[t0] offen offset:3072 sc1\n\t"
        "v_mfma_f32_16x16x32_bf16 v[76:79], v[108:111], v[140:143], v[76:79]\n\t"
        "ds_read_b128 v[140:143], %[pbH] offset:2496\n\t"
        "v_mfma_f32_16x16x32_bf16 v[72:75], v[108:111], v[136:139], v[72:75]\n\t"
        "ds_read_b64_tr_b16 v[106:107], %[paH] offset:1032\n\t"
        "v_mfma_f32_16x16x32_bf16 v[68:71], v[108:111], v[132:135], v[68:71]\n\t"
        "s_add_u32 %[t0], %[w2o], 40960\n\t"
        "buffer_load_dwordx4 a[160:163], %[vl16], %[rws], %[t0] offen offset:0 sc1\n\t"
        "v_mfma_f32_16x16x32_bf16 v[64:67], v[108:111], v[128:131], v[64:67]\n\t"
        "v_mfma_f32_16x16x32_bf16 v[60:63], v[108:111], v[156:159], v[60:63]\n\t"
        "ds_read_b64_tr_b16 v[108:109], %[paH] offset:1536\n\t"
        "v_mfma_f32_16x16x32_bf16 v[76:79], v[112:115], v[136:139], v[76:79]\n\t"
        "ds_read_b128 v[136:139], %[pbH] offset:1984\n\t"
        "v_mfma_f32_16x16x32_bf16 v[72:75], v[112:115], v[132:135], v[72:75]\n\t"
        "ds_read_b64_tr_b16 v[110:111], %[paH] offset:1544\n\t"
        "buffer_load_dwordx4 a[164:167], %[vl16], %[rws], %[t0] offen offset:1024 sc1\n\t"
        "v_mfma_f32_16x16x32_bf16 v[68:71], v[112:115], v[128:131], v[68:71]\n\t"
        "v_mfma_f32_16x16x32_bf16 v[64:67], v[112:115], v[156:159], v[64:67]\n\t"
        "ds_read_b64_tr_b16 v[112:113], %[paH] offset:2048\n\t"
        "s_waitcnt lgkmcnt(15)\n\t"
        "v_mfma_f32_16x16x32_bf16 v[76:79], v[116:119], v[132:135], v[76:79]\n\t"
        "ds_read_b128 v[132:135], %[pbH] offset:1472\n\t"
        "buffer_load_dwordx4 a[168:171], %[vl16], %[rws], %[t0] offen offset:2048 sc1\n\t"
        "v_mfma_f32_16x16x32_bf16 v[72:75], v[116:119], v[128:131], v[72:75]\n\t"
        "ds_read_b64_tr_b16 v[114:115], %[paH] offset:2056\n\t"
        "v_mfma_f32_16x16x32_bf16 v[68:71], v[116:119], v[156:159], v[68:71]\n\t"
        "ds_read_b64_tr_b16 v[116:117], %[paH] offset:2560\n\t"
        "v_mfma_f32_16x16x32_bf16 v[76:79], v[120:123], v[128:131], v[76:79]\n\t"
        "ds_read_b128 v[128:131], %[pbH] offset:960\n\t"
        "buffer_load_dwordx4 a[172:175], %[vl16], %[rws], %[t0] offen offset:3072 sc1\n\t"
        "v_mfma_f32_16x16x32_bf16 v[72:75], v[120:123], v[156:159], v[72:75]\n\t"
        "ds_read_b64_tr_b16 v[118:119], %[paH] offset:2568\n\t"
        "v_mfma_f32_16x16x32_bf16 v[76:79], v[124:127], v[156:159], v[76:79]\n\t"
        "ds_read_b64_tr_b16 v[120:121], %[paH] offset:3072\n\t"
        "s_waitcnt lgkmcnt(15)\n\t"
        "v_mfma_f32_16x16x32_bf16 v[92:95], v[96:99], v[152:155], 0\n\t"
        "ds_read_b64_tr_b16 v[122:123], %[paH] offset:3080\n\t"
        "s_waitcnt lgkmcnt(15)\n\t"
        "v_mfma_f32_16x16x32_bf16 v[88:91], v[96:99], v[144:147], 0\n\t"
        "ds_read_b64_tr_b16 v[124:125], %[paH] offset:3584\n\t"
        "s_add_u32 %[t0], %[w2o], 45056\n\t"
        "buffer_load_dwordx4 a[176:179], %[vl16], %[rws], %[t0] offen offset:0 sc1\n\t"
        "s_waitcnt lgkmcnt(7)\n\t"
        "v_mfma_f32_16x16x32_bf16 v[84:87], v[96:99], v[132:135], 0\n\t"
        "ds_read_b64_tr_b16 v[126:127], %[paH] offset:3592\n\t"
        "v_mfma_f32_16x16x32_bf16 v[80:83], v[96:99], v[160:163], 0\n\t"
        "ds_read_b128 v[152:155], %[pbH] offset:3968\n\t"
        "v_mfma_f32_16x16x32_bf16 v[92:95], v[100:103], v[148:151], v[92:95]\n\t"
        "ds_read_b64_tr_b16 v[96:97], %[paH] offset:64\n\t"
        "buffer_load_dwordx4 a[180:183], %[vl16], %[rws], %[t0] offen offset:1024 sc1\n\t"
        "v_mfma_f32_16x16x32_bf16 v[88:91], v[100:103], v[140:143], v[88:91]\n\t"
        "ds_read_b64_tr_b16 v[98:99], %[paH] offset:72\n\t"
        "s_waitcnt lgkmcnt(8)\n\t"
        "v_mfma_f32_16x16x32_bf16 v[84:87], v[100:103], v[128:131], v[84:87]\n\t"
        "ds_read_b128 v[156:159], %[pbH] offset:384\n\t"
        "v_mfma_f32_16x16x32_bf16 v[92:95], v[104:107], v[144:147], v[92:95]\n\t"
        "ds_read_b128 v[144:147], %[pbH] offset:2944\n\t"
        "buffer_load_dwordx4 a[184:187], %[vl16], %[rws], %[t0] offen offset:2048 sc1\n\t"
        "v_mfma_f32_16x16x32_bf16 v[88:91], v[104:107], v[136:139], v[88:91]\n\t"
        "ds_read_b128 v[148:151], %[pbH] offset:3456\n\t"
        "v_mfma_f32_16x16x32_bf16 v[84:87], v[104:107], v[160:163], v[84:87]\n\t"
        "ds_read_b64_tr_b16 v[100:101], %[paH] offset:576\n\t"
        "v_mfma_f32_16x16x32_bf16 v[92:95], v[108:111], v[140:143], v[92:95]\n\t"
        "ds_read_b64_tr_b16 v[102:103], %[paH] offset:584\n\t"
        "v_mfma_f32_16x16x32_bf16 v[88:91], v[108:111], v[132:135], v[88:91]\n\t"
        "ds_read_b128 v[140:143], %[pbH] offset:2432\n\t"
        "buffer_load_dwordx4 a[188:191], %[vl16], %[rws], %[t0] offen offset:3072 sc1\n\t"
        "v_mfma_f32_16x16x32_bf16 v[92:95], v[112:115], v[136:139], v[92:95]\n\t"
        "ds_read_b64_tr_b16 v[104:105], %[paH] offset:1088\n\t"
        "v_mfma_f32_16x16x32_bf16 v[88:91], v[112:115], v[128:131], v[88:91]\n\t"
        "ds_read_b64_tr_b16 v[106:107], %[paH] offset:1096\n\t"
        "s_waitcnt lgkmcnt(15)\n\t"
        "v_mfma_f32_16x16x32_bf16 v[92:95], v[116:119], v[132:135], v[92:95]\n\t"
        "ds_read_b128 v[132:135], %[pbH] offset:1408\n\t"
        "s_add_u32 %[t0], %[w2o], 49152\n\t"
        "buffer_load_dwordx4 a[192:195], %[vl16], %[rws], %[t0] offen offset:0 sc1\n\t"
        "v_mfma_f32_16x16x32_bf16 v[88:91], v[116:119], v[160:163], v[88:91]\n\t"
        "ds_read_b128 v[136:139], %[pbH] offset:1920\n\t"
        "s_waitcnt lgkmcnt(15)\n\t"
        "v_mfma_f32_16x16x32_bf16 v[92:95], v[120:123], v[128:131], v[92:95]\n\t"
        "ds_read_b128 v[128:131], %[pbH] offset:896\n\t"
        "buffer_load_dwordx2 v[204:205], %[pollh], %[rws], 0 offen sc1\n\t"
        "s_waitcnt lgkmcnt(14)\n\t"
        "v_mfma_f32_16x16x32_bf16 v[92:95], v[124:127], v[160:163], v[92:95]\n\t"
        "ds_read_b64_tr_b16 v[108:109], %[paH] offset:1600\n\t"
        "buffer_load_dwordx4 a[196:199], %[vl16], %[rws], %[t0] offen offset:1024 sc1\n\t"
        "s_waitcnt lgkmcnt(12)\n\t"
        "v_mfma_f32_16x16x32_bf16 v[92:95], v[96:99], v[152:155], v[92:95]\n\t"
        "ds_read_b64_tr_b16 v[110:111], %[paH] offset:1608\n\t"
        "s_waitcnt lgkmcnt(11)\n\t"
        "v_mfma_f32_16x16x32_bf16 v[88:91], v[96:99], v[144:147], v[88:91]\n\t"
        "ds_read_b64_tr_b16 v[112:113], %[paH] offset:2112\n\t"
        "s_waitcnt lgkmcnt(5)\n\t"
        "v_mfma_f32_16x16x32_bf16 v[84:87], v[96:99], v[132:135], v[84:87]\n\t"
        "ds_read_b64_tr_b16 v[114:115], %[paH] offset:2120\n\t"
        "v_mfma_f32_16x16x32_bf16 v[80:83], v[96:99], v[156:159], v[80:83]\n\t"
        "ds_read_b64_tr_b16 v[116:117], %[paH] offset:2624\n\t"
        "buffer_load_dwordx4 a[200:203], %[vl16], %[rws], %[t0] offen offset:2048 sc1\n\t"
        "v_mfma_f32_16x16x32_bf16 v[92:95], v[100:103], v[148:151], v[92:95]\n\t"
        "ds_read_b64_tr_b16 v[118:119], %[paH] offset:2632\n\t"
        "v_mfma_f32_16x16x32_bf16 v[88:91], v[100:103], v[140:143], v[88:91]\n\t"
        "ds_read_b64_tr_b16 v[120:121], %[paH] offset:3136\n\t"
        "s_waitcnt lgkmcnt(7)\n\t"
        "v_mfma_f32_16x16x32_bf16 v[84:87], v[100:103], v[128:131], v[84:87]\n\t"
        "ds_read_b64_tr_b16 v[122:123], %[paH] offset:3144\n\t"
        "buffer_load_dwordx4 a[204:207], %[vl16], %[rws], %[t0] offen offset:3072 sc1\n\t"
        "v_mfma_f32_16x16x32_bf16 v[92:95], v[104:107], v[144:147], v[92:95]\n\t"
        "ds_read_b64_tr_b16 v[124:125], %[paH] offset:3648\n\t"
        "v_mfma_f32_16x16x32_bf16 v[88:91], v[104:107], v[136:139], v[88:91]\n\t"
        "ds_read_b64_tr_b16 v[126:127], %[paH] offset:3656\n\t"
        "v_mfma_f32_16x16x32_bf16 v[84:87], v[104:107], v[156:159], v[84:87]\n\t"
        "ds_read_b128 v[152:155], %[pbH] offset:3904\n\t"
        "s_add_u32 %[t0], %[w2o], 53248\n\t"
        "buffer_load_dwordx4 a[208:211], %[vl16], %[rws], %[t0] offen offset:0 sc1\n\t"
        "s_waitcnt lgkmcnt(9)\n\t"
        "v_mfma_f32_16x16x32_bf16 v[92:95], v[108:111], v[140:143], v[92:95]\n\t"
        "ds_read_b64_tr_b16 v[96:97], %[paH] offset:128\n\t"
        "v_mfma_f32_16x16x32_bf16 v[88:91], v[108:111], v[132:135], v[88:91]\n\t"
        "ds_read_b64_tr_b16 v[98:99], %[paH] offset:136\n\t"
        "s_waitcnt lgkmcnt(9)\n\t"
        "v_mfma_f32_16x16x32_bf16 v[92:95], v[112:115], v[136:139], v[92:95]\n\t"
        "ds_read_b128 v[144:147], %[pbH] offset:2880\n\t"
        "v_mfma_f32_16x16x32_bf16 v[88:91], v[112:115], v[128:131], v[88:91]\n\t"
        "ds_read_b128 v[160:163], %[pbH] offset:320\n\t"
        "buffer_load_dwordx4 a[212:215], %[vl16], %[rws], %[t0] offen offset:1024 sc1\n\t"
        "s_waitcnt lgkmcnt(9)\n\t"
        "v_mfma_f32_16x16x32_bf16 v[92:95], v[116:119], v[132:135], v[92:95]\n\t"
        "ds_read_b128 v[132:135], %[pbH] offset:1344\n\t"
        "v_mfma_f32_16x16x32_bf16 v[88:91], v[116:119], v[156:159], v[88:91]\n\t"
        "ds_read_b128 v[148:151], %[pbH] offset:3392\n\t"
        "s_waitcnt lgkmcnt(9)\n\t"
        "v_mfma_f32_16x16x32_bf16 v[92:95], v[120:123], v[128:131], v[92:95]\n\t"
        "ds_read_b64_tr_b16 v[100:101], %[paH] offset:640\n\t"
        "buffer_load_dwordx4 a[216:219], %[vl16], %[rws], %[t0] offen offset:2048 sc1\n\t"
        "s_waitcnt lgkmcnt(8)\n\t"
        "v_mfma_f32_16x16x32_bf16 v[92:95], v[124:127], v[156:159], v[92:95]\n\t"
        "ds_read_b64_tr_b16 v[102:103], %[paH] offset:648\n\t"
        "ds_read_b128 v[140:143], %[pbH] offset:2368\n\t"
        "s_waitcnt lgkmcnt(7)\n\t"
        "v_mfma_f32_16x16x32_bf16 v[92:95], v[96:99], v[152:155], v[92:95]\n\t"
        "ds_read_b128 v[128:131], %[pbH] offset:832\n\t"
        "s_waitcnt lgkmcnt(7)\n\t"
        "v_mfma_f32_16x16x32_bf16 v[88:91], v[96:99], v[144:147], v[88:91]\n\t"
        "ds_read_b64_tr_b16 v[104:105], %[paH] offset:1152\n\t"
        "buffer_load_dwordx4 a[220:223], %[vl16], %[rws], %[t0] offen offset:3072 sc1\n\t"
        "s_waitcnt lgkmcnt(6)\n\t"
        "v_mfma_f32_16x16x32_bf16 v[84:87], v[96:99], v[132:135], v[84:87]\n\t"
        "ds_read_b64_tr_b16 v[106:107], %[paH] offset:1160\n\t"
        "ds_read_b128 v[136:139], %[pbH] offset:1856\n\t"
        "v_mfma_f32_16x16x32_bf16 v[80:83], v[96:99], v[160:163], v[80:83]\n\t"
        "ds_read_b64_tr_b16 v[108:109], %[paH] offset:1664\n\t"
        "s_waitcnt lgkmcnt(6)\n\t"
        "v_mfma_f32_16x16x32_bf16 v[92:95], v[100:103], v[148:151], v[92:95]\n\t"
        "ds_read_b64_tr_b16 v[110:111], %[paH] offset:1672\n\t"
        "s_waitcnt lgkmcnt(6)\n\t"
        "v_mfma_f32_16x16x32_bf16 v[88:91], v[100:103], v[140:143], v[88:91]\n\t"
        "ds_read_b64_tr_b16 v[112:113], %[paH] offset:2176\n\t"
        "s_add_u32 %[t0], %[w2o], 57344\n\t"
        "buffer_load_dwordx4 a[224:227], %[vl16], %[rws], %[t0] offen offset:0 sc1\n\t"
        "s_waitcnt lgkmcnt(6)\n\t"
        "v_mfma_f32_16x16x32_bf16 v[84:87], v[100:103], v[128:131], v[84:87]\n\t"
        "ds_read_b64_tr_b16 v[114:115], %[paH] offset:2184\n\t"
        "s_waitcnt lgkmcnt(5)\n\t"
        "v_mfma_f32_16x16x32_bf16 v[92:95], v[104:107], v[144:147], v[92:95]\n\t"
        "ds_read_b64_tr_b16 v[116:117], %[paH] offset:2688\n\t"
        "s_waitcnt lgkmcnt(5)\n\t"
        "v_mfma_f32_16x16x32_bf16 v[88:91], v[104:107], v[136:139], v[88:91]\n\t"
        "ds_read_b64_tr_b16 v[118:119], %[paH] offset:2696\n\t"
        "buffer_load_dwordx4 a[228:231], %[vl16], %[rws], %[t0] offen offset:1024 sc1\n\t"
        "s_waitcnt vmcnt(9)\n\t"
        "v_cmp_ne_u32 vcc, %[elo], v204\n\t"
        "v_cmp_ne_u32 %[tm], %[ehi], v205\n\t"
        "s_or_b64 vcc, vcc, %[tm]\n\t"
        "s_cbranch_vccz Lok2_%=\n\t"
        "s_mov_b32 %[t1], 0\n\t"
        "Lsp2_%=:\n\t"
        "buffer_load_dwordx2 v[204:205], %[pollh], %[rws], 0 offen sc1\n\t"
        "s_waitcnt vmcnt(0)\n\t"
        "v_cmp_ne_u32 vcc, %[elo], v204\n\t"
        "v_cmp_ne_u32 %[tm], %[ehi], v205\n\t"
        "s_or_b64 vcc, vcc, %[tm]\n\t"
        "s_cbranch_vccz Lok2_%=\n\t"
        "s_sleep 2\n\t"
        "s_add_u32 %[t1], %[t1], 1\n\t"
        "s_cmp_lt_u32 %[t1], 0x40000\n\t"
        "s_cbranch_scc1 Lsp2_%=\n\t"
        "Lok2_%=:\n\t"
        "v_mfma_f32_16x16x32_bf16 v[84:87], v[104:107], v[160:163], v[84:87]\n\t"
        "ds_read_b64_tr_b16 v[120:121], %[paH] offset:3200\n\t"
        "s_waitcnt lgkmcnt(5)\n\t"
        "v_mfma_f32_16x16x32_bf16 v[92:95], v[108:111], v[140:143], v[92:95]\n\t"
        "ds_read_b64_tr_b16 v[122:123], %[paH] offset:3208\n\t"
        "v_mfma_f32_16x16x32_bf16 v[88:91], v[108:111], v[132:135], v[88:91]\n\t"
        "ds_read_b64_tr_b16 v[124:125], %[paH] offset:3712\n\t"
        "buffer_load_dwordx4 a[232:235], %[vl16], %[rws], %[t0] offen offset:2048 sc1\n\t"
        "s_add_u32 %[t1], %[hoff0], 0\n\t"
        "buffer_load_dwordx4 v[0:3], %[vl16], %[rws], %[t1] offen offset:0 sc1\n\t"
        "s_waitcnt lgkmcnt(5)\n\t"
        "v_mfma_f32_16x16x32_bf16 v[92:95], v[112:115], v[136:139], v[92:95]\n\t"
        "ds_read_b64_tr_b16 v[126:127], %[paH] offset:3720\n\t"
        "ds_read_b128 v[152:155], %[pbH] offset:3840\n\t"
        "ds_read_b64_tr_b16 v[96:97], %[paH] offset:192\n\t"
        "v_mfma_f32_16x16x32_bf16 v[88:91], v[112:115], v[128:131], v[88:91]\n\t"
        "ds_read_b64_tr_b16 v[98:99], %[paH] offset:200\n\t"
        "ds_read_b128 v[144:147], %[pbH] offset:2816\n\t"
        "s_waitcnt lgkmcnt(8)\n\t"
        "v_mfma_f32_16x16x32_bf16 v[92:95], v[116:119], v[132:135], v[92:95]\n\t"
        "ds_read_b128 v[132:135], %[pbH] offset:1280\n\t"
        "buffer_load_dwordx4 v[4:7], %[vl16], %[rws], %[t1] offen offset:1024 sc1\n\t"
        "v_mfma_f32_16x16x32_bf16 v[88:91], v[116:119], v[160:163], v[88:91]\n\t"
        "ds_read_b128 v[156:159], %[pbH] offset:256\n\t"
        "buffer_load_dwordx4 a[236:239], %[vl16], %[rws], %[t0] offen offset:3072 sc1\n\t"
        "s_waitcnt lgkmcnt(8)\n\t"
        "v_mfma_f32_16x16x32_bf16 v[92:95], v[120:123], v[128:131], v[92:95]\n\t"
        "ds_read_b128 v[148:151], %[pbH] offset:3328\n\t"
        "ds_read_b64_tr_b16 v[100:101], %[paH] offset:704\n\t"
        "s_waitcnt lgkmcnt(8)\n\t"
        "v_mfma_f32_16x16x32_bf16 v[92:95], v[124:127], v[160:163], v[92:95]\n\t"
        "ds_read_b64_tr_b16 v[102:103], %[paH] offset:712\n\t"
        "ds_read_b128 v[140:143], %[pbH] offset:2304\n\t"
        "buffer_load_dwordx4 v[8:11], %[vl16], %[rws], %[t1] offen offset:2048 sc1\n\t"
        "s_waitcnt lgkmcnt(7)\n\t"
        "v_mfma_f32_16x16x32_bf16 v[92:95], v[96:99], v[152:155], v[92:95]\n\t"
        "ds_read_b128 v[128:131], %[pbH] offset:768\n\t"
        "s_waitcnt lgkmcnt(7)\n\t"
        "v_mfma_f32_16x16x32_bf16 v[88:91], v[96:99], v[144:147], v[88:91]\n\t"
        "ds_read_b64_tr_b16 v[104:105], %[paH] offset:1216\n\t"
        "s_waitcnt lgkmcnt(7)\n\t"
        "v_mfma_f32_16x16x32_bf16 v[84:87], v[96:99], v[132:135], v[84:87]\n\t"
        "ds_read_b64_tr_b16 v[106:107], %[paH] offset:1224\n\t"
        "ds_read_b128 v[136:139], %[pbH] offset:1792\n\t"
        "buffer_load_dwordx4 v[12:15], %[vl16], %[rws], %[t1] offen offset:3072 sc1\n\t"
        "s_waitcnt lgkmcnt(8)\n\t"
        "v_mfma_f32_16x16x32_bf16 v[80:83], v[96:99], v[156:159], v[80:83]\n\t"
        "ds_read_b64_tr_b16 v[108:109], %[paH] offset:1728\n\t"
        "s_waitcnt lgkmcnt(6)\n\t"
        "v_mfma_f32_16x16x32_bf16 v[92:95], v[100:103], v[148:151], v[92:95]\n\t"
        "ds_read_b64_tr_b16 v[110:111], %[paH] offset:1736\n\t"
        "s_waitcnt lgkmcnt(6)\n\t"
        "v_mfma_f32_16x16x32_bf16 v[88:91], v[100:103], v[140:143], v[88:91]\n\t"
        "ds_read_b64_tr_b16 v[112:113], %[paH] offset:2240\n\t"
        "s_add_u32 %[t1], %[hoff0], 4096\n\t"
        "buffer_load_dwordx4 v[16:19], %[vl16], %[rws], %[t1] offen offset:0 sc1\n\t"
        "s_waitcnt lgkmcnt(6)\n\t"
        "v_mfma_f32_16x16x32_bf16 v[84:87], v[100:103], v[128:131], v[84:87]\n\t"
        "ds_read_b64_tr_b16 v[114:115], %[paH] offset:2248\n\t"
        "s_waitcnt lgkmcnt(5)\n\t"
        "v_mfma_f32_16x16x32_bf16 v[92:95], v[104:107], v[144:147], v[92:95]\n\t"
        "ds_read_b64_tr_b16 v[116:117], %[paH] offset:2752\n\t"
        "s_waitcnt lgkmcnt(5)\n\t"
        "v_mfma_f32_16x16x32_bf16 v[88:91], v[104:107], v[136:139], v[88:91]\n\t"
        "ds_read_b64_tr_b16 v[118:119], %[paH] offset:2760\n\t"
        "buffer_load_dwordx4 v[20:23], %[vl16], %[rws], %[t1] offen offset:1024 sc1\n\t"
        "v_mfma_f32_16x16x32_bf16 v[84:87], v[104:107], v[156:159], v[84:87]\n\t"
        "ds_read_b64_tr_b16 v[120:121], %[paH] offset:3264\n\t"
        "s_waitcnt lgkmcnt(5)\n\t"
        "v_mfma_f32_16x16x32_bf16 v[92:95], v[108:111], v[140:143], v[92:95]\n\t"
        "ds_read_b64_tr_b16 v[122:123], %[paH] offset:3272\n\t"
        "v_mfma_f32_16x16x32_bf16 v[88:91], v[108:111], v[132:135], v[88:91]\n\t"
        "ds_read_b64_tr_b16 v[124:125], %[paH] offset:3776\n\t"
        "buffer_load_dwordx4 v[24:27], %[vl16], %[rws], %[t1] offen offset:2048 sc1\n\t"
        "s_waitcnt lgkmcnt(5)\n\t"
        "v_mfma_f32_16x16x32_bf16 v[92:95], v[112:115], v[136:139], v[92:95]\n\t"
        "ds_read_b64_tr_b16 v[126:127], %[paH] offset:3784\n\t"
        "v_mfma_f32_16x16x32_bf16 v[88:91], v[112:115], v[128:131], v[88:91]\n\t"
        "s_waitcnt lgkmcnt(4)\n\t"
        "v_mfma_f32_16x16x32_bf16 v[92:95], v[116:119], v[132:135], v[92:95]\n\t"
        "buffer_load_dwordx4 v[28:31], %[vl16], %[rws], %[t1] offen offset:3072 sc1\n\t"
        "v_mfma_f32_16x16x32_bf16 v[88:91], v[116:119], v[156:159], v[88:91]\n\t"
        "s_waitcnt lgkmcnt(2)\n\t"
        "v_mfma_f32_16x16x32_bf16 v[92:95], v[120:123], v[128:131], v[92:95]\n\t"
        "s_waitcnt lgkmcnt(0)\n\t"
        "v_mfma_f32_16x16x32_bf16 v[92:95], v[124:127], v[156:159], v[92:95]\n\t"
        "s_add_u32 %[t1], %[hoff0], 8192\n\t"
        "buffer_load_dwordx4 v[32:35], %[vl16], %[rws], %[t1] offen offset:0 sc1\n\t"
        "s_cmp_eq_u32 %[w], 1\n\t"
        "s_cbranch_scc0 Lw30_%=\n\t"
        "s_nop 7\n\t"
        "ds_write_b128 %[aEx], v[48:51] offset:0\n\t"
        "ds_write_b128 %[aEx], v[52:55] offset:1024\n\t"
        "ds_write_b128 %[aEx], v[56:59] offset:2048\n\t"
        "ds_write_b128 %[aEx], v[60:63] offset:3072\n\t"
        "ds_write_b128 %[aEx], v[64:67] offset:4096\n\t"
        "ds_write_b128 %[aEx], v[88:91] offset:18432\n\t"
        "ds_write_b128 %[aEx], v[92:95] offset:19456\n\t"
        "buffer_load_dwordx4 v[36:39], %[vl16], %[rws], %[t1] offen offset:1024 sc1\n\t"
        "buffer_load_dwordx4 v[40:43], %[vl16], %[rws], %[t1] offen offset:2048 sc1\n\t"
        "buffer_load_dwordx4 v[44:47], %[vl16], %[rws], %[t1] offen offset:3072 sc1\n\t"
        "s_add_u32 %[t1], %[hoff1], 0\n\t"
        "buffer_load_dwordx4 v[96:99], %[vl16], %[rws], %[t1] offen offset:0 sc1\n\t"
        "buffer_load_dwordx4 v[100:103], %[vl16], %[rws], %[t1] offen offset:1024 sc1\n\t"
        "buffer_load_dwordx4 v[104:107], %[vl16], %[rws], %[t1] offen offset:2048 sc1\n\t"
        "v_bfe_u32 v193, %[vl16], 8, 1\n\t"
        "v_mul_u32_u24 v192, 0x1f8, v193\n\t"
        "v_add_u32 v192, v192, %[oY]\n\t"
        "v_mul_u32_u24 v193, 0x3f8, v193\n\t"
        "v_add_u32 v193, v193, %[oY]\n\t"
        "s_waitcnt lgkmcnt(0)\n\t"
        "s_barrier\n\t"
        "ds_read_b128 v[144:147], %[aEx] offset:5120\n\t"
        "ds_read_b128 v[148:151], %[aEx] offset:6144\n\t"
        "ds_read_b128 v[152:155], %[aEx] offset:16384\n\t"
        "ds_read_b128 v[156:159], %[aEx] offset:17408\n\t"
        "ds_read_b128 v[160:163], %[aEx] offset:7168\n\t"
        "buffer_load_dwordx4 v[108:111], %[vl16], %[rws], %[t1] offen offset:3072 sc1\n\t"
        "s_waitcnt lgkmcnt(4)\n\t"
        "v_pk_add_f32 v[68:69], v[68:69], v[144:145]\n\t"
        "v_pk_add_f32 v[70:71], v[70:71], v[146:147]\n\t"
        "v_cvt_pk_bf16_f32 v182, v68, v69\n\t"
        "v_cvt_pk_bf16_f32 v183, v70, v71\n\t"
        "s_add_u32 %[t1], %[hoff1], 4096\n\t"
        "buffer_load_dwordx4 v[112:115], %[vl16], %[rws], %[t1] offen offset:0 sc1\n\t"
        "s_waitcnt lgkmcnt(3)\n\t"
        "v_pk_add_f32 v[72:73], v[72:73], v[148:149]\n\t"
        "v_pk_add_f32 v[74:75], v[74:75], v[150:151]\n\t"
        "v_cvt_pk_bf16_f32 v184, v72, v73\n\t"
        "v_cvt_pk_bf16_f32 v185, v74, v75\n\t"
        "s_nop 1\n\t"
        "v_permlane16_swap_b32 v182, v184\n\t"
        "v_permlane16_swap_b32 v183, v185\n\t"
        "buffer_store_dwordx4 v[182:185], v192, %[rws], 0 offen offset:2560 sc1\n\t"
        "s_waitcnt lgkmcnt(2)\n\t"
        "v_pk_add_f32 v[80:81], v[80:81], v[152:153]\n\t"
        "v_pk_add_f32 v[82:83], v[82:83], v[154:155]\n\t"
        "v_cvt_pk_bf16_f32 v186, v80, v81\n\t"
        "v_cvt_pk_bf16_f32 v187, v82, v83\n\t"
        "s_waitcnt lgkmcnt(1)\n\t"
        "v_pk_add_f32 v[84:85], v[84:85], v[156:157]\n\t"
        "v_pk_add_f32 v[86:87], v[86:87], v[158:159]\n\t"
        "v_cvt_pk_bf16_f32 v188, v84, v85\n\t"
        "v_cvt_pk_bf16_f32 v189, v86, v87\n\t"
        "s_nop 1\n\t"
        "v_permlane16_swap_b32 v186, v188\n\t"
        "v_permlane16_swap_b32 v187, v189\n\t"
        "buffer_store_dwordx4 v[186:189], v193, %[rws], %[dY2] offen offset:0 sc1\n\t"
        "s_waitcnt lgkmcnt(0)\n\t"
        "v_pk_add_f32 v[76:77], v[76:77], v[160:161]\n\t"
        "v_pk_add_f32 v[78:79], v[78:79], v[162:163]\n\t"
        "v_cvt_pk_bf16_f32 v190, v76, v77\n\t"
        "v_cvt_pk_bf16_f32 v191, v78, v79\n\t"
        "buffer_store_dwordx2 v[190:191], %[oY], %[rws], 0 offen offset:3584 sc1\n\t"
        "s_branch Ljoin_%=\n\t"
        "Lw30_%=:\n\t"
        "s_nop 7\n\t"
        "ds_write_b128 %[aEx], v[48:51] offset:0\n\t"
        "ds_write_b128 %[aEx], v[52:55] offset:1024\n\t"
        "ds_write_b128 %[aEx], v[56:59] offset:2048\n\t"
        "ds_write_b128 %[aEx], v[60:63] offset:3072\n\t"
        "ds_write_b128 %[aEx], v[64:67] offset:4096\n\t"
        "ds_write_b128 %[aEx], v[80:83] offset:8192\n\t"
        "ds_write_b128 %[aEx], v[84:87] offset:9216\n\t"
        "buffer_load_dwordx4 v[36:39], %[vl16], %[rws], %[t1] offen offset:1024 sc1\n\t"
        "buffer_load_dwordx4 v[40:43], %[vl16], %[rws], %[t1] offen offset:2048 sc1\n\t"
        "buffer_load_dwordx4 v[44:47], %[vl16], %[rws], %[t1] offen offset:3072 sc1\n\t"
        "s_add_u32 %[t1], %[hoff1], 0\n\t"
        "buffer_load_dwordx4 v[96:99], %[vl16], %[rws], %[t1] offen offset:0 sc1\n\t"
        "buffer_load_dwordx4 v[100:103], %[vl16], %[rws], %[t1] offen offset:1024 sc1\n\t"
        "buffer_load_dwordx4 v[104:107], %[vl16], %[rws], %[t1] offen offset:2048 sc1\n\t"
        "v_bfe_u32 v193, %[vl16], 8, 1\n\t"
        "v_mul_u32_u24 v192, 0x1f8, v193\n\t"
        "v_add_u32 v192, v192, %[oY]\n\t"
        "v_mul_u32_u24 v193, 0x3f8, v193\n\t"
        "v_add_u32 v193, v193, %[oY]\n\t"
        "s_waitcnt lgkmcnt(0)\n\t"
        "s_barrier\n\t"
        "ds_read_b128 v[144:147], %[aEx] offset:5120\n\t"
        "ds_read_b128 v[148:151], %[aEx] offset:6144\n\t"
        "ds_read_b128 v[152:155], %[aEx] offset:10240\n\t"
        "ds_read_b128 v[156:159], %[aEx] offset:11264\n\t"
        "ds_read_b128 v[160:163], %[aEx] offset:7168\n\t"
        "buffer_load_dwordx4 v[108:111], %[vl16], %[rws], %[t1] offen offset:3072 sc1\n\t"
        "s_waitcnt lgkmcnt(4)\n\t"
        "v_pk_add_f32 v[68:69], v[68:69], v[144:145]\n\t"
        "v_pk_add_f32 v[70:71], v[70:71], v[146:147]\n\t"
        "v_cvt_pk_bf16_f32 v182, v68, v69\n\t"
        "v_cvt_pk_bf16_f32 v183, v70, v71\n\t"
        "s_add_u32 %[t1], %[hoff1], 4096\n\t"
        "buffer_load_dwordx4 v[112:115], %[vl16], %[rws], %[t1] offen offset:0 sc1\n\t"
        "s_waitcnt lgkmcnt(3)\n\t"
        "v_pk_add_f32 v[72:73], v[72:73], v[148:149]\n\t"
        "v_pk_add_f32 v[74:75], v[74:75], v[150:151]\n\t"
        "v_cvt_pk_bf16_f32 v184, v72, v73\n\t"
        "v_cvt_pk_bf16_f32 v185, v74, v75\n\t"
        "s_nop 1\n\t"
        "v_permlane16_swap_b32 v182, v184\n\t"
        "v_permlane16_swap_b32 v183, v185\n\t"
        "buffer_store_dwordx4 v[182:185], v192, %[rws], 0 offen offset:2560 sc1\n\t"
        "s_waitcnt lgkmcnt(2)\n\t"
        "v_pk_add_f32 v[88:89], v[88:89], v[152:153]\n\t"
        "v_pk_add_f32 v[90:91], v[90:91], v[154:155]\n\t"
        "v_cvt_pk_bf16_f32 v186, v88, v89\n\t"
        "v_cvt_pk_bf16_f32 v187, v90, v91\n\t"
        "s_waitcnt lgkmcnt(1)\n\t"
        "v_pk_add_f32 v[92:93], v[92:93], v[156:157]\n\t"
        "v_pk_add_f32 v[94:95], v[94:95], v[158:159]\n\t"
        "v_cvt_pk_bf16_f32 v188, v92, v93\n\t"
        "v_cvt_pk_bf16_f32 v189, v94, v95\n\t"
        "s_nop 1\n\t"
        "v_permlane16_swap_b32 v186, v188\n\t"
        "v_permlane16_swap_b32 v187, v189\n\t"
        "buffer_store_dwordx4 v[186:189], v193, %[rws], %[dY2] offen offset:2560 sc1\n\t"
        "s_waitcnt lgkmcnt(0)\n\t"
        "v_pk_add_f32 v[76:77], v[76:77], v[160:161]\n\t"
        "v_pk_add_f32 v[78:79], v[78:79], v[162:163]\n\t"
        "v_cvt_pk_bf16_f32 v190, v76, v77\n\t"
        "v_cvt_pk_bf16_f32 v191, v78, v79\n\t"
        "buffer_store_dwordx2 v[190:191], %[oY], %[rws], 0 offen offset:3584 sc1\n\t"
        "s_branch Ljoin_%=\n\t"
        "Lodd1_%=:\n\t"
        "s_waitcnt lgkmcnt(0)\n\t"
        "v_pk_fma_f32 v[180:181], v[172:173], %[st0], v[164:165] op_sel:[0,1,0] op_sel_hi:[1,1,1]\n\t"
        "v_pk_fma_f32 v[182:183], v[174:175], %[st0], v[166:167] op_sel:[0,1,0] op_sel_hi:[1,1,1]\n\t"
        "v_pk_fma_f32 v[184:185], v[176:177], %[st0], v[168:169] op_sel:[0,1,0] op_sel_hi:[1,1,1]\n\t"
        "v_pk_fma_f32 v[186:187], v[178:179], %[st0], v[170:171] op_sel:[0,1,0] op_sel_hi:[1,1,1]\n\t"
        "v_pk_fma_f32 v[180:181], v[0:1], %[st0], v[180:181] op_sel:[0,0,0] op_sel_hi:[1,0,1]\n\t"
        "v_pk_fma_f32 v[182:183], v[2:3], %[st0], v[182:183] op_sel:[0,0,0] op_sel_hi:[1,0,1]\n\t"
        "v_pk_fma_f32 v[184:185], v[4:5], %[st0], v[184:185] op_sel:[0,0,0] op_sel_hi:[1,0,1]\n\t"
        "v_pk_fma_f32 v[186:187], v[6:7], %[st0], v[186:187] op_sel:[0,0,0] op_sel_hi:[1,0,1]\n\t"
        "v_pk_mul_f32 v[188:189], v[180:181], v[180:181]\n\t"
        "v_pk_mul_f32 v[190:191], v[182:183], v[182:183]\n\t"
        "v_pk_mul_f32 v[192:193], v[184:185], v[184:185]\n\t"
        "v_pk_mul_f32 v[194:195], v[186:187], v[186:187]\n\t"
        "v_pk_fma_f32 v[188:189], v[188:189], %[cc], %[cc] op_sel:[0,0,1] op_sel_hi:[1,0,1]\n\t"
        "v_pk_fma_f32 v[190:191], v[190:191], %[cc], %[cc] op_sel:[0,0,1] op_sel_hi:[1,0,1]\n\t"
        "v_pk_fma_f32 v[192:193], v[192:193], %[cc], %[cc] op_sel:[0,0,1] op_sel_hi:[1,0,1]\n\t"
        "v_pk_fma_f32 v[194:195], v[194:195], %[cc], %[cc] op_sel:[0,0,1] op_sel_hi:[1,0,1]\n\t"
        "v_pk_mul_f32 v[188:189], v[188:189], v[180:181]\n\t"
        "v_pk_mul_f32 v[190:191], v[190:191], v[182:183]\n\t"
        "v_pk_mul_f32 v[192:193], v[192:193], v[184:185]\n\t"
        "v_pk_mul_f32 v[194:195], v[194:195], v[186:187]\n\t"
        "v_exp_f32 v188, v188\n\t"
        "v_exp_f32 v189, v189\n\t"
        "v_exp_f32 v190, v190\n\t"
        "s_add_u32 %[t0], %[w2o], 0\n\t"
        "buffer_load_dwordx4 a[0:3], %[vl16], %[rws], %[t0] offen offset:0 sc1\n\t"
        "v_exp_f32 v191, v191\n\t"
        "v_exp_f32 v192, v192\n\t"
        "v_exp_f32 v193, v193\n\t"
        "v_exp_f32 v194, v194\n\t"
        "v_exp_f32 v195, v195\n\t"
        "v_pk_add_f32 v[188:189], v[188:189], 1.0 op_sel_hi:[1,0]\n\t"
        "v_pk_add_f32 v[190:191], v[190:191], 1.0 op_sel_hi:[1,0]\n\t"
        "v_pk_add_f32 v[192:193], v[192:193], 1.0 op_sel_hi:[1,0]\n\t"
        "v_pk_add_f32 v[194:195], v[194:195], 1.0 op_sel_hi:[1,0]\n\t"
        "v_rcp_f32 v188, v188\n\t"
        "v_rcp_f32 v189, v189\n\t"
        "v_rcp_f32 v190, v190\n\t"
        "buffer_load_dwordx4 a[4:7], %[vl16], %[rws], %[t0] offen offset:1024 sc1\n\t"
        "v_rcp_f32 v191, v191\n\t"
        "v_rcp_f32 v192, v192\n\t"
        "v_rcp_f32 v193, v193\n\t"
        "v_rcp_f32 v194, v194\n\t"
        "v_rcp_f32 v195, v195\n\t"
        "v_pk_mul_f32 v[188:189], v[188:189], v[180:181]\n\t"
        "v_pk_mul_f32 v[190:191], v[190:191], v[182:183]\n\t"
        "v_pk_mul_f32 v[192:193], v[192:193], v[184:185]\n\t"
        "v_pk_mul_f32 v[194:195], v[194:195], v[186:187]\n\t"
        "v_cvt_pk_bf16_f32 v196, v188, v189\n\t"
        "v_cvt_pk_bf16_f32 v197, v190, v191\n\t"
        "v_cvt_pk_bf16_f32 v198, v192, v193\n\t"
        "v_cvt_pk_bf16_f32 v199, v194, v195\n\t"
        "buffer_store_dwordx4 v[196:199], %[vl16], %[rws], %[hs0] offen sc1\n\t"
        "v_mov_b32 v136, 0\n\t"
        "v_mov_b32 v137, 0\n\t"
        "v_readlane_b32 %[t0], %[ddF], %[w]\n\t"
        "v_lshlrev_b32 v128, 16, %[pref]\n\t"
        "s_lshl_b32 %[t0], %[t0], 16\n\t"
        "v_add_f32 v128, %[t0], v128\n\t"
        "s_mov_b32 vcc_lo, 0x80000000\n\t"
        "s_mov_b32 vcc_hi, 0x20004000\n\t"
        "v_cvt_pk_bf16_f32 v128, v128, v128\n\t"
        "v_cndmask_b32 v128, %[pref], v128, vcc\n\t"
        "ds_write_b16 %[aPre], v128\n\t"
        "ds_write_b64 %[aZ], v[136:137]\n\t"
        "v_pk_fma_f32 v[180:181], v[172:173], %[st1], v[164:165] op_sel:[0,1,0] op_sel_hi:[1,1,1]\n\t"
        "v_pk_fma_f32 v[182:183], v[174:175], %[st1], v[166:167] op_sel:[0,1,0] op_sel_hi:[1,1,1]\n\t"
        "v_pk_fma_f32 v[184:185], v[176:177], %[st1], v[168:169] op_sel:[0,1,0] op_sel_hi:[1,1,1]\n\t"
        "v_pk_fma_f32 v[186:187], v[178:179], %[st1], v[170:171] op_sel:[0,1,0] op_sel_hi:[1,1,1]\n\t"
        "v_pk_fma_f32 v[180:181], v[8:9], %[st1], v[180:181] op_sel:[0,0,0] op_sel_hi:[1,0,1]\n\t"
        "v_pk_fma_f32 v[182:183], v[10:11], %[st1], v[182:183] op_sel:[0,0,0] op_sel_hi:[1,0,1]\n\t"
        "v_pk_fma_f32 v[184:185], v[12:13], %[st1], v[184:185] op_sel:[0,0,0] op_sel_hi:[1,0,1]\n\t"
        "v_pk_fma_f32 v[186:187], v[14:15], %[st1], v[186:187] op_sel:[0,0,0] op_sel_hi:[1,0,1]\n\t"
        "v_pk_mul_f32 v[188:189], v[180:181], v[180:181]\n\t"
        "v_pk_mul_f32 v[190:191], v[182:183], v[182:183]\n\t"
        "v_pk_mul_f32 v[192:193], v[184:185], v[184:185]\n\t"
        "v_pk_mul_f32 v[194:195], v[186:187], v[186:187]\n\t"
        "v_pk_fma_f32 v[188:189], v[188:189], %[cc], %[cc] op_sel:[0,0,1] op_sel_hi:[1,0,1]\n\t"
        "v_pk_fma_f32 v[190:191], v[190:191], %[cc], %[cc] op_sel:[0,0,1] op_sel_hi:[1,0,1]\n\t"
        "v_pk_fma_f32 v[192:193], v[192:193], %[cc], %[cc] op_sel:[0,0,1] op_sel_hi:[1,0,1]\n\t"
        "v_pk_fma_f32 v[194:195], v[194:195], %[cc], %[cc] op_sel:[0,0,1] op_sel_hi:[1,0,1]\n\t"
        "v_pk_mul_f32 v[188:189], v[188:189], v[180:181]\n\t"
        "v_pk_mul_f32 v[190:191], v[190:191], v[182:183]\n\t"
        "v_pk_mul_f32 v[192:193], v[192:193], v[184:185]\n\t"
        "v_pk_mul_f32 v[194:195], v[194:195], v[186:187]\n\t"
        "ds_read_b128 v[164:167], %[aC1] offset:0\n\t"
        "ds_read_b128 v[172:175], %[aC1] offset:384\n\t"
        "ds_read_b128 v[168:171], %[aC1] offset:32\n\t"
        "ds_read_b128 v[176:179], %[aC1] offset:416\n\t"
        "v_exp_f32 v188, v188\n\t"
        "s_waitcnt vmcnt(10)\n\t"
        "v_exp_f32 v189, v189\n\t"
        "v_readlane_b32 %[t0], %[ddF], 0\n\t"
        "v_lshlrev_b32 v128, 16, v96\n\t"
        "s_lshl_b32 %[t0], %[t0], 16\n\t"
        "v_add_f32 v128, %[t0], v128\n\t"
        "v_exp_f32 v190, v190\n\t"
        "s_add_u32 %[t0], %[w2o], 0\n\t"
        "buffer_load_dwordx4 a[8:11], %[vl16], %[rws], %[t0] offen offset:2048 sc1\n\t"
        "v_and_b32 v129, 0xffff0000, v96\n\t"
        "s_cmp_eq_u32 %[w], 0\n\t"
        "v_cvt_pk_bf16_f32 v128, v128, v129\n\t"
        "s_cselect_b64 vcc, 1, 0\n\t"
        "v_cndmask_b32 v96, v96, v128, vcc\n\t"
        "v_exp_f32 v191, v191\n\t"
        "ds_write_b128 %[aFc0], v[96:99] offset:0\n\t"
        "v_exp_f32 v192, v192\n\t"
        "v_alignbit_b32 v128, v100, v96, 16\n\t"
        "v_exp_f32 v193, v193\n\t"
        "v_exp_f32 v194, v194\n\t"
        "v_exp_f32 v195, v195\n\t"
        "v_pk_add_f32 v[188:189], v[188:189], 1.0 op_sel_hi:[1,0]\n\t"
        "v_pk_add_f32 v[190:191], v[190:191], 1.0 op_sel_hi:[1,0]\n\t"
        "v_pk_add_f32 v[192:193], v[192:193], 1.0 op_sel_hi:[1,0]\n\t"
        "v_pk_add_f32 v[194:195], v[194:195], 1.0 op_sel_hi:[1,0]\n\t"
        "v_alignbit_b32 v129, v101, v97, 16\n\t"
        "v_alignbit_b32 v130, v102, v98, 16\n\t"
        "v_rcp_f32 v188, v188\n\t"
        "v_alignbit_b32 v131, v103, v99, 16\n\t"
        "ds_write_b128 %[aFc0], v[100:103] offset:8320\n\t"
        "v_rcp_f32 v189, v189\n\t"
        "v_alignbit_b32 v132, v101, v100, 16\n\t"
        "v_alignbit_b32 v133, v102, v101, 16\n\t"
        "v_rcp_f32 v190, v190\n\t"
        "buffer_load_dwordx4 a[12:15], %[vl16], %[rws], %[t0] offen offset:3072 sc1\n\t"
        "v_alignbit_b32 v134, v103, v102, 16\n\t"
        "v_alignbit_b32 v135, v104, v103, 16\n\t"
        "v_rcp_f32 v191, v191\n\t"
        "ds_write_b128 %[aFc0], v[128:131] offset:4160\n\t"
        "ds_write_b128 %[aFc0], v[132:135] offset:12480\n\t"
        "v_rcp_f32 v192, v192\n\t"
        "v_rcp_f32 v193, v193\n\t"
        "v_rcp_f32 v194, v194\n\t"
        "v_rcp_f32 v195, v195\n\t"
        "v_pk_mul_f32 v[188:189], v[188:189], v[180:181]\n\t"
        "v_pk_mul_f32 v[190:191], v[190:191], v[182:183]\n\t"
        "v_pk_mul_f32 v[192:193], v[192:193], v[184:185]\n\t"
        "v_pk_mul_f32 v[194:195], v[194:195], v[186:187]\n\t"
        "v_cvt_pk_bf16_f32 v196, v188, v189\n\t"
        "v_cvt_pk_bf16_f32 v197, v190, v191\n\t"
        "v_cvt_pk_bf16_f32 v198, v192, v193\n\t"
        "v_cvt_pk_bf16_f32 v199, v194, v195\n\t"
        "s_add_u32 %[t1], %[hs0], 0xc000\n\t"
        "buffer_store_dwordx4 v[196:199], %[vl16], %[rws], %[t1] offen sc1\n\t"
        "s_waitcnt lgkmcnt(4)\n\t"
        "v_pk_fma_f32 v[180:181], v[172:173], %[st0], v[164:165] op_sel:[0,1,0] op_sel_hi:[1,1,1]\n\t"
        "v_pk_fma_f32 v[182:183], v[174:175], %[st0], v[166:167] op_sel:[0,1,0] op_sel_hi:[1,1,1]\n\t"
        "v_pk_fma_f32 v[184:185], v[176:177], %[st0], v[168:169] op_sel:[0,1,0] op_sel_hi:[1,1,1]\n\t"
        "v_pk_fma_f32 v[186:187], v[178:179], %[st0], v[170:171] op_sel:[0,1,0] op_sel_hi:[1,1,1]\n\t"
        "v_pk_fma_f32 v[180:181], v[16:17], %[st0], v[180:181] op_sel:[0,0,0] op_sel_hi:[1,0,1]\n\t"
        "v_pk_fma_f32 v[182:183], v[18:19], %[st0], v[182:183] op_sel:[0,0,0] op_sel_hi:[1,0,1]\n\t"
        "v_pk_fma_f32 v[184:185], v[20:21], %[st0], v[184:185] op_sel:[0,0,0] op_sel_hi:[1,0,1]\n\t"
        "v_pk_fma_f32 v[186:187], v[22:23], %[st0], v[186:187] op_sel:[0,0,0] op_sel_hi:[1,0,1]\n\t"
        "v_pk_mul_f32 v[188:189], v[180:181], v[180:181]\n\t"
        "v_pk_mul_f32 v[190:191], v[182:183], v[182:183]\n\t"
        "v_pk_mul_f32 v[192:193], v[184:185], v[184:185]\n\t"
        "v_pk_mul_f32 v[194:195], v[186:187], v[186:187]\n\t"
        "v_pk_fma_f32 v[188:189], v[188:189], %[cc], %[cc] op_sel:[0,0,1] op_sel_hi:[1,0,1]\n\t"
        "v_pk_fma_f32 v[190:191], v[190:191], %[cc], %[cc] op_sel:[0,0,1] op_sel_hi:[1,0,1]\n\t"
        "v_pk_fma_f32 v[192:193], v[192:193], %[cc], %[cc] op_sel:[0,0,1] op_sel_hi:[1,0,1]\n\t"
        "v_pk_fma_f32 v[194:195], v[194:195], %[cc], %[cc] op_sel:[0,0,1] op_sel_hi:[1,0,1]\n\t"
        "v_pk_mul_f32 v[188:189], v[188:189], v[180:181]\n\t"
        "v_pk_mul_f32 v[190:191], v[190:191], v[182:183]\n\t"
        "v_pk_mul_f32 v[192:193], v[192:193], v[184:185]\n\t"
        "v_pk_mul_f32 v[194:195], v[194:195], v[186:187]\n\t"
        "v_exp_f32 v188, v188\n\t"
        "s_waitcnt vmcnt(10)\n\t"
        "v_exp_f32 v189, v189\n\t"
        "v_readlane_b32 %[t0], %[ddF], 1\n\t"
        "v_lshlrev_b32 v128, 16, v106\n\t"
        "s_lshl_b32 %[t0], %[t0], 16\n\t"
        "v_add_f32 v128, %[t0], v128\n\t"
        "v_exp_f32 v190, v190\n\t"
        "s_add_u32 %[t0], %[w2o], 4096\n\t"
        "buffer_load_dwordx4 a[16:19], %[vl16], %[rws], %[t0] offen offset:0 sc1\n\t"
        "v_and_b32 v129, 0xffff0000, v106\n\t"
        "s_cmp_eq_u32 %[w], 0\n\t"
        "v_cvt_pk_bf16_f32 v128, v128, v129\n\t"
        "s_cselect_b64 vcc, 1, 0\n\t"
        "v_cndmask_b32 v106, v106, v128, vcc\n\t"
        "v_exp_f32 v191, v191\n\t"
        "ds_write_b128 %[aFc1], v[106:109] offset:0\n\t"
        "v_exp_f32 v192, v192\n\t"
        "v_alignbit_b32 v128, v110, v106, 16\n\t"
        "v_exp_f32 v193, v193\n\t"
        "v_exp_f32 v194, v194\n\t"
        "v_exp_f32 v195, v195\n\t"
        "v_pk_add_f32 v[188:189], v[188:189], 1.0 op_sel_hi:[1,0]\n\t"
        "v_pk_add_f32 v[190:191], v[190:191], 1.0 op_sel_hi:[1,0]\n\t"
        "v_pk_add_f32 v[192:193], v[192:193], 1.0 op_sel_hi:[1,0]\n\t"
        "v_pk_add_f32 v[194:195], v[194:195], 1.0 op_sel_hi:[1,0]\n\t"
        "v_alignbit_b32 v129, v111, v107, 16\n\t"
        "v_alignbit_b32 v130, v112, v108, 16\n\t"
        "v_rcp_f32 v188, v188\n\t"
        "v_alignbit_b32 v131, v113, v109, 16\n\t"
        "ds_write_b128 %[aFc1], v[110:113] offset:8320\n\t"
        "v_rcp_f32 v189, v189\n\t"
        "v_alignbit_b32 v132, v111, v110, 16\n\t"
        "v_alignbit_b32 v133, v112, v111, 16\n\t"
        "v_rcp_f32 v190, v190\n\t"
        "buffer_load_dwordx4 a[20:23], %[vl16], %[rws], %[t0] offen offset:1024 sc1\n\t"
        "v_alignbit_b32 v134, v113, v112, 16\n\t"
        "v_alignbit_b32 v135, v114, v113, 16\n\t"
        "v_rcp_f32 v191, v191\n\t"
        "ds_write_b128 %[aFc1], v[128:131] offset:4160\n\t"
        "ds_write_b128 %[aFc1], v[132:135] offset:12480\n\t"
        "v_rcp_f32 v192, v192\n\t"
        "v_rcp_f32 v193, v193\n\t"
        "v_rcp_f32 v194, v194\n\t"
        "v_rcp_f32 v195, v195\n\t"
        "v_pk_mul_f32 v[188:189], v[188:189], v[180:181]\n\t"
        "v_pk_mul_f32 v[190:191], v[190:191], v[182:183]\n\t"
        "v_pk_mul_f32 v[192:193], v[192:193], v[184:185]\n\t"
        "v_pk_mul_f32 v[194:195], v[194:195], v[186:187]\n\t"
        "v_cvt_pk_bf16_f32 v196, v188, v189\n\t"
        "v_cvt_pk_bf16_f32 v197, v190, v191\n\t"
        "v_cvt_pk_bf16_f32 v198, v192, v193\n\t"
        "v_cvt_pk_bf16_f32 v199, v194, v195\n\t"
        "buffer_store_dwordx4 v[196:199], %[vl16], %[rws], %[hs1] offen sc1\n\t"
        "v_pk_fma_f32 v[180:181], v[172:173], %[st1], v[164:165] op_sel:[0,1,0] op_sel_hi:[1,1,1]\n\t"
        "v_pk_fma_f32 v[182:183], v[174:175], %[st1], v[166:167] op_sel:[0,1,0] op_sel_hi:[1,1,1]\n\t"
        "v_pk_fma_f32 v[184:185], v[176:177], %[st1], v[168:169] op_sel:[0,1,0] op_sel_hi:[1,1,1]\n\t"
        "v_pk_fma_f32 v[186:187], v[178:179], %[st1], v[170:171] op_sel:[0,1,0] op_sel_hi:[1,1,1]\n\t"
        "v_pk_fma_f32 v[180:181], v[24:25], %[st1], v[180:181] op_sel:[0,0,0] op_sel_hi:[1,0,1]\n\t"
        "v_pk_fma_f32 v[182:183], v[26:27], %[st1], v[182:183] op_sel:[0,0,0] op_sel_hi:[1,0,1]\n\t"
        "v_pk_fma_f32 v[184:185], v[28:29], %[st1], v[184:185] op_sel:[0,0,0] op_sel_hi:[1,0,1]\n\t"
        "v_pk_fma_f32 v[186:187], v[30:31], %[st1], v[186:187] op_sel:[0,0,0] op_sel_hi:[1,0,1]\n\t"
        "v_pk_mul_f32 v[188:189], v[180:181], v[180:181]\n\t"
        "v_pk_mul_f32 v[190:191], v[182:183], v[182:183]\n\t"
        "v_pk_mul_f32 v[192:193], v[184:185], v[184:185]\n\t"
        "v_pk_mul_f32 v[194:195], v[186:187], v[186:187]\n\t"
        "v_pk_fma_f32 v[188:189], v[188:189], %[cc], %[cc] op_sel:[0,0,1] op_sel_hi:[1,0,1]\n\t"
        "v_pk_fma_f32 v[190:191], v[190:191], %[cc], %[cc] op_sel:[0,0,1] op_sel_hi:[1,0,1]\n\t"
        "v_pk_fma_f32 v[192:193], v[192:193], %[cc], %[cc] op_sel:[0,0,1] op_sel_hi:[1,0,1]\n\t"
        "v_pk_fma_f32 v[194:195], v[194:195], %[cc], %[cc] op_sel:[0,0,1] op_sel_hi:[1,0,1]\n\t"
        "v_pk_mul_f32 v[188:189], v[188:189], v[180:181]\n\t"
        "v_pk_mul_f32 v[190:191], v[190:191], v[182:183]\n\t"
        "v_pk_mul_f32 v[192:193], v[192:193], v[184:185]\n\t"
        "v_pk_mul_f32 v[194:195], v[194:195], v[186:187]\n\t"
        "ds_read_b128 v[164:167], %[aC2] offset:0\n\t"
        "ds_read_b128 v[172:175], %[aC2] offset:384\n\t"
        "ds_read_b128 v[168:171], %[aC2] offset:32\n\t"
        "ds_read_b128 v[176:179], %[aC2] offset:416\n\t"
        "v_exp_f32 v188, v188\n\t"
        "s_waitcnt vmcnt(10)\n\t"
        "v_exp_f32 v189, v189\n\t"
        "v_readlane_b32 %[t0], %[ddF], 2\n\t"
        "v_lshlrev_b32 v128, 16, v116\n\t"
        "s_lshl_b32 %[t0], %[t0], 16\n\t"
        "v_add_f32 v128, %[t0], v128\n\t"
        "v_exp_f32 v190, v190\n\t"
        "s_add_u32 %[t0], %[w2o], 4096\n\t"
        "buffer_load_dwordx4 a[24:27], %[vl16], %[rws], %[t0] offen offset:2048 sc1\n\t"
        "v_and_b32 v129, 0xffff0000, v116\n\t"
        "s_cmp_eq_u32 %[w], 0\n\t"
        "v_cvt_pk_bf16_f32 v128, v128, v129\n\t"
        "s_cselect_b64 vcc, 1, 0\n\t"
        "v_cndmask_b32 v116, v116, v128, vcc\n\t"
        "v_exp_f32 v191, v191\n\t"
        "ds_write_b128 %[aFc2], v[116:119] offset:0\n\t"
        "v_exp_f32 v192, v192\n\t"
        "v_alignbit_b32 v128, v120, v116, 16\n\t"
        "v_exp_f32 v193, v193\n\t"
        "v_exp_f32 v194, v194\n\t"
        "v_exp_f32 v195, v195\n\t"
        "v_pk_add_f32 v[188:189], v[188:189], 1.0 op_sel_hi:[1,0]\n\t"
        "v_pk_add_f32 v[190:191], v[190:191], 1.0 op_sel_hi:[1,0]\n\t"
        "v_pk_add_f32 v[192:193], v[192:193], 1.0 op_sel_hi:[1,0]\n\t"
        "v_pk_add_f32 v[194:195], v[194:195], 1.0 op_sel_hi:[1,0]\n\t"
        "v_alignbit_b32 v129, v121, v117, 16\n\t"
        "v_alignbit_b32 v130, v122, v118, 16\n\t"
        "v_rcp_f32 v188, v188\n\t"
        "v_alignbit_b32 v131, v123, v119, 16\n\t"
        "ds_write_b128 %[aFc2], v[120:123] offset:8320\n\t"
        "v_rcp_f32 v189, v189\n\t"
        "v_alignbit_b32 v132, v121, v120, 16\n\t"
        "v_alignbit_b32 v133, v122, v121, 16\n\t"
        "v_rcp_f32 v190, v190\n\t"
        "buffer_load_dwordx4 a[28:31], %[vl16], %[rws], %[t0] offen offset:3072 sc1\n\t"
        "v_alignbit_b32 v134, v123, v122, 16\n\t"
        "v_alignbit_b32 v135, v124, v123, 16\n\t"
        "v_rcp_f32 v191, v191\n\t"
        "ds_write_b128 %[aFc2], v[128:131] offset:4160\n\t"
        "ds_write_b128 %[aFc2], v[132:135] offset:12480\n\t"
        "v_rcp_f32 v192, v192\n\t"
        "v_rcp_f32 v193, v193\n\t"
        "v_rcp_f32 v194, v194\n\t"
        "v_rcp_f32 v195, v195\n\t"
        "v_pk_mul_f32 v[188:189], v[188:189], v[180:181]\n\t"
        "v_pk_mul_f32 v[190:191], v[190:191], v[182:183]\n\t"
        "v_pk_mul_f32 v[192:193], v[192:193], v[184:185]\n\t"
        "v_pk_mul_f32 v[194:195], v[194:195], v[186:187]\n\t"
        "v_cvt_pk_bf16_f32 v196, v188, v189\n\t"
        "v_cvt_pk_bf16_f32 v197, v190, v191\n\t"
        "v_cvt_pk_bf16_f32 v198, v192, v193\n\t"
        "v_cvt_pk_bf16_f32 v199, v194, v195\n\t"
        "s_add_u32 %[t1], %[hs1], 0xc000\n\t"
        "buffer_store_dwordx4 v[196:199], %[vl16], %[rws], %[t1] offen sc1\n\t"
        "s_waitcnt lgkmcnt(4)\n\t"
        "v_pk_fma_f32 v[180:181], v[172:173], %[st0], v[164:165] op_sel:[0,1,0] op_sel_hi:[1,1,1]\n\t"
        "v_pk_fma_f32 v[182:183], v[174:175], %[st0], v[166:167] op_sel:[0,1,0] op_sel_hi:[1,1,1]\n\t"
        "v_pk_fma_f32 v[184:185], v[176:177], %[st0], v[168:169] op_sel:[0,1,0] op_sel_hi:[1,1,1]\n\t"
        "v_pk_fma_f32 v[186:187], v[178:179], %[st0], v[170:171] op_sel:[0,1,0] op_sel_hi:[1,1,1]\n\t"
        "v_pk_fma_f32 v[180:181], v[32:33], %[st0], v[180:181] op_sel:[0,0,0] op_sel_hi:[1,0,1]\n\t"
        "v_pk_fma_f32 v[182:183], v[34:35], %[st0], v[182:183] op_sel:[0,0,0] op_sel_hi:[1,0,1]\n\t"
        "v_pk_fma_f32 v[184:185], v[36:37], %[st0], v[184:185] op_sel:[0,0,0] op_sel_hi:[1,0,1]\n\t"
        "v_pk_fma_f32 v[186:187], v[38:39], %[st0], v[186:187] op_sel:[0,0,0] op_sel_hi:[1,0,1]\n\t"
        "v_pk_mul_f32 v[188:189], v[180:181], v[180:181]\n\t"
        "v_pk_mul_f32 v[190:191], v[182:183], v[182:183]\n\t"
        "v_pk_mul_f32 v[192:193], v[184:185], v[184:185]\n\t"
        "v_pk_mul_f32 v[194:195], v[186:187], v[186:187]\n\t"
        "v_pk_fma_f32 v[188:189], v[188:189], %[cc], %[cc] op_sel:[0,0,1] op_sel_hi:[1,0,1]\n\t"
        "v_pk_fma_f32 v[190:191], v[190:191], %[cc], %[cc] op_sel:[0,0,1] op_sel_hi:[1,0,1]\n\t"
        "v_pk_fma_f32 v[192:193], v[192:193], %[cc], %[cc] op_sel:[0,0,1] op_sel_hi:[1,0,1]\n\t"
        "v_pk_fma_f32 v[194:195], v[194:195], %[cc], %[cc] op_sel:[0,0,1] op_sel_hi:[1,0,1]\n\t"
        "v_pk_mul_f32 v[188:189], v[188:189], v[180:181]\n\t"
        "v_pk_mul_f32 v[190:191], v[190:191], v[182:183]\n\t"
        "v_pk_mul_f32 v[192:193], v[192:193], v[184:185]\n\t"
        "v_pk_mul_f32 v[194:195], v[194:195], v[186:187]\n\t"
        "v_exp_f32 v188, v188\n\t"
        "v_exp_f32 v189, v189\n\t"
        "v_exp_f32 v190, v190\n\t"
        "s_add_u32 %[t0], %[w2o], 8192\n\t"
        "buffer_load_dwordx4 a[32:35], %[vl16], %[rws], %[t0] offen offset:0 sc1\n\t"
        "v_exp_f32 v191, v191\n\t"
        "v_exp_f32 v192, v192\n\t"
        "v_exp_f32 v193, v193\n\t"
        "v_exp_f32 v194, v194\n\t"
        "v_exp_f32 v195, v195\n\t"
        "v_pk_add_f32 v[188:189], v[188:189], 1.0 op_sel_hi:[1,0]\n\t"
        "v_pk_add_f32 v[190:191], v[190:191], 1.0 op_sel_hi:[1,0]\n\t"
        "v_pk_add_f32 v[192:193], v[192:193], 1.0 op_sel_hi:[1,0]\n\t"
        "v_pk_add_f32 v[194:195], v[194:195], 1.0 op_sel_hi:[1,0]\n\t"
        "v_rcp_f32 v188, v188\n\t"
        "v_rcp_f32 v189, v189\n\t"
        "v_rcp_f32 v190, v190\n\t"
        "buffer_load_dwordx4 a[36:39], %[vl16], %[rws], %[t0] offen offset:1024 sc1\n\t"
        "v_rcp_f32 v191, v191\n\t"
        "v_rcp_f32 v192, v192\n\t"
        "v_rcp_f32 v193, v193\n\t"
        "v_rcp_f32 v194, v194\n\t"
        "v_rcp_f32 v195, v195\n\t"
        "v_pk_mul_f32 v[188:189], v[188:189], v[180:181]\n\t"
        "v_pk_mul_f32 v[190:191], v[190:191], v[182:183]\n\t"
        "v_pk_mul_f32 v[192:193], v[192:193], v[184:185]\n\t"
        "v_pk_mul_f32 v[194:195], v[194:195], v[186:187]\n\t"
        "v_cvt_pk_bf16_f32 v196, v188, v189\n\t"
        "v_cvt_pk_bf16_f32 v197, v190, v191\n\t"
        "v_cvt_pk_bf16_f32 v198, v192, v193\n\t"
        "v_cvt_pk_bf16_f32 v199, v194, v195\n\t"
        "buffer_store_dwordx4 v[196:199], %[vl16], %[rws], %[hs2] offen sc1\n\t"
        "v_pk_fma_f32 v[180:181], v[172:173], %[st1], v[164:165] op_sel:[0,1,0] op_sel_hi:[1,1,1]\n\t"
        "v_pk_fma_f32 v[182:183], v[174:175], %[st1], v[166:167] op_sel:[0,1,0] op_sel_hi:[1,1,1]\n\t"
        "v_pk_fma_f32 v[184:185], v[176:177], %[st1], v[168:169] op_sel:[0,1,0] op_sel_hi:[1,1,1]\n\t"
        "v_pk_fma_f32 v[186:187], v[178:179], %[st1], v[170:171] op_sel:[0,1,0] op_sel_hi:[1,1,1]\n\t"
        "v_pk_fma_f32 v[180:181], v[40:41], %[st1], v[180:181] op_sel:[0,0,0] op_sel_hi:[1,0,1]\n\t"
        "v_pk_fma_f32 v[182:183], v[42:43], %[st1], v[182:183] op_sel:[0,0,0] op_sel_hi:[1,0,1]\n\t"
        "v_pk_fma_f32 v[184:185], v[44:45], %[st1], v[184:185] op_sel:[0,0,0] op_sel_hi:[1,0,1]\n\t"
        "v_pk_fma_f32 v[186:187], v[46:47], %[st1], v[186:187] op_sel:[0,0,0] op_sel_hi:[1,0,1]\n\t"
        "v_pk_mul_f32 v[188:189], v[180:181], v[180:181]\n\t"
        "v_pk_mul_f32 v[190:191], v[182:183], v[182:183]\n\t"
        "v_pk_mul_f32 v[192:193], v[184:185], v[184:185]\n\t"
        "v_pk_mul_f32 v[194:195], v[186:187], v[186:187]\n\t"
        "v_pk_fma_f32 v[188:189], v[188:189], %[cc], %[cc] op_sel:[0,0,1] op_sel_hi:[1,0,1]\n\t"
        "v_pk_fma_f32 v[190:191], v[190:191], %[cc], %[cc] op_sel:[0,0,1] op_sel_hi:[1,0,1]\n\t"
        "v_pk_fma_f32 v[192:193], v[192:193], %[cc], %[cc] op_sel:[0,0,1] op_sel_hi:[1,0,1]\n\t"
        "v_pk_fma_f32 v[194:195], v[194:195], %[cc], %[cc] op_sel:[0,0,1] op_sel_hi:[1,0,1]\n\t"
        "v_pk_mul_f32 v[188:189], v[188:189], v[180:181]\n\t"
        "v_pk_mul_f32 v[190:191], v[190:191], v[182:183]\n\t"
        "v_pk_mul_f32 v[192:193], v[192:193], v[184:185]\n\t"
        "v_pk_mul_f32 v[194:195], v[194:195], v[186:187]\n\t"
        "v_exp_f32 v188, v188\n\t"
        "v_exp_f32 v189, v189\n\t"
        "v_exp_f32 v190, v190\n\t"
        "buffer_load_dwordx4 a[40:43], %[vl16], %[rws], %[t0] offen offset:2048 sc1\n\t"
        "v_exp_f32 v191, v191\n\t"
        "v_exp_f32 v192, v192\n\t"
        "v_exp_f32 v193, v193\n\t"
        "v_exp_f32 v194, v194\n\t"
        "v_exp_f32 v195, v195\n\t"
        "v_pk_add_f32 v[188:189], v[188:189], 1.0 op_sel_hi:[1,0]\n\t"
        "v_pk_add_f32 v[190:191], v[190:191], 1.0 op_sel_hi:[1,0]\n\t"
        "v_pk_add_f32 v[192:193], v[192:193], 1.0 op_sel_hi:[1,0]\n\t"
        "v_pk_add_f32 v[194:195], v[194:195], 1.0 op_sel_hi:[1,0]\n\t"
        "v_rcp_f32 v188, v188\n\t"
        "v_rcp_f32 v189, v189\n\t"
        "v_rcp_f32 v190, v190\n\t"
        "buffer_load_dwordx4 a[44:47], %[vl16], %[rws], %[t0] offen offset:3072 sc1\n\t"
        "v_rcp_f32 v191, v191\n\t"
        "v_rcp_f32 v192, v192\n\t"
        "v_rcp_f32 v193, v193\n\t"
        "v_rcp_f32 v194, v194\n\t"
        "v_rcp_f32 v195, v195\n\t"
        "v_pk_mul_f32 v[188:189], v[188:189], v[180:181]\n\t"
        "v_pk_mul_f32 v[190:191], v[190:191], v[182:183]\n\t"
        "v_pk_mul_f32 v[192:193], v[192:193], v[184:185]\n\t"
        "v_pk_mul_f32 v[194:195], v[194:195], v[186:187]\n\t"
        "v_cvt_pk_bf16_f32 v196, v188, v189\n\t"
        "v_cvt_pk_bf16_f32 v197, v190, v191\n\t"
        "v_cvt_pk_bf16_f32 v198, v192, v193\n\t"
        "v_cvt_pk_bf16_f32 v199, v194, v195\n\t"
        "s_add_u32 %[t1], %[hs2], 0xc000\n\t"
        "buffer_store_dwordx4 v[196:199], %[vl16], %[rws], %[t1] offen sc1\n\t"
        "s_waitcnt lgkmcnt(0)\n\t"
        "s_barrier\n\t"
        "ds_read_b128 v[152:155], %[pbF] offset:4032\n\t"
        "ds_read_b64_tr_b16 v[96:97], %[paF] offset:0\n\t"
        "ds_read_b64_tr_b16 v[98:99], %[paF] offset:8\n\t"
        "ds_read_b128 v[148:151], %[pbF] offset:3520\n\t"
        "ds_read_b128 v[144:147], %[pbF] offset:3008\n\t"
        "ds_read_b128 v[140:143], %[pbF] offset:2496\n\t"
        "ds_read_b128 v[136:139], %[pbF] offset:1984\n\t"
        "ds_read_b128 v[132:135], %[pbF] offset:1472\n\t"
        "ds_read_b128 v[128:131], %[pbF] offset:960\n\t"
        "ds_read_b128 v[156:159], %[pbF] offset:448\n\t"
        "ds_read_b64_tr_b16 v[100:101], %[paF] offset:512\n\t"
        "ds_read_b64_tr_b16 v[102:103], %[paF] offset:520\n\t"
        "ds_read_b64_tr_b16 v[104:105], %[paF] offset:1024\n\t"
        "ds_read_b64_tr_b16 v[106:107], %[paF] offset:1032\n\t"
        "ds_read_b64_tr_b16 v[108:109], %[paF] offset:1536\n\t"
        "ds_read_b64_tr_b16 v[110:111], %[paF] offset:1544\n\t"
        "ds_read_b64_tr_b16 v[112:113], %[paF] offset:2048\n\t"
        "ds_read_b64_tr_b16 v[114:115], %[paF] offset:2056\n\t"
        "ds_read_b64_tr_b16 v[116:117], %[paF] offset:2560\n\t"
        "ds_read_b64_tr_b16 v[118:119], %[paF] offset:2568\n\t"
        "ds_read_b64_tr_b16 v[120:121], %[paF] offset:3072\n\t"
        "ds_read_b64_tr_b16 v[122:123], %[paF] offset:3080\n\t"
        "ds_read_b64_tr_b16 v[124:125], %[paF] offset:3584\n\t"
        "ds_read_b64_tr_b16 v[126:127], %[paF] offset:3592\n\t"
        "ds_read_b128 v[160:163], %[pbF] offset:384\n\t"
        "ds_read_b64 a[246:247], %[aQ0] offset:61440\n\t"
        "ds_read_b64 a[248:249], %[aQ1] offset:61440\n\t"
        "ds_read_b64 a[250:251], %[aQ2] offset:61440\n\t"
        "ds_read_b64 a[252:253], %[aQ3] offset:61440\n\t"
        "ds_read_b64 a[254:255], %[aQ4] offset:61440\n\t"
        "s_waitcnt lgkmcnt(15)\n\t"
        "v_mfma_f32_16x16x32_bf16 v[76:79], v[96:99], v[152:155], 0\n\t"
        "ds_read_b128 v[152:155], %[pbF] offset:3968\n\t"
        "v_mfma_f32_16x16x32_bf16 v[72:75], v[96:99], v[148:151], 0\n\t"
        "v_mfma_f32_16x16x32_bf16 v[68:71], v[96:99], v[144:147], 0\n\t"
        "s_add_u32 %[t0], %[w2o], 12288\n\t"
        "buffer_load_dwordx4 a[48:51], %[vl16], %[rws], %[t0] offen offset:0 sc1\n\t"
        "v_mfma_f32_16x16x32_bf16 v[64:67], v[96:99], v[140:143], 0\n\t"
        "v_mfma_f32_16x16x32_bf16 v[60:63], v[96:99], v[136:139], 0\n\t"
        "v_mfma_f32_16x16x32_bf16 v[56:59], v[96:99], v[132:135], 0\n\t"
        "buffer_load_dwordx4 a[52:55], %[vl16], %[rws], %[t0] offen offset:1024 sc1\n\t"
        "v_mfma_f32_16x16x32_bf16 v[52:55], v[96:99], v[128:131], 0\n\t"
        "v_mfma_f32_16x16x32_bf16 v[48:51], v[96:99], v[156:159], 0\n\t"
        "ds_read_b64_tr_b16 v[96:97], %[paF] offset:64\n\t"
        "v_mfma_f32_16x16x32_bf16 v[76:79], v[100:103], v[148:151], v[76:79]\n\t"
        "ds_read_b64_tr_b16 v[98:99], %[paF] offset:72\n\t"
        "v_mfma_f32_16x16x32_bf16 v[72:75], v[100:103], v[144:147], v[72:75]\n\t"
        "ds_read_b128 v[148:151], %[pbF] offset:3456\n\t"
        "buffer_load_dwordx4 a[56:59], %[vl16], %[rws], %[t0] offen offset:2048 sc1\n\t"
        "v_mfma_f32_16x16x32_bf16 v[68:71], v[100:103], v[140:143], v[68:71]\n\t"
        "v_mfma_f32_16x16x32_bf16 v[64:67], v[100:103], v[136:139], v[64:67]\n\t"
        "v_mfma_f32_16x16x32_bf16 v[60:63], v[100:103], v[132:135], v[60:63]\n\t"
        "buffer_load_dwordx4 a[60:63], %[vl16], %[rws], %[t0] offen offset:3072 sc1\n\t"
        "v_mfma_f32_16x16x32_bf16 v[56:59], v[100:103], v[128:131], v[56:59]\n\t"
        "v_mfma_f32_16x16x32_bf16 v[52:55], v[100:103], v[156:159], v[52:55]\n\t"
        "ds_read_b64_tr_b16 v[100:101], %[paF] offset:576\n\t"
        "v_mfma_f32_16x16x32_bf16 v[76:79], v[104:107], v[144:147], v[76:79]\n\t"
        "ds_read_b128 v[144:147], %[pbF] offset:2944\n\t"
        "s_add_u32 %[t0], %[w2o], 16384\n\t"
        "buffer_load_dwordx4 a[64:67], %[vl16], %[rws], %[t0] offen offset:0 sc1\n\t"
        "v_mfma_f32_16x16x32_bf16 v[72:75], v[104:107], v[140:143], v[72:75]\n\t"
        "ds_read_b64_tr_b16 v[102:103], %[paF] offset:584\n\t"
        "v_mfma_f32_16x16x32_bf16 v[68:71], v[104:107], v[136:139], v[68:71]\n\t"
        "v_mfma_f32_16x16x32_bf16 v[64:67], v[104:107], v[132:135], v[64:67]\n\t"
        "v_mfma_f32_16x16x32_bf16 v[60:63], v[104:107], v[128:131], v[60:63]\n\t"
        "buffer_load_dwordx4 a[68:71], %[vl16], %[rws], %[t0] offen offset:1024 sc1\n\t"
        "v_mfma_f32_16x16x32_bf16 v[56:59], v[104:107], v[156:159], v[56:59]\n\t"
        "ds_read_b64_tr_b16 v[104:105], %[paF] offset:1088\n\t"
        "s_waitcnt lgkmcnt(15)\n\t"
        "v_mfma_f32_16x16x32_bf16 v[76:79], v[108:111], v[140:143], v[76:79]\n\t"
        "ds_read_b128 v[140:143], %[pbF] offset:2432\n\t"
        "v_mfma_f32_16x16x32_bf16 v[72:75], v[108:111], v[136:139], v[72:75]\n\t"
        "ds_read_b64_tr_b16 v[106:107], %[paF] offset:1096\n\t"
        "buffer_load_dwordx4 a[72:75], %[vl16], %[rws], %[t0] offen offset:2048 sc1\n\t"
        "v_mfma_f32_16x16x32_bf16 v[68:71], v[108:111], v[132:135], v[68:71]\n\t"
        "v_mfma_f32_16x16x32_bf16 v[64:67], v[108:111], v[128:131], v[64:67]\n\t"
        "s_waitcnt vmcnt(7)\n\t"
        "s_barrier\n\t"
        "buffer_store_dwordx2 %[evv], %[vfH], %[rws], 0 offen sc1\n\t"
        "v_mfma_f32_16x16x32_bf16 v[60:63], v[108:111], v[156:159], v[60:63]\n\t"
        "ds_read_b64_tr_b16 v[108:109], %[paF] offset:1600\n\t"
        "buffer_load_dwordx4 a[76:79], %[vl16], %[rws], %[t0] offen offset:3072 sc1\n\t"
        "v_mfma_f32_16x16x32_bf16 v[76:79], v[112:115], v[136:139], v[76:79]\n\t"
        "ds_read_b128 v[136:139], %[pbF] offset:1920\n\t"
        "v_mfma_f32_16x16x32_bf16 v[72:75], v[112:115], v[132:135], v[72:75]\n\t"
        "ds_read_b64_tr_b16 v[110:111], %[paF] offset:1608\n\t"
        "v_mfma_f32_16x16x32_bf16 v[68:71], v[112:115], v[128:131], v[68:71]\n\t"
        "v_mfma_f32_16x16x32_bf16 v[64:67], v[112:115], v[156:159], v[64:67]\n\t"
        "ds_read_b64_tr_b16 v[112:113], %[paF] offset:2112\n\t"
        "s_add_u32 %[t0], %[w2o], 20480\n\t"
        "buffer_load_dwordx4 a[80:83], %[vl16], %[rws], %[t0] offen offset:0 sc1\n\t"
        "v_mfma_f32_16x16x32_bf16 v[76:79], v[116:119], v[132:135], v[76:79]\n\t"
        "ds_read_b128 v[132:135], %[pbF] offset:1408\n\t"
        "v_mfma_f32_16x16x32_bf16 v[72:75], v[116:119], v[128:131], v[72:75]\n\t"
        "ds_read_b64_tr_b16 v[114:115], %[paF] offset:2120\n\t"
        "v_mfma_f32_16x16x32_bf16 v[68:71], v[116:119], v[156:159], v[68:71]\n\t"
        "ds_read_b64_tr_b16 v[116:117], %[paF] offset:2624\n\t"
        "buffer_load_dwordx4 a[84:87], %[vl16], %[rws], %[t0] offen offset:1024 sc1\n\t"
        "v_mfma_f32_16x16x32_bf16 v[76:79], v[120:123], v[128:131], v[76:79]\n\t"
        "ds_read_b128 v[128:131], %[pbF] offset:896\n\t"
        "v_mfma_f32_16x16x32_bf16 v[72:75], v[120:123], v[156:159], v[72:75]\n\t"
        "ds_read_b64_tr_b16 v[118:119], %[paF] offset:2632\n\t"
        "s_waitcnt lgkmcnt(15)\n\t"
        "v_mfma_f32_16x16x32_bf16 v[76:79], v[124:127], v[156:159], v[76:79]\n\t"
        "ds_read_b64_tr_b16 v[120:121], %[paF] offset:3136\n\t"
        "buffer_load_dwordx4 a[88:91], %[vl16], %[rws], %[t0] offen offset:2048 sc1\n\t"
        "v_mfma_f32_16x16x32_bf16 v[76:79], v[96:99], v[152:155], v[76:79]\n\t"
        "ds_read_b64_tr_b16 v[122:123], %[paF] offset:3144\n\t"
        "v_mfma_f32_16x16x32_bf16 v[72:75], v[96:99], v[148:151], v[72:75]\n\t"
        "ds_read_b64_tr_b16 v[124:125], %[paF] offset:3648\n\t"
        "s_waitcnt lgkmcnt(15)\n\t"
        "v_mfma_f32_16x16x32_bf16 v[68:71], v[96:99], v[144:147], v[68:71]\n\t"
        "ds_read_b64_tr_b16 v[126:127], %[paF] offset:3656\n\t"
        "s_waitcnt lgkmcnt(14)\n\t"
        "v_mfma_f32_16x16x32_bf16 v[64:67], v[96:99], v[140:143], v[64:67]\n\t"
        "ds_read_b128 v[152:155], %[pbF] offset:3904\n\t"
        "buffer_load_dwordx4 a[92:95], %[vl16], %[rws], %[t0] offen offset:3072 sc1\n\t"
        "s_waitcnt lgkmcnt(12)\n\t"
        "v_mfma_f32_16x16x32_bf16 v[60:63], v[96:99], v[136:139], v[60:63]\n\t"
        "ds_read_b128 v[156:159], %[pbF] offset:320\n\t"
        "s_waitcnt lgkmcnt(10)\n\t"
        "v_mfma_f32_16x16x32_bf16 v[56:59], v[96:99], v[132:135], v[56:59]\n\t"
        "s_waitcnt lgkmcnt(7)\n\t"
        "v_mfma_f32_16x16x32_bf16 v[52:55], v[96:99], v[128:131], v[52:55]\n\t"
        "s_add_u32 %[t0], %[w2o], 24576\n\t"
        "buffer_load_dwordx4 a[96:99], %[vl16], %[rws], %[t0] offen offset:0 sc1\n\t"
        "v_mfma_f32_16x16x32_bf16 v[48:51], v[96:99], v[160:163], v[48:51]\n\t"
        "ds_read_b64_tr_b16 v[96:97], %[paF] offset:128\n\t"
        "v_mfma_f32_16x16x32_bf16 v[76:79], v[100:103], v[148:151], v[76:79]\n\t"
        "ds_read_b64_tr_b16 v[98:99], %[paF] offset:136\n\t"
        "v_mfma_f32_16x16x32_bf16 v[72:75], v[100:103], v[144:147], v[72:75]\n\t"
        "ds_read_b128 v[148:151], %[pbF] offset:3392\n\t"
        "buffer_load_dwordx4 a[100:103], %[vl16], %[rws], %[t0] offen offset:1024 sc1\n\t"
        "v_mfma_f32_16x16x32_bf16 v[68:71], v[100:103], v[140:143], v[68:71]\n\t"
        "v_mfma_f32_16x16x32_bf16 v[64:67], v[100:103], v[136:139], v[64:67]\n\t"
        "v_mfma_f32_16x16x32_bf16 v[60:63], v[100:103], v[132:135], v[60:63]\n\t"
        "v_mfma_f32_16x16x32_bf16 v[56:59], v[100:103], v[128:131], v[56:59]\n\t"
        "buffer_load_dwordx4 a[104:107], %[vl16], %[rws], %[t0] offen offset:2048 sc1\n\t"
        "v_mfma_f32_16x16x32_bf16 v[52:55], v[100:103], v[160:163], v[52:55]\n\t"
        "ds_read_b64_tr_b16 v[100:101], %[paF] offset:640\n\t"
        "v_mfma_f32_16x16x32_bf16 v[76:79], v[104:107], v[144:147], v[76:79]\n\t"
        "ds_read_b128 v[144:147], %[pbF] offset:2880\n\t"
        "v_mfma_f32_16x16x32_bf16 v[72:75], v[104:107], v[140:143], v[72:75]\n\t"
        "ds_read_b64_tr_b16 v[102:103], %[paF] offset:648\n\t"
        "buffer_load_dwordx4 a[108:111], %[vl16], %[rws], %[t0] offen offset:3072 sc1\n\t"
        "v_mfma_f32_16x16x32_bf16 v[68:71], v[104:107], v[136:139], v[68:71]\n\t"
        "v_mfma_f32_16x16x32_bf16 v[64:67], v[104:107], v[132:135], v[64:67]\n\t"
        "v_mfma_f32_16x16x32_bf16 v[60:63], v[104:107], v[128:131], v[60:63]\n\t"
        "s_add_u32 %[t0], %[w2o], 28672\n\t"
        "buffer_load_dwordx4 a[112:115], %[vl16], %[rws], %[t0] offen offset:0 sc1\n\t"
        "v_mfma_f32_16x16x32_bf16 v[56:59], v[104:107], v[160:163], v[56:59]\n\t"
        "ds_read_b64_tr_b16 v[104:105], %[paF] offset:1152\n\t"
        "v_mfma_f32_16x16x32_bf16 v[76:79], v[108:111], v[140:143], v[76:79]\n\t"
        "ds_read_b128 v[140:143], %[pbF] offset:2368\n\t"
        "v_mfma_f32_16x16x32_bf16 v[72:75], v[108:111], v[136:139], v[72:75]\n\t"
        "ds_read_b64_tr_b16 v[106:107], %[paF] offset:1160\n\t"
        "v_mfma_f32_16x16x32_bf16 v[68:71], v[108:111], v[132:135], v[68:71]\n\t"
        "buffer_load_dwordx4 a[116:119], %[vl16], %[rws], %[t0] offen offset:1024 sc1\n\t"
        "v_mfma_f32_16x16x32_bf16 v[64:67], v[108:111], v[128:131], v[64:67]\n\t"
        "v_mfma_f32_16x16x32_bf16 v[60:63], v[108:111], v[160:163], v[60:63]\n\t"
        "ds_read_b64_tr_b16 v[108:109], %[paF] offset:1664\n\t"
        "v_mfma_f32_16x16x32_bf16 v[76:79], v[112:115], v[136:139], v[76:79]\n\t"
        "ds_read_b128 v[136:139], %[pbF] offset:1856\n\t"
        "buffer_load_dwordx4 a[120:123], %[vl16], %[rws], %[t0] offen offset:2048 sc1\n\t"
        "v_mfma_f32_16x16x32_bf16 v[72:75], v[112:115], v[132:135], v[72:75]\n\t"
        "ds_read_b64_tr_b16 v[110:111], %[paF] offset:1672\n\t"
        "v_mfma_f32_16x16x32_bf16 v[68:71], v[112:115], v[128:131], v[68:71]\n\t"
        "v_mfma_f32_16x16x32_bf16 v[64:67], v[112:115], v[160:163], v[64:67]\n\t"
        "ds_read_b64_tr_b16 v[112:113], %[paF] offset:2176\n\t"
        "buffer_load_dwordx4 a[124:127], %[vl16], %[rws], %[t0] offen offset:3072 sc1\n\t"
        "s_waitcnt lgkmcnt(15)\n\t"
        "v_mfma_f32_16x16x32_bf16 v[76:79], v[116:119], v[132:135], v[76:79]\n\t"
        "ds_read_b128 v[132:135], %[pbF] offset:1344\n\t"
        "v_mfma_f32_16x16x32_bf16 v[72:75], v[116:119], v[128:131], v[72:75]\n\t"
        "ds_read_b64_tr_b16 v[114:115], %[paF] offset:2184\n\t"
        "v_mfma_f32_16x16x32_bf16 v[68:71], v[116:119], v[160:163], v[68:71]\n\t"
        "ds_read_b64_tr_b16 v[116:117], %[paF] offset:2688\n\t"
        "v_mfma_f32_16x16x32_bf16 v[76:79], v[120:123], v[128:131], v[76:79]\n\t"
        "ds_read_b128 v[128:131], %[pbF] offset:832\n\t"
        "s_add_u32 %[t0], %[w2o], 32768\n\t"
        "buffer_load_dwordx4 a[128:131], %[vl16], %[rws], %[t0] offen offset:0 sc1\n\t"
        "v_mfma_f32_16x16x32_bf16 v[72:75], v[120:123], v[160:163], v[72:75]\n\t"
        "ds_read_b64_tr_b16 v[118:119], %[paF] offset:2696\n\t"
        "v_mfma_f32_16x16x32_bf16 v[76:79], v[124:127], v[160:163], v[76:79]\n\t"
        "ds_read_b64_tr_b16 v[120:121], %[paF] offset:3200\n\t"
        "s_waitcnt lgkmcnt(15)\n\t"
        "v_mfma_f32_16x16x32_bf16 v[76:79], v[96:99], v[152:155], v[76:79]\n\t"
        "ds_read_b64_tr_b16 v[122:123], %[paF] offset:3208\n\t"
        "buffer_load_dwordx4 a[132:135], %[vl16], %[rws], %[t0] offen offset:1024 sc1\n\t"
        "v_mfma_f32_16x16x32_bf16 v[72:75], v[96:99], v[148:151], v[72:75]\n\t"
        "ds_read_b64_tr_b16 v[124:125], %[paF] offset:3712\n\t"
        "s_waitcnt lgkmcnt(15)\n\t"
        "v_mfma_f32_16x16x32_bf16 v[68:71], v[96:99], v[144:147], v[68:71]\n\t"
        "ds_read_b64_tr_b16 v[126:127], %[paF] offset:3720\n\t"
        "s_waitcnt lgkmcnt(14)\n\t"
        "v_mfma_f32_16x16x32_bf16 v[64:67], v[96:99], v[140:143], v[64:67]\n\t"
        "ds_read_b128 v[160:163], %[pbH] offset:448\n\t"
        "buffer_load_dwordx4 a[136:139], %[vl16], %[rws], %[t0] offen offset:2048 sc1\n\t"
        "s_waitcnt lgkmcnt(12)\n\t"
        "v_mfma_f32_16x16x32_bf16 v[60:63], v[96:99], v[136:139], v[60:63]\n\t"
        "s_waitcnt lgkmcnt(9)\n\t"
        "v_mfma_f32_16x16x32_bf16 v[56:59], v[96:99], v[132:135], v[56:59]\n\t"
        "s_waitcnt lgkmcnt(6)\n\t"
        "v_mfma_f32_16x16x32_bf16 v[52:55], v[96:99], v[128:131], v[52:55]\n\t"
        "v_mfma_f32_16x16x32_bf16 v[48:51], v[96:99], v[156:159], v[48:51]\n\t"
        "ds_read_b64_tr_b16 v[96:97], %[paH] offset:0\n\t"
        "buffer_load_dwordx4 a[140:143], %[vl16], %[rws], %[t0] offen offset:3072 sc1\n\t"
        "v_mfma_f32_16x16x32_bf16 v[76:79], v[100:103], v[148:151], v[76:79]\n\t"
        "ds_read_b128 v[148:151], %[pbH] offset:3520\n\t"
        "v_mfma_f32_16x16x32_bf16 v[72:75], v[100:103], v[144:147], v[72:75]\n\t"
        "ds_read_b64_tr_b16 v[98:99], %[paH] offset:8\n\t"
        "v_mfma_f32_16x16x32_bf16 v[68:71], v[100:103], v[140:143], v[68:71]\n\t"
        "s_add_u32 %[t0], %[w2o], 36864\n\t"
        "buffer_load_dwordx4 a[144:147], %[vl16], %[rws], %[t0] offen offset:0 sc1\n\t"
        "v_mfma_f32_16x16x32_bf16 v[64:67], v[100:103], v[136:139], v[64:67]\n\t"
        "v_mfma_f32_16x16x32_bf16 v[60:63], v[100:103], v[132:135], v[60:63]\n\t"
        "v_mfma_f32_16x16x32_bf16 v[56:59], v[100:103], v[128:131], v[56:59]\n\t"
        "buffer_load_dwordx4 a[148:151], %[vl16], %[rws], %[t0] offen offset:1024 sc1\n\t"
        "v_mfma_f32_16x16x32_bf16 v[52:55], v[100:103], v[156:159], v[52:55]\n\t"
        "ds_read_b64_tr_b16 v[100:101], %[paH] offset:512\n\t"
        "v_mfma_f32_16x16x32_bf16 v[76:79], v[104:107], v[144:147], v[76:79]\n\t"
        "ds_read_b128 v[144:147], %[pbH] offset:3008\n\t"
        "v_mfma_f32_16x16x32_bf16 v[72:75], v[104:107], v[140:143], v[72:75]\n\t"
        "ds_read_b64_tr_b16 v[102:103], %[paH] offset:520\n\t"
        "v_mfma_f32_16x16x32_bf16 v[68:71], v[104:107], v[136:139], v[68:71]\n\t"
        "buffer_load_dwordx4 a[152:155], %[vl16], %[rws], %[t0] offen offset:2048 sc1\n\t"
        "v_mfma_f32_16x16x32_bf16 v[64:67], v[104:107], v[132:135], v[64:67]\n\t"
        "v_mfma_f32_16x16x32_bf16 v[60:63], v[104:107], v[128:131], v[60:63]\n\t"
        "v_mfma_f32_16x16x32_bf16 v[56:59], v[104:107], v[156:159], v[56:59]\n\t"
        "ds_read_b64_tr_b16 v[104:105], %[paH] offset:1024\n\t"
        "buffer_load_dwordx4 a[156:159], %[vl16], %[rws], %[t0] offen offset:3072 sc1\n\t"
        "v_mfma_f32_16x16x32_bf16 v[76:79], v[108:111], v[140:143], v[76:79]\n\t"
        "ds_read_b128 v[140:143], %[pbH] offset:2496\n\t"
        "v_mfma_f32_16x16x32_bf16 v[72:75], v[108:111], v[136:139], v[72:75]\n\t"
        "ds_read_b64_tr_b16 v[106:107], %[paH] offset:1032\n\t"
        "v_mfma_f32_16x16x32_bf16 v[68:71], v[108:111], v[132:135], v[68:71]\n\t"
        "s_add_u32 %[t0], %[w2o], 40960\n\t"
        "buffer_load_dwordx4 a[160:163], %[vl16], %[rws], %[t0] offen offset:0 sc1\n\t"
        "v_mfma_f32_16x16x32_bf16 v[64:67], v[108:111], v[128:131], v[64:67]\n\t"
        "v_mfma_f32_16x16x32_bf16 v[60:63], v[108:111], v[156:159], v[60:63]\n\t"
        "ds_read_b64_tr_b16 v[108:109], %[paH] offset:1536\n\t"
        "v_mfma_f32_16x16x32_bf16 v[76:79], v[112:115], v[136:139], v[76:79]\n\t"
        "ds_read_b128 v[136:139], %[pbH] offset:1984\n\t"
        "v_mfma_f32_16x16x32_bf16 v[72:75], v[112:115], v[132:135], v[72:75]\n\t"
        "ds_read_b64_tr_b16 v[110:111], %[paH] offset:1544\n\t"
        "buffer_load_dwordx4 a[164:167], %[vl16], %[rws], %[t0] offen offset:1024 sc1\n\t"
        "v_mfma_f32_16x16x32_bf16 v[68:71], v[112:115], v[128:131], v[68:71]\n\t"
        "v_mfma_f32_16x16x32_bf16 v[64:67], v[112:115], v[156:159], v[64:67]\n\t"
        "ds_read_b64_tr_b16 v[112:113], %[paH] offset:2048\n\t"
        "s_waitcnt lgkmcnt(15)\n\t"
        "v_mfma_f32_16x16x32_bf16 v[76:79], v[116:119], v[132:135], v[76:79]\n\t"
        "ds_read_b128 v[132:135], %[pbH] offset:1472\n\t"
        "buffer_load_dwordx4 a[168:171], %[vl16], %[rws], %[t0] offen offset:2048 sc1\n\t"
        "v_mfma_f32_16x16x32_bf16 v[72:75], v[116:119], v[128:131], v[72:75]\n\t"
        "ds_read_b64_tr_b16 v[114:115], %[paH] offset:2056\n\t"
        "v_mfma_f32_16x16x32_bf16 v[68:71], v[116:119], v[156:159], v[68:71]\n\t"
        "ds_read_b64_tr_b16 v[116:117], %[paH] offset:2560\n\t"
        "v_mfma_f32_16x16x32_bf16 v[76:79], v[120:123], v[128:131], v[76:79]\n\t"
        "ds_read_b128 v[128:131], %[pbH] offset:960\n\t"
        "buffer_load_dwordx4 a[172:175], %[vl16], %[rws], %[t0] offen offset:3072 sc1\n\t"
        "v_mfma_f32_16x16x32_bf16 v[72:75], v[120:123], v[156:159], v[72:75]\n\t"
        "ds_read_b64_tr_b16 v[118:119], %[paH] offset:2568\n\t"
        "s_waitcnt lgkmcnt(15)\n\t"
        "v_mfma_f32_16x16x32_bf16 v[76:79], v[124:127], v[156:159], v[76:79]\n\t"
        "ds_read_b64_tr_b16 v[120:121], %[paH] offset:3072\n\t"
        "v_mfma_f32_16x16x32_bf16 v[92:95], v[96:99], v[148:151], 0\n\t"
        "ds_read_b64_tr_b16 v[122:123], %[paH] offset:3080\n\t"
        "s_waitcnt lgkmcnt(12)\n\t"
        "v_mfma_f32_16x16x32_bf16 v[88:91], v[96:99], v[140:143], 0\n\t"
        "ds_read_b128 v[148:151], %[pbH] offset:3456\n\t"
        "s_add_u32 %[t0], %[w2o], 45056\n\t"
        "buffer_load_dwordx4 a[176:179], %[vl16], %[rws], %[t0] offen offset:0 sc1\n\t"
        "s_waitcnt lgkmcnt(10)\n\t"
        "v_mfma_f32_16x16x32_bf16 v[84:87], v[96:99], v[136:139], 0\n\t"
        "ds_read_b128 v[156:159], %[pbH] offset:384\n\t"
        "s_waitcnt lgkmcnt(5)\n\t"
        "v_mfma_f32_16x16x32_bf16 v[80:83], v[96:99], v[128:131], 0\n\t"
        "ds_read_b64_tr_b16 v[96:97], %[paH] offset:64\n\t"
        "v_mfma_f32_16x16x32_bf16 v[92:95], v[100:103], v[144:147], v[92:95]\n\t"
        "ds_read_b64_tr_b16 v[98:99], %[paH] offset:72\n\t"
        "buffer_load_dwordx4 a[180:183], %[vl16], %[rws], %[t0] offen offset:1024 sc1\n\t"
        "v_mfma_f32_16x16x32_bf16 v[88:91], v[100:103], v[136:139], v[88:91]\n\t"
        "ds_read_b128 v[144:147], %[pbH] offset:2944\n\t"
        "v_mfma_f32_16x16x32_bf16 v[84:87], v[100:103], v[132:135], v[84:87]\n\t"
        "v_mfma_f32_16x16x32_bf16 v[80:83], v[100:103], v[160:163], v[80:83]\n\t"
        "ds_read_b64_tr_b16 v[100:101], %[paH] offset:576\n\t"
        "buffer_load_dwordx4 a[184:187], %[vl16], %[rws], %[t0] offen offset:2048 sc1\n\t"
        "v_mfma_f32_16x16x32_bf16 v[92:95], v[104:107], v[140:143], v[92:95]\n\t"
        "ds_read_b128 v[140:143], %[pbH] offset:2432\n\t"
        "v_mfma_f32_16x16x32_bf16 v[88:91], v[104:107], v[132:135], v[88:91]\n\t"
        "ds_read_b64_tr_b16 v[102:103], %[paH] offset:584\n\t"
        "v_mfma_f32_16x16x32_bf16 v[84:87], v[104:107], v[128:131], v[84:87]\n\t"
        "ds_read_b64_tr_b16 v[104:105], %[paH] offset:1088\n\t"
        "v_mfma_f32_16x16x32_bf16 v[92:95], v[108:111], v[136:139], v[92:95]\n\t"
        "ds_read_b128 v[136:139], %[pbH] offset:1920\n\t"
        "buffer_load_dwordx4 a[188:191], %[vl16], %[rws], %[t0] offen offset:3072 sc1\n\t"
        "v_mfma_f32_16x16x32_bf16 v[88:91], v[108:111], v[128:131], v[88:91]\n\t"
        "ds_read_b64_tr_b16 v[106:107], %[paH] offset:1096\n\t"
        "v_mfma_f32_16x16x32_bf16 v[84:87], v[108:111], v[160:163], v[84:87]\n\t"
        "ds_read_b64_tr_b16 v[108:109], %[paH] offset:1600\n\t"
        "v_mfma_f32_16x16x32_bf16 v[92:95], v[112:115], v[132:135], v[92:95]\n\t"
        "ds_read_b128 v[132:135], %[pbH] offset:1408\n\t"
        "s_add_u32 %[t0], %[w2o], 49152\n\t"
        "buffer_load_dwordx4 a[192:195], %[vl16], %[rws], %[t0] offen offset:0 sc1\n\t"
        "v_mfma_f32_16x16x32_bf16 v[88:91], v[112:115], v[160:163], v[88:91]\n\t"
        "ds_read_b64_tr_b16 v[110:111], %[paH] offset:1608\n\t"
        "s_waitcnt lgkmcnt(15)\n\t"
        "v_mfma_f32_16x16x32_bf16 v[92:95], v[116:119], v[128:131], v[92:95]\n\t"
        "ds_read_b128 v[128:131], %[pbH] offset:896\n\t"
        "buffer_load_dwordx2 v[204:205], %[pollh], %[rws], 0 offen sc1\n\t"
        "s_waitcnt lgkmcnt(15)\n\t"
        "v_mfma_f32_16x16x32_bf16 v[92:95], v[120:123], v[160:163], v[92:95]\n\t"
        "ds_read_b64_tr_b16 v[112:113], %[paH] offset:2112\n\t"
        "buffer_load_dwordx4 a[196:199], %[vl16], %[rws], %[t0] offen offset:1024 sc1\n\t"
        "s_waitcnt lgkmcnt(12)\n\t"
        "v_mfma_f32_16x16x32_bf16 v[92:95], v[96:99], v[148:151], v[92:95]\n\t"
        "ds_read_b64_tr_b16 v[114:115], %[paH] offset:2120\n\t"
        "s_waitcnt lgkmcnt(10)\n\t"
        "v_mfma_f32_16x16x32_bf16 v[88:91], v[96:99], v[140:143], v[88:91]\n\t"
        "ds_read_b64_tr_b16 v[116:117], %[paH] offset:2624\n\t"
        "s_waitcnt lgkmcnt(8)\n\t"
        "v_mfma_f32_16x16x32_bf16 v[84:87], v[96:99], v[136:139], v[84:87]\n\t"
        "ds_read_b64_tr_b16 v[118:119], %[paH] offset:2632\n\t"
        "s_waitcnt lgkmcnt(4)\n\t"
        "v_mfma_f32_16x16x32_bf16 v[80:83], v[96:99], v[128:131], v[80:83]\n\t"
        "ds_read_b64_tr_b16 v[120:121], %[paH] offset:3136\n\t"
        "buffer_load_dwordx4 a[200:203], %[vl16], %[rws], %[t0] offen offset:2048 sc1\n\t"
        "v_mfma_f32_16x16x32_bf16 v[92:95], v[100:103], v[144:147], v[92:95]\n\t"
        "ds_read_b64_tr_b16 v[122:123], %[paH] offset:3144\n\t"
        "v_mfma_f32_16x16x32_bf16 v[88:91], v[100:103], v[136:139], v[88:91]\n\t"
        "ds_read_b128 v[148:151], %[pbH] offset:3392\n\t"
        "v_mfma_f32_16x16x32_bf16 v[84:87], v[100:103], v[132:135], v[84:87]\n\t"
        "ds_read_b64_tr_b16 v[96:97], %[paH] offset:128\n\t"
        "buffer_load_dwordx4 a[204:207], %[vl16], %[rws], %[t0] offen offset:3072 sc1\n\t"
        "v_mfma_f32_16x16x32_bf16 v[80:83], v[100:103], v[156:159], v[80:83]\n\t"
        "ds_read_b64_tr_b16 v[98:99], %[paH] offset:136\n\t"
        "v_mfma_f32_16x16x32_bf16 v[92:95], v[104:107], v[140:143], v[92:95]\n\t"
        "ds_read_b128 v[140:143], %[pbH] offset:2368\n\t"
        "v_mfma_f32_16x16x32_bf16 v[88:91], v[104:107], v[132:135], v[88:91]\n\t"
        "ds_read_b128 v[144:147], %[pbH] offset:2880\n\t"
        "s_add_u32 %[t0], %[w2o], 53248\n\t"
        "buffer_load_dwordx4 a[208:211], %[vl16], %[rws], %[t0] offen offset:0 sc1\n\t"
        "v_mfma_f32_16x16x32_bf16 v[84:87], v[104:107], v[128:131], v[84:87]\n\t"
        "ds_read_b64_tr_b16 v[100:101], %[paH] offset:640\n\t"
        "v_mfma_f32_16x16x32_bf16 v[92:95], v[108:111], v[136:139], v[92:95]\n\t"
        "ds_read_b128 v[136:139], %[pbH] offset:1856\n\t"
        "v_mfma_f32_16x16x32_bf16 v[88:91], v[108:111], v[128:131], v[88:91]\n\t"
        "ds_read_b64_tr_b16 v[102:103], %[paH] offset:648\n\t"
        "v_mfma_f32_16x16x32_bf16 v[84:87], v[108:111], v[156:159], v[84:87]\n\t"
        "ds_read_b128 v[160:163], %[pbH] offset:320\n\t"
        "buffer_load_dwordx4 a[212:215], %[vl16], %[rws], %[t0] offen offset:1024 sc1\n\t"
        "s_waitcnt lgkmcnt(13)\n\t"
        "v_mfma_f32_16x16x32_bf16 v[92:95], v[112:115], v[132:135], v[92:95]\n\t"
        "ds_read_b128 v[132:135], %[pbH] offset:1344\n\t"
        "v_mfma_f32_16x16x32_bf16 v[88:91], v[112:115], v[156:159], v[88:91]\n\t"
        "ds_read_b64_tr_b16 v[104:105], %[paH] offset:1152\n\t"
        "s_waitcnt lgkmcnt(13)\n\t"
        "v_mfma_f32_16x16x32_bf16 v[92:95], v[116:119], v[128:131], v[92:95]\n\t"
        "ds_read_b128 v[128:131], %[pbH] offset:832\n\t"
        "buffer_load_dwordx4 a[216:219], %[vl16], %[rws], %[t0] offen offset:2048 sc1\n\t"
        "s_waitcnt lgkmcnt(12)\n\t"
        "v_mfma_f32_16x16x32_bf16 v[92:95], v[120:123], v[156:159], v[92:95]\n\t"
        "ds_read_b64_tr_b16 v[106:107], %[paH] offset:1160\n\t"
        "s_waitcnt lgkmcnt(10)\n\t"
        "v_mfma_f32_16x16x32_bf16 v[92:95], v[96:99], v[148:151], v[92:95]\n\t"
        "ds_read_b64_tr_b16 v[108:109], %[paH] offset:1664\n\t"
        "s_waitcnt lgkmcnt(10)\n\t"
        "v_mfma_f32_16x16x32_bf16 v[88:91], v[96:99], v[140:143], v[88:91]\n\t"
        "ds_read_b64_tr_b16 v[110:111], %[paH] offset:1672\n\t"
        "buffer_load_dwordx4 a[220:223], %[vl16], %[rws], %[t0] offen offset:3072 sc1\n\t"
        "s_waitcnt lgkmcnt(8)\n\t"
        "v_mfma_f32_16x16x32_bf16 v[84:87], v[96:99], v[136:139], v[84:87]\n\t"
        "ds_read_b64_tr_b16 v[112:113], %[paH] offset:2176\n\t"
        "s_waitcnt lgkmcnt(4)\n\t"
        "v_mfma_f32_16x16x32_bf16 v[80:83], v[96:99], v[128:131], v[80:83]\n\t"
        "ds_read_b64_tr_b16 v[114:115], %[paH] offset:2184\n\t"
        "v_mfma_f32_16x16x32_bf16 v[92:95], v[100:103], v[144:147], v[92:95]\n\t"
        "ds_read_b64_tr_b16 v[116:117], %[paH] offset:2688\n\t"
        "v_mfma_f32_16x16x32_bf16 v[88:91], v[100:103], v[136:139], v[88:91]\n\t"
        "ds_read_b64_tr_b16 v[118:119], %[paH] offset:2696\n\t"
        "s_add_u32 %[t0], %[w2o], 57344\n\t"
        "buffer_load_dwordx4 a[224:227], %[vl16], %[rws], %[t0] offen offset:0 sc1\n\t"
        "v_mfma_f32_16x16x32_bf16 v[84:87], v[100:103], v[132:135], v[84:87]\n\t"
        "ds_read_b64_tr_b16 v[120:121], %[paH] offset:3200\n\t"
        "v_mfma_f32_16x16x32_bf16 v[80:83], v[100:103], v[160:163], v[80:83]\n\t"
        "ds_read_b64_tr_b16 v[122:123], %[paH] offset:3208\n\t"
        "s_waitcnt lgkmcnt(8)\n\t"
        "v_mfma_f32_16x16x32_bf16 v[92:95], v[104:107], v[140:143], v[92:95]\n\t"
        "ds_read_b128 v[148:151], %[pbH] offset:3328\n\t"
        "buffer_load_dwordx4 a[228:231], %[vl16], %[rws], %[t0] offen offset:1024 sc1\n\t"
        "s_waitcnt vmcnt(9)\n\t"
        "v_cmp_ne_u32 vcc, %[elo], v204\n\t"
        "v_cmp_ne_u32 %[tm], %[ehi], v205\n\t"
        "s_or_b64 vcc, vcc, %[tm]\n\t"
        "s_cbranch_vccz Lok3_%=\n\t"
        "s_mov_b32 %[t1], 0\n\t"
        "Lsp3_%=:\n\t"
        "buffer_load_dwordx2 v[204:205], %[pollh], %[rws], 0 offen sc1\n\t"
        "s_waitcnt vmcnt(0)\n\t"
        "v_cmp_ne_u32 vcc, %[elo], v204\n\t"
        "v_cmp_ne_u32 %[tm], %[ehi], v205\n\t"
        "s_or_b64 vcc, vcc, %[tm]\n\t"
        "s_cbranch_vccz Lok3_%=\n\t"
        "s_sleep 2\n\t"
        "s_add_u32 %[t1], %[t1], 1\n\t"
        "s_cmp_lt_u32 %[t1], 0x40000\n\t"
        "s_cbranch_scc1 Lsp3_%=\n\t"
        "Lok3_%=:\n\t"
        "v_mfma_f32_16x16x32_bf16 v[88:91], v[104:107], v[132:135], v[88:91]\n\t"
        "ds_read_b64_tr_b16 v[96:97], %[paH] offset:192\n\t"
        "v_mfma_f32_16x16x32_bf16 v[84:87], v[104:107], v[128:131], v[84:87]\n\t"
        "ds_read_b64_tr_b16 v[98:99], %[paH] offset:200\n\t"
        "s_waitcnt lgkmcnt(9)\n\t"
        "v_mfma_f32_16x16x32_bf16 v[92:95], v[108:111], v[136:139], v[92:95]\n\t"
        "ds_read_b128 v[140:143], %[pbH] offset:2304\n\t"
        "buffer_load_dwordx4 a[232:235], %[vl16], %[rws], %[t0] offen offset:2048 sc1\n\t"
        "s_add_u32 %[t1], %[hoff0], 0\n\t"
        "buffer_load_dwordx4 v[0:3], %[vl16], %[rws], %[t1] offen offset:0 sc1\n\t"
        "v_mfma_f32_16x16x32_bf16 v[88:91], v[108:111], v[128:131], v[88:91]\n\t"
        "ds_read_b128 v[136:139], %[pbH] offset:1792\n\t"
        "v_mfma_f32_16x16x32_bf16 v[84:87], v[108:111], v[160:163], v[84:87]\n\t"
        "ds_read_b128 v[144:147], %[pbH] offset:2816\n\t"
        "s_waitcnt lgkmcnt(10)\n\t"
        "v_mfma_f32_16x16x32_bf16 v[92:95], v[112:115], v[132:135], v[92:95]\n\t"
        "ds_read_b64_tr_b16 v[100:101], %[paH] offset:704\n\t"
        "buffer_load_dwordx4 v[4:7], %[vl16], %[rws], %[t1] offen offset:1024 sc1\n\t"
        "v_mfma_f32_16x16x32_bf16 v[88:91], v[112:115], v[160:163], v[88:91]\n\t"
        "ds_read_b64_tr_b16 v[102:103], %[paH] offset:712\n\t"
        "buffer_load_dwordx4 a[236:239], %[vl16], %[rws], %[t0] offen offset:3072 sc1\n\t"
        "s_waitcnt lgkmcnt(10)\n\t"
        "v_mfma_f32_16x16x32_bf16 v[92:95], v[116:119], v[128:131], v[92:95]\n\t"
        "ds_read_b128 v[128:131], %[pbH] offset:768\n\t"
        "s_waitcnt lgkmcnt(9)\n\t"
        "v_mfma_f32_16x16x32_bf16 v[92:95], v[120:123], v[160:163], v[92:95]\n\t"
        "ds_read_b128 v[132:135], %[pbH] offset:1280\n\t"
        "buffer_load_dwordx4 v[8:11], %[vl16], %[rws], %[t1] offen offset:2048 sc1\n\t"
        "s_waitcnt lgkmcnt(7)\n\t"
        "v_mfma_f32_16x16x32_bf16 v[92:95], v[96:99], v[148:151], v[92:95]\n\t"
        "ds_read_b128 v[156:159], %[pbH] offset:256\n\t"
        "s_waitcnt lgkmcnt(7)\n\t"
        "v_mfma_f32_16x16x32_bf16 v[88:91], v[96:99], v[140:143], v[88:91]\n\t"
        "ds_read_b64_tr_b16 v[104:105], %[paH] offset:1216\n\t"
        "s_waitcnt lgkmcnt(7)\n\t"
        "v_mfma_f32_16x16x32_bf16 v[84:87], v[96:99], v[136:139], v[84:87]\n\t"
        "ds_read_b64_tr_b16 v[106:107], %[paH] offset:1224\n\t"
        "buffer_load_dwordx4 v[12:15], %[vl16], %[rws], %[t1] offen offset:3072 sc1\n\t"
        "s_waitcnt lgkmcnt(4)\n\t"
        "v_mfma_f32_16x16x32_bf16 v[80:83], v[96:99], v[128:131], v[80:83]\n\t"
        "ds_read_b64_tr_b16 v[108:109], %[paH] offset:1728\n\t"
        "v_mfma_f32_16x16x32_bf16 v[92:95], v[100:103], v[144:147], v[92:95]\n\t"
        "ds_read_b64_tr_b16 v[110:111], %[paH] offset:1736\n\t"
        "v_mfma_f32_16x16x32_bf16 v[88:91], v[100:103], v[136:139], v[88:91]\n\t"
        "ds_read_b64_tr_b16 v[112:113], %[paH] offset:2240\n\t"
        "s_add_u32 %[t1], %[hoff0], 4096\n\t"
        "buffer_load_dwordx4 v[16:19], %[vl16], %[rws], %[t1] offen offset:0 sc1\n\t"
        "s_waitcnt lgkmcnt(6)\n\t"
        "v_mfma_f32_16x16x32_bf16 v[84:87], v[100:103], v[132:135], v[84:87]\n\t"
        "ds_read_b64_tr_b16 v[114:115], %[paH] offset:2248\n\t"
        "s_waitcnt lgkmcnt(6)\n\t"
        "v_mfma_f32_16x16x32_bf16 v[80:83], v[100:103], v[156:159], v[80:83]\n\t"
        "ds_read_b64_tr_b16 v[116:117], %[paH] offset:2752\n\t"
        "s_waitcnt lgkmcnt(5)\n\t"
        "v_mfma_f32_16x16x32_bf16 v[92:95], v[104:107], v[140:143], v[92:95]\n\t"
        "ds_read_b64_tr_b16 v[118:119], %[paH] offset:2760\n\t"
        "buffer_load_dwordx4 v[20:23], %[vl16], %[rws], %[t1] offen offset:1024 sc1\n\t"
        "v_mfma_f32_16x16x32_bf16 v[88:91], v[104:107], v[132:135], v[88:91]\n\t"
        "ds_read_b64_tr_b16 v[120:121], %[paH] offset:3264\n\t"
        "v_mfma_f32_16x16x32_bf16 v[84:87], v[104:107], v[128:131], v[84:87]\n\t"
        "ds_read_b64_tr_b16 v[122:123], %[paH] offset:3272\n\t"
        "s_waitcnt lgkmcnt(6)\n\t"
        "v_mfma_f32_16x16x32_bf16 v[92:95], v[108:111], v[136:139], v[92:95]\n\t"
        "buffer_load_dwordx4 v[24:27], %[vl16], %[rws], %[t1] offen offset:2048 sc1\n\t"
        "v_mfma_f32_16x16x32_bf16 v[88:91], v[108:111], v[128:131], v[88:91]\n\t"
        "v_mfma_f32_16x16x32_bf16 v[84:87], v[108:111], v[156:159], v[84:87]\n\t"
        "s_waitcnt lgkmcnt(4)\n\t"
        "v_mfma_f32_16x16x32_bf16 v[92:95], v[112:115], v[132:135], v[92:95]\n\t"
        "buffer_load_dwordx4 v[28:31], %[vl16], %[rws], %[t1] offen offset:3072 sc1\n\t"
        "v_mfma_f32_16x16x32_bf16 v[88:91], v[112:115], v[156:159], v[88:91]\n\t"
        "s_waitcnt lgkmcnt(2)\n\t"
        "v_mfma_f32_16x16x32_bf16 v[92:95], v[116:119], v[128:131], v[92:95]\n\t"
        "s_waitcnt lgkmcnt(0)\n\t"
        "v_mfma_f32_16x16x32_bf16 v[92:95], v[120:123], v[156:159], v[92:95]\n\t"
        "s_add_u32 %[t1], %[hoff0], 8192\n\t"
        "buffer_load_dwordx4 v[32:35], %[vl16], %[rws], %[t1] offen offset:0 sc1\n\t"
        "s_cmp_eq_u32 %[w], 1\n\t"
        "s_cbranch_scc0 Lw31_%=\n\t"
        "s_nop 7\n\t"
        "ds_write_b128 %[aEx], v[48:51] offset:0\n\t"
        "ds_write_b128 %[aEx], v[52:55] offset:1024\n\t"
        "ds_write_b128 %[aEx], v[56:59] offset:2048\n\t"
        "ds_write_b128 %[aEx], v[60:63] offset:3072\n\t"
        "ds_write_b128 %[aEx], v[64:67] offset:4096\n\t"
        "ds_write_b128 %[aEx], v[88:91] offset:18432\n\t"
        "ds_write_b128 %[aEx], v[92:95] offset:19456\n\t"
        "buffer_load_dwordx4 v[36:39], %[vl16], %[rws], %[t1] offen offset:1024 sc1\n\t"
        "buffer_load_dwordx4 v[40:43], %[vl16], %[rws], %[t1] offen offset:2048 sc1\n\t"
        "buffer_load_dwordx4 v[44:47], %[vl16], %[rws], %[t1] offen offset:3072 sc1\n\t"
        "s_add_u32 %[t1], %[hoff1], 0\n\t"
        "buffer_load_dwordx4 v[96:99], %[vl16], %[rws], %[t1] offen offset:0 sc1\n\t"
        "buffer_load_dwordx4 v[100:103], %[vl16], %[rws], %[t1] offen offset:1024 sc1\n\t"
        "buffer_load_dwordx4 v[104:107], %[vl16], %[rws], %[t1] offen offset:2048 sc1\n\t"
        "v_bfe_u32 v193, %[vl16], 8, 1\n\t"
        "v_mul_u32_u24 v192, 0x1f8, v193\n\t"
        "v_add_u32 v192, v192, %[oY]\n\t"
        "v_mul_u32_u24 v193, 0x3f8, v193\n\t"
        "v_add_u32 v193, v193, %[oY]\n\t"
        "s_waitcnt lgkmcnt(0)\n\t"
        "s_barrier\n\t"
        "ds_read_b128 v[144:147], %[aEx] offset:5120\n\t"
        "ds_read_b128 v[148:151], %[aEx] offset:6144\n\t"
        "ds_read_b128 v[152:155], %[aEx] offset:16384\n\t"
        "ds_read_b128 v[156:159], %[aEx] offset:17408\n\t"
        "ds_read_b128 v[160:163], %[aEx] offset:7168\n\t"
        "buffer_load_dwordx4 v[108:111], %[vl16], %[rws], %[t1] offen offset:3072 sc1\n\t"
        "s_waitcnt lgkmcnt(4)\n\t"
        "v_pk_add_f32 v[68:69], v[68:69], v[144:145]\n\t"
        "v_pk_add_f32 v[70:71], v[70:71], v[146:147]\n\t"
        "v_cvt_pk_bf16_f32 v182, v68, v69\n\t"
        "v_cvt_pk_bf16_f32 v183, v70, v71\n\t"
        "s_add_u32 %[t1], %[hoff1], 4096\n\t"
        "buffer_load_dwordx4 v[112:115], %[vl16], %[rws], %[t1] offen offset:0 sc1\n\t"
        "s_waitcnt lgkmcnt(3)\n\t"
        "v_pk_add_f32 v[72:73], v[72:73], v[148:149]\n\t"
        "v_pk_add_f32 v[74:75], v[74:75], v[150:151]\n\t"
        "v_cvt_pk_bf16_f32 v184, v72, v73\n\t"
        "v_cvt_pk_bf16_f32 v185, v74, v75\n\t"
        "s_nop 1\n\t"
        "v_permlane16_swap_b32 v182, v184\n\t"
        "v_permlane16_swap_b32 v183, v185\n\t"
        "buffer_store_dwordx4 v[182:185], v192, %[rws], 0 offen offset:2560 sc1\n\t"
        "s_waitcnt lgkmcnt(2)\n\t"
        "v_pk_add_f32 v[80:81], v[80:81], v[152:153]\n\t"
        "v_pk_add_f32 v[82:83], v[82:83], v[154:155]\n\t"
        "v_cvt_pk_bf16_f32 v186, v80, v81\n\t"
        "v_cvt_pk_bf16_f32 v187, v82, v83\n\t"
        "s_waitcnt lgkmcnt(1)\n\t"
        "v_pk_add_f32 v[84:85], v[84:85], v[156:157]\n\t"
        "v_pk_add_f32 v[86:87], v[86:87], v[158:159]\n\t"
        "v_cvt_pk_bf16_f32 v188, v84, v85\n\t"
        "v_cvt_pk_bf16_f32 v189, v86, v87\n\t"
        "s_nop 1\n\t"
        "v_permlane16_swap_b32 v186, v188\n\t"
        "v_permlane16_swap_b32 v187, v189\n\t"
        "buffer_store_dwordx4 v[186:189], v193, %[rws], %[dY2] offen offset:512 sc1\n\t"
        "s_waitcnt lgkmcnt(0)\n\t"
        "v_pk_add_f32 v[76:77], v[76:77], v[160:161]\n\t"
        "v_pk_add_f32 v[78:79], v[78:79], v[162:163]\n\t"
        "v_cvt_pk_bf16_f32 v190, v76, v77\n\t"
        "v_cvt_pk_bf16_f32 v191, v78, v79\n\t"
        "buffer_store_dwordx2 v[190:191], %[oY], %[rws], 0 offen offset:3584 sc1\n\t"
        "s_branch Ljoin_%=\n\t"
        "Lw31_%=:\n\t"
        "s_nop 7\n\t"
        "ds_write_b128 %[aEx], v[48:51] offset:0\n\t"
        "ds_write_b128 %[aEx], v[52:55] offset:1024\n\t"
        "ds_write_b128 %[aEx], v[56:59] offset:2048\n\t"
        "ds_write_b128 %[aEx], v[60:63] offset:3072\n\t"
        "ds_write_b128 %[aEx], v[64:67] offset:4096\n\t"
        "ds_write_b128 %[aEx], v[80:83] offset:8192\n\t"
        "ds_write_b128 %[aEx], v[84:87] offset:9216\n\t"
        "buffer_load_dwordx4 v[36:39], %[vl16], %[rws], %[t1] offen offset:1024 sc1\n\t"
        "buffer_load_dwordx4 v[40:43], %[vl16], %[rws], %[t1] offen offset:2048 sc1\n\t"
        "buffer_load_dwordx4 v[44:47], %[vl16], %[rws], %[t1] offen offset:3072 sc1\n\t"
        "s_add_u32 %[t1], %[hoff1], 0\n\t"
        "buffer_load_dwordx4 v[96:99], %[vl16], %[rws], %[t1] offen offset:0 sc1\n\t"
        "buffer_load_dwordx4 v[100:103], %[vl16], %[rws], %[t1] offen offset:1024 sc1\n\t"
        "buffer_load_dwordx4 v[104:107], %[vl16], %[rws], %[t1] offen offset:2048 sc1\n\t"
        "v_bfe_u32 v193, %[vl16], 8, 1\n\t"
        "v_mul_u32_u24 v192, 0x1f8, v193\n\t"
        "v_add_u32 v192, v192, %[oY]\n\t"
        "v_mul_u32_u24 v193, 0x3f8, v193\n\t"
        "v_add_u32 v193, v193, %[oY]\n\t"
        "s_waitcnt lgkmcnt(0)\n\t"
        "s_barrier\n\t"
        "ds_read_b128 v[144:147], %[aEx] offset:5120\n\t"
        "ds_read_b128 v[148:151], %[aEx] offset:6144\n\t"
        "ds_read_b128 v[152:155], %[aEx] offset:10240\n\t"
        "ds_read_b128 v[156:159], %[aEx] offset:11264\n\t"
        "ds_read_b128 v[160:163], %[aEx] offset:7168\n\t"
        "buffer_load_dwordx4 v[108:111], %[vl16], %[rws], %[t1] offen offset:3072 sc1\n\t"
        "s_waitcnt lgkmcnt(4)\n\t"
        "v_pk_add_f32 v[68:69], v[68:69], v[144:145]\n\t"
        "v_pk_add_f32 v[70:71], v[70:71], v[146:147]\n\t"
        "v_cvt_pk_bf16_f32 v182, v68, v69\n\t"
        "v_cvt_pk_bf16_f32 v183, v70, v71\n\t"
        "s_add_u32 %[t1], %[hoff1], 4096\n\t"
        "buffer_load_dwordx4 v[112:115], %[vl16], %[rws], %[t1] offen offset:0 sc1\n\t"
        "s_waitcnt lgkmcnt(3)\n\t"
        "v_pk_add_f32 v[72:73], v[72:73], v[148:149]\n\t"
        "v_pk_add_f32 v[74:75], v[74:75], v[150:151]\n\t"
        "v_cvt_pk_bf16_f32 v184, v72, v73\n\t"
        "v_cvt_pk_bf16_f32 v185, v74, v75\n\t"
        "s_nop 1\n\t"
        "v_permlane16_swap_b32 v182, v184\n\t"
        "v_permlane16_swap_b32 v183, v185\n\t"
        "buffer_store_dwordx4 v[182:185], v192, %[rws], 0 offen offset:2560 sc1\n\t"
        "s_waitcnt lgkmcnt(2)\n\t"
        "v_pk_add_f32 v[88:89], v[88:89], v[152:153]\n\t"
        "v_pk_add_f32 v[90:91], v[90:91], v[154:155]\n\t"
        "v_cvt_pk_bf16_f32 v186, v88, v89\n\t"
        "v_cvt_pk_bf16_f32 v187, v90, v91\n\t"
        "s_waitcnt lgkmcnt(1)\n\t"
        "v_pk_add_f32 v[92:93], v[92:93], v[156:157]\n\t"
        "v_pk_add_f32 v[94:95], v[94:95], v[158:159]\n\t"
        "v_cvt_pk_bf16_f32 v188, v92, v93\n\t"
        "v_cvt_pk_bf16_f32 v189, v94, v95\n\t"
        "s_nop 1\n\t"
        "v_permlane16_swap_b32 v186, v188\n\t"
        "v_permlane16_swap_b32 v187, v189\n\t"
        "buffer_store_dwordx4 v[186:189], v193, %[rws], %[dY2] offen offset:2048 sc1\n\t"
        "s_waitcnt lgkmcnt(0)\n\t"
        "v_pk_add_f32 v[76:77], v[76:77], v[160:161]\n\t"
        "v_pk_add_f32 v[78:79], v[78:79], v[162:163]\n\t"
        "v_cvt_pk_bf16_f32 v190, v76, v77\n\t"
        "v_cvt_pk_bf16_f32 v191, v78, v79\n\t"
        "buffer_store_dwordx2 v[190:191], %[oY], %[rws], 0 offen offset:3584 sc1\n\t"
        "Ljoin_%=:\n\t"
        "s_waitcnt vmcnt(20)\n\t"
        "v_mfma_f32_16x16x32_bf16 v[48:51], a[0:3], v[0:3], 0\n\t"
        "v_mfma_f32_16x16x32_bf16 v[52:55], a[4:7], v[0:3], 0\n\t"
        "v_mfma_f32_16x16x32_bf16 v[56:59], a[8:11], v[0:3], 0\n\t"
        "v_mfma_f32_16x16x32_bf16 v[60:63], a[12:15], v[0:3], 0\n\t"
        "v_mfma_f32_16x16x32_bf16 v[64:67], a[16:19], v[0:3], 0\n\t"
        "s_add_u32 %[t1], %[hoff1], 4096\n\t"
        "buffer_load_dwordx4 v[116:119], %[vl16], %[rws], %[t1] offen offset:1024 sc1\n\t"
        "s_waitcnt vmcnt(20)\n\t"
        "v_mfma_f32_16x16x32_bf16 v[48:51], a[20:23], v[4:7], v[48:51]\n\t"
        "ds_read_b64 v[194:195], %[aB2] offset:0\n\t"
        "v_mfma_f32_16x16x32_bf16 v[52:55], a[24:27], v[4:7], v[52:55]\n\t"
        "ds_read_b64 v[196:197], %[aB2] offset:32\n\t"
        "ds_read_b64 v[198:199], %[aB2] offset:64\n\t"
        "v_mfma_f32_16x16x32_bf16 v[56:59], a[28:31], v[4:7], v[56:59]\n\t"
        "v_mfma_f32_16x16x32_bf16 v[60:63], a[32:35], v[4:7], v[60:63]\n\t"
        "ds_read_b64 v[200:201], %[aB2] offset:96\n\t"
        "v_mfma_f32_16x16x32_bf16 v[64:67], a[36:39], v[4:7], v[64:67]\n\t"
        "buffer_load_dwordx4 v[120:123], %[vl16], %[rws], %[t1] offen offset:2048 sc1\n\t"
        "s_waitcnt vmcnt(19)\n\t"
        "v_mfma_f32_16x16x32_bf16 v[48:51], a[40:43], v[8:11], v[48:51]\n\t"
        "ds_read_b64 v[202:203], %[aB2] offset:128\n\t"
        "v_mfma_f32_16x16x32_bf16 v[52:55], a[44:47], v[8:11], v[52:55]\n\t"
        "v_accvgpr_read_b32 v184, a246\n\t"
        "v_accvgpr_read_b32 v185, a247\n\t"
        "v_mfma_f32_16x16x32_bf16 v[56:59], a[48:51], v[8:11], v[56:59]\n\t"
        "v_mfma_f32_16x16x32_bf16 v[60:63], a[52:55], v[8:11], v[60:63]\n\t"
        "v_accvgpr_read_b32 v186, a248\n\t"
        "v_mfma_f32_16x16x32_bf16 v[64:67], a[56:59], v[8:11], v[64:67]\n\t"
        "buffer_load_dwordx4 v[124:127], %[vl16], %[rws], %[t1] offen offset:3072 sc1\n\t"
        "s_waitcnt vmcnt(19)\n\t"
        "v_mfma_f32_16x16x32_bf16 v[48:51], a[60:63], v[12:15], v[48:51]\n\t"
        "v_accvgpr_read_b32 v187, a249\n\t"
        "v_mfma_f32_16x16x32_bf16 v[52:55], a[64:67], v[12:15], v[52:55]\n\t"
        "v_accvgpr_read_b32 v188, a250\n\t"
        "v_accvgpr_read_b32 v189, a251\n\t"
        "v_mfma_f32_16x16x32_bf16 v[56:59], a[68:71], v[12:15], v[56:59]\n\t"
        "v_mfma_f32_16x16x32_bf16 v[60:63], a[72:75], v[12:15], v[60:63]\n\t"
        "v_accvgpr_read_b32 v190, a252\n\t"
        "v_mfma_f32_16x16x32_bf16 v[64:67], a[76:79], v[12:15], v[64:67]\n\t"
        "s_add_u32 %[t1], %[hoff1], 8192\n\t"
        "buffer_load_dwordx4 v[128:131], %[vl16], %[rws], %[t1] offen offset:0 sc1\n\t"
        "s_waitcnt vmcnt(19)\n\t"
        "v_mfma_f32_16x16x32_bf16 v[48:51], a[80:83], v[16:19], v[48:51]\n\t"
        "v_accvgpr_read_b32 v191, a253\n\t"
        "v_mfma_f32_16x16x32_bf16 v[52:55], a[84:87], v[16:19], v[52:55]\n\t"
        "v_accvgpr_read_b32 v192, a254\n\t"
        "v_accvgpr_read_b32 v193, a255\n\t"
        "v_mfma_f32_16x16x32_bf16 v[56:59], a[88:91], v[16:19], v[56:59]\n\t"
        "v_mfma_f32_16x16x32_bf16 v[60:63], a[92:95], v[16:19], v[60:63]\n\t"
        "v_sub_f32 v88, 1.0, %[gate]\n\t"
        "v_mfma_f32_16x16x32_bf16 v[64:67], a[96:99], v[16:19], v[64:67]\n\t"
        "buffer_load_dwordx4 v[132:135], %[vl16], %[rws], %[t1] offen offset:1024 sc1\n\t"
        "s_waitcnt vmcnt(19)\n\t"
        "v_mfma_f32_16x16x32_bf16 v[48:51], a[100:103], v[20:23], v[48:51]\n\t"
        "v_lshlrev_b32 v144, 16, v184\n\t"
        "v_mfma_f32_16x16x32_bf16 v[52:55], a[104:107], v[20:23], v[52:55]\n\t"
        "v_and_b32 v145, 0xffff0000, v184\n\t"
        "v_lshlrev_b32 v146, 16, v185\n\t"
        "v_mfma_f32_16x16x32_bf16 v[56:59], a[108:111], v[20:23], v[56:59]\n\t"
        "v_mfma_f32_16x16x32_bf16 v[60:63], a[112:115], v[20:23], v[60:63]\n\t"
        "v_and_b32 v147, 0xffff0000, v185\n\t"
        "v_mfma_f32_16x16x32_bf16 v[64:67], a[116:119], v[20:23], v[64:67]\n\t"
        "buffer_load_dwordx4 v[136:139], %[vl16], %[rws], %[t1] offen offset:2048 sc1\n\t"
        "s_waitcnt vmcnt(19)\n\t"
        "v_mfma_f32_16x16x32_bf16 v[48:51], a[120:123], v[24:27], v[48:51]\n\t"
        "v_lshlrev_b32 v148, 16, v186\n\t"
        "v_mfma_f32_16x16x32_bf16 v[52:55], a[124:127], v[24:27], v[52:55]\n\t"
        "v_and_b32 v149, 0xffff0000, v186\n\t"
        "v_lshlrev_b32 v150, 16, v187\n\t"
        "v_mfma_f32_16x16x32_bf16 v[56:59], a[128:131], v[24:27], v[56:59]\n\t"
        "v_mfma_f32_16x16x32_bf16 v[60:63], a[132:135], v[24:27], v[60:63]\n\t"
        "v_and_b32 v151, 0xffff0000, v187\n\t"
        "v_mfma_f32_16x16x32_bf16 v[64:67], a[136:139], v[24:27], v[64:67]\n\t"
        "buffer_load_dwordx4 v[140:143], %[vl16], %[rws], %[t1] offen offset:3072 sc1\n\t"
        "s_waitcnt vmcnt(19)\n\t"
        "v_mfma_f32_16x16x32_bf16 v[48:51], a[140:143], v[28:31], v[48:51]\n\t"
        "v_lshlrev_b32 v152, 16, v188\n\t"
        "v_mfma_f32_16x16x32_bf16 v[52:55], a[144:147], v[28:31], v[52:55]\n\t"
        "v_and_b32 v153, 0xffff0000, v188\n\t"
        "v_lshlrev_b32 v154, 16, v189\n\t"
        "v_mfma_f32_16x16x32_bf16 v[56:59], a[148:151], v[28:31], v[56:59]\n\t"
        "v_mfma_f32_16x16x32_bf16 v[60:63], a[152:155], v[28:31], v[60:63]\n\t"
        "v_and_b32 v155, 0xffff0000, v189\n\t"
        "v_mfma_f32_16x16x32_bf16 v[64:67], a[156:159], v[28:31], v[64:67]\n\t"
        "s_add_u32 %[t1], %[hoff2], 0\n\t"
        "buffer_load_dwordx4 v[0:3], %[vl16], %[rws], %[t1] offen offset:0 sc1\n\t"
        "s_waitcnt vmcnt(19)\n\t"
        "v_mfma_f32_16x16x32_bf16 v[48:51], a[160:163], v[32:35], v[48:51]\n\t"
        "v_lshlrev_b32 v156, 16, v190\n\t"
        "v_mfma_f32_16x16x32_bf16 v[52:55], a[164:167], v[32:35], v[52:55]\n\t"
        "v_and_b32 v157, 0xffff0000, v190\n\t"
        "v_lshlrev_b32 v158, 16, v191\n\t"
        "v_mfma_f32_16x16x32_bf16 v[56:59], a[168:171], v[32:35], v[56:59]\n\t"
        "v_mfma_f32_16x16x32_bf16 v[60:63], a[172:175], v[32:35], v[60:63]\n\t"
        "v_and_b32 v159, 0xffff0000, v191\n\t"
        "v_mfma_f32_16x16x32_bf16 v[64:67], a[176:179], v[32:35], v[64:67]\n\t"
        "buffer_load_dwordx4 v[4:7], %[vl16], %[rws], %[t1] offen offset:1024 sc1\n\t"
        "s_waitcnt vmcnt(9)\n\t"
        "s_barrier\n\t"
        "buffer_store_dwordx2 %[evv], %[vfY], %[rws], 0 offen sc1\n\t"
        "v_mfma_f32_16x16x32_bf16 v[48:51], a[180:183], v[36:39], v[48:51]\n\t"
        "v_lshlrev_b32 v160, 16, v192\n\t"
        "v_mfma_f32_16x16x32_bf16 v[52:55], a[184:187], v[36:39], v[52:55]\n\t"
        "v_and_b32 v161, 0xffff0000, v192\n\t"
        "v_lshlrev_b32 v162, 16, v193\n\t"
        "v_mfma_f32_16x16x32_bf16 v[56:59], a[188:191], v[36:39], v[56:59]\n\t"
        "v_mfma_f32_16x16x32_bf16 v[60:63], a[192:195], v[36:39], v[60:63]\n\t"
        "v_and_b32 v163, 0xffff0000, v193\n\t"
        "v_mfma_f32_16x16x32_bf16 v[64:67], a[196:199], v[36:39], v[64:67]\n\t"
        "buffer_load_dwordx4 v[8:11], %[vl16], %[rws], %[t1] offen offset:2048 sc1\n\t"
        "v_mfma_f32_16x16x32_bf16 v[48:51], a[200:203], v[40:43], v[48:51]\n\t"
        "v_mul_f32 v144, %[gate], v144\n\t"
        "v_mfma_f32_16x16x32_bf16 v[52:55], a[204:207], v[40:43], v[52:55]\n\t"
        "v_mul_f32 v145, %[gate], v145\n\t"
        "v_mul_f32 v146, %[gate], v146\n\t"
        "v_mfma_f32_16x16x32_bf16 v[56:59], a[208:211], v[40:43], v[56:59]\n\t"
        "v_mfma_f32_16x16x32_bf16 v[60:63], a[212:215], v[40:43], v[60:63]\n\t"
        "v_mul_f32 v147, %[gate], v147\n\t"
        "v_mfma_f32_16x16x32_bf16 v[64:67], a[216:219], v[40:43], v[64:67]\n\t"
        "buffer_load_dwordx4 v[12:15], %[vl16], %[rws], %[t1] offen offset:3072 sc1\n\t"
        "v_mfma_f32_16x16x32_bf16 v[48:51], a[220:223], v[44:47], v[48:51]\n\t"
        "v_mul_f32 v148, %[gate], v148\n\t"
        "v_mfma_f32_16x16x32_bf16 v[52:55], a[224:227], v[44:47], v[52:55]\n\t"
        "v_mul_f32 v149, %[gate], v149\n\t"
        "v_mul_f32 v150, %[gate], v150\n\t"
        "v_mfma_f32_16x16x32_bf16 v[56:59], a[228:231], v[44:47], v[56:59]\n\t"
        "v_mfma_f32_16x16x32_bf16 v[60:63], a[232:235], v[44:47], v[60:63]\n\t"
        "v_mul_f32 v151, %[gate], v151\n\t"
        "v_mfma_f32_16x16x32_bf16 v[64:67], a[236:239], v[44:47], v[64:67]\n\t"
        "s_add_u32 %[t1], %[hoff2], 4096\n\t"
        "buffer_load_dwordx4 v[16:19], %[vl16], %[rws], %[t1] offen offset:0 sc1\n\t"
        "v_mfma_f32_16x16x32_bf16 v[68:71], a[0:3], v[96:99], 0\n\t"
        "v_mul_f32 v152, %[gate], v152\n\t"
        "v_mfma_f32_16x16x32_bf16 v[72:75], a[4:7], v[96:99], 0\n\t"
        "v_mul_f32 v153, %[gate], v153\n\t"
        "v_mul_f32 v154, %[gate], v154\n\t"
        "v_mfma_f32_16x16x32_bf16 v[76:79], a[8:11], v[96:99], 0\n\t"
        "v_mfma_f32_16x16x32_bf16 v[80:83], a[12:15], v[96:99], 0\n\t"
        "v_mul_f32 v155, %[gate], v155\n\t"
        "v_mfma_f32_16x16x32_bf16 v[84:87], a[16:19], v[96:99], 0\n\t"
        "buffer_load_dwordx4 v[20:23], %[vl16], %[rws], %[t1] offen offset:1024 sc1\n\t"
        "buffer_load_dwordx4 v[90:93], %[vPE], %[rws], 0 offen sc1\n\t"
        "buffer_load_dwordx4 v[204:207], %[vPE], %[rws], 0 offen offset:1024 sc1\n\t"
        "v_mfma_f32_16x16x32_bf16 v[68:71], a[20:23], v[100:103], v[68:71]\n\t"
        "v_mul_f32 v156, %[gate], v156\n\t"
        "v_mfma_f32_16x16x32_bf16 v[72:75], a[24:27], v[100:103], v[72:75]\n\t"
        "v_mul_f32 v157, %[gate], v157\n\t"
        "v_mul_f32 v158, %[gate], v158\n\t"
        "v_mfma_f32_16x16x32_bf16 v[76:79], a[28:31], v[100:103], v[76:79]\n\t"
        "ds_write_b128 %[gxw0], v[48:51] offset:0\n\t"
        "v_mfma_f32_16x16x32_bf16 v[80:83], a[32:35], v[100:103], v[80:83]\n\t"
        "v_mul_f32 v159, %[gate], v159\n\t"
        "v_mfma_f32_16x16x32_bf16 v[84:87], a[36:39], v[100:103], v[84:87]\n\t"
        "buffer_load_dwordx4 v[24:27], %[vl16], %[rws], %[t1] offen offset:2048 sc1\n\t"
        "v_mfma_f32_16x16x32_bf16 v[68:71], a[40:43], v[104:107], v[68:71]\n\t"
        "v_mul_f32 v160, %[gate], v160\n\t"
        "v_mfma_f32_16x16x32_bf16 v[72:75], a[44:47], v[104:107], v[72:75]\n\t"
        "v_mul_f32 v161, %[gate], v161\n\t"
        "v_mul_f32 v162, %[gate], v162\n\t"
        "v_mfma_f32_16x16x32_bf16 v[76:79], a[48:51], v[104:107], v[76:79]\n\t"
        "ds_write_b128 %[gxw0], v[52:55] offset:1024\n\t"
        "v_mfma_f32_16x16x32_bf16 v[80:83], a[52:55], v[104:107], v[80:83]\n\t"
        "v_mul_f32 v163, %[gate], v163\n\t"
        "v_mfma_f32_16x16x32_bf16 v[84:87], a[56:59], v[104:107], v[84:87]\n\t"
        "buffer_load_dwordx4 v[28:31], %[vl16], %[rws], %[t1] offen offset:3072 sc1\n\t"
        "v_mfma_f32_16x16x32_bf16 v[68:71], a[60:63], v[108:111], v[68:71]\n\t"
        "s_waitcnt lgkmcnt(2)\n\t"
        "v_mfma_f32_16x16x32_bf16 v[72:75], a[64:67], v[108:111], v[72:75]\n\t"
        "v_lshlrev_b32 v164, 16, v194\n\t"
        "v_and_b32 v165, 0xffff0000, v194\n\t"
        "v_mfma_f32_16x16x32_bf16 v[76:79], a[68:71], v[108:111], v[76:79]\n\t"
        "ds_write_b128 %[gxw0], v[56:59] offset:2048\n\t"
        "v_mfma_f32_16x16x32_bf16 v[80:83], a[72:75], v[108:111], v[80:83]\n\t"
        "v_lshlrev_b32 v166, 16, v195\n\t"
        "v_mfma_f32_16x16x32_bf16 v[84:87], a[76:79], v[108:111], v[84:87]\n\t"
        "s_add_u32 %[t1], %[hoff2], 8192\n\t"
        "buffer_load_dwordx4 v[32:35], %[vl16], %[rws], %[t1] offen offset:0 sc1\n\t"
        "v_mfma_f32_16x16x32_bf16 v[68:71], a[80:83], v[112:115], v[68:71]\n\t"
        "v_and_b32 v167, 0xffff0000, v195\n\t"
        "v_mfma_f32_16x16x32_bf16 v[72:75], a[84:87], v[112:115], v[72:75]\n\t"
        "v_lshlrev_b32 v168, 16, v196\n\t"
        "v_and_b32 v169, 0xffff0000, v196\n\t"
        "v_mfma_f32_16x16x32_bf16 v[76:79], a[88:91], v[112:115], v[76:79]\n\t"
        "ds_write_b128 %[gxw0], v[60:63] offset:3072\n\t"
        "v_mfma_f32_16x16x32_bf16 v[80:83], a[92:95], v[112:115], v[80:83]\n\t"
        "v_lshlrev_b32 v170, 16, v197\n\t"
        "v_mfma_f32_16x16x32_bf16 v[84:87], a[96:99], v[112:115], v[84:87]\n\t"
        "buffer_load_dwordx4 v[36:39], %[vl16], %[rws], %[t1] offen offset:1024 sc1\n\t"
        "s_waitcnt vmcnt(19)\n\t"
        "v_mfma_f32_16x16x32_bf16 v[68:71], a[100:103], v[116:119], v[68:71]\n\t"
        "v_and_b32 v171, 0xffff0000, v197\n\t"
        "v_mfma_f32_16x16x32_bf16 v[72:75], a[104:107], v[116:119], v[72:75]\n\t"
        "v_lshlrev_b32 v172, 16, v198\n\t"
        "v_and_b32 v173, 0xffff0000, v198\n\t"
        "v_mfma_f32_16x16x32_bf16 v[76:79], a[108:111], v[116:119], v[76:79]\n\t"
        "ds_write_b128 %[gxw0], v[64:67] offset:4096\n\t"
        "v_mfma_f32_16x16x32_bf16 v[80:83], a[112:115], v[116:119], v[80:83]\n\t"
        "v_lshlrev_b32 v174, 16, v199\n\t"
        "v_mfma_f32_16x16x32_bf16 v[84:87], a[116:119], v[116:119], v[84:87]\n\t"
        "buffer_load_dwordx4 v[40:43], %[vl16], %[rws], %[t1] offen offset:2048 sc1\n\t"
        "s_waitcnt vmcnt(19)\n\t"
        "v_mfma_f32_16x16x32_bf16 v[68:71], a[120:123], v[120:123], v[68:71]\n\t"
        "v_and_b32 v175, 0xffff0000, v199\n\t"
        "v_mfma_f32_16x16x32_bf16 v[72:75], a[124:127], v[120:123], v[72:75]\n\t"
        "v_lshlrev_b32 v176, 16, v200\n\t"
        "v_and_b32 v177, 0xffff0000, v200\n\t"
        "v_mfma_f32_16x16x32_bf16 v[76:79], a[128:131], v[120:123], v[76:79]\n\t"
        "v_mfma_f32_16x16x32_bf16 v[80:83], a[132:135], v[120:123], v[80:83]\n\t"
        "v_lshlrev_b32 v178, 16, v201\n\t"
        "v_mfma_f32_16x16x32_bf16 v[84:87], a[136:139], v[120:123], v[84:87]\n\t"
        "buffer_load_dwordx4 v[44:47], %[vl16], %[rws], %[t1] offen offset:3072 sc1\n\t"
        "s_waitcnt vmcnt(19)\n\t"
        "v_mfma_f32_16x16x32_bf16 v[68:71], a[140:143], v[124:127], v[68:71]\n\t"
        "v_and_b32 v179, 0xffff0000, v201\n\t"
        "v_mfma_f32_16x16x32_bf16 v[72:75], a[144:147], v[124:127], v[72:75]\n\t"
        "v_lshlrev_b32 v180, 16, v202\n\t"
        "v_and_b32 v181, 0xffff0000, v202\n\t"
        "v_mfma_f32_16x16x32_bf16 v[76:79], a[148:151], v[124:127], v[76:79]\n\t"
        "v_mfma_f32_16x16x32_bf16 v[80:83], a[152:155], v[124:127], v[80:83]\n\t"
        "v_lshlrev_b32 v182, 16, v203\n\t"
        "v_mfma_f32_16x16x32_bf16 v[84:87], a[156:159], v[124:127], v[84:87]\n\t"
        "s_add_u32 %[t1], %[hoff3], 0\n\t"
        "buffer_load_dwordx4 v[96:99], %[vl16], %[rws], %[t1] offen offset:0 sc1\n\t"
        "s_waitcnt vmcnt(19)\n\t"
        "v_mfma_f32_16x16x32_bf16 v[68:71], a[160:163], v[128:131], v[68:71]\n\t"
        "v_and_b32 v183, 0xffff0000, v203\n\t"
        "v_mfma_f32_16x16x32_bf16 v[72:75], a[164:167], v[128:131], v[72:75]\n\t"
        "v_mul_f32 v164, v88, v164\n\t"
        "v_mul_f32 v165, v88, v165\n\t"
        "v_mfma_f32_16x16x32_bf16 v[76:79], a[168:171], v[128:131], v[76:79]\n\t"
        "v_mfma_f32_16x16x32_bf16 v[80:83], a[172:175], v[128:131], v[80:83]\n\t"
        "v_mul_f32 v166, v88, v166\n\t"
        "v_mfma_f32_16x16x32_bf16 v[84:87], a[176:179], v[128:131], v[84:87]\n\t"
        "buffer_load_dwordx4 v[100:103], %[vl16], %[rws], %[t1] offen offset:1024 sc1\n\t"
        "s_waitcnt vmcnt(19)\n\t"
        "v_mfma_f32_16x16x32_bf16 v[68:71], a[180:183], v[132:135], v[68:71]\n\t"
        "v_mul_f32 v167, v88, v167\n\t"
        "v_mfma_f32_16x16x32_bf16 v[72:75], a[184:187], v[132:135], v[72:75]\n\t"
        "v_mul_f32 v168, v88, v168\n\t"
        "v_mul_f32 v169, v88, v169\n\t"
        "v_mfma_f32_16x16x32_bf16 v[76:79], a[188:191], v[132:135], v[76:79]\n\t"
        "v_mfma_f32_16x16x32_bf16 v[80:83], a[192:195], v[132:135], v[80:83]\n\t"
        "v_mul_f32 v170, v88, v170\n\t"
        "v_mfma_f32_16x16x32_bf16 v[84:87], a[196:199], v[132:135], v[84:87]\n\t"
        "buffer_load_dwordx4 v[104:107], %[vl16], %[rws], %[t1] offen offset:2048 sc1\n\t"
        "s_waitcnt vmcnt(19)\n\t"
        "v_mfma_f32_16x16x32_bf16 v[68:71], a[200:203], v[136:139], v[68:71]\n\t"
        "v_mul_f32 v171, v88, v171\n\t"
        "v_mfma_f32_16x16x32_bf16 v[72:75], a[204:207], v[136:139], v[72:75]\n\t"
        "v_mul_f32 v172, v88, v172\n\t"
        "v_mul_f32 v173, v88, v173\n\t"
        "v_mfma_f32_16x16x32_bf16 v[76:79], a[208:211], v[136:139], v[76:79]\n\t"
        "v_mfma_f32_16x16x32_bf16 v[80:83], a[212:215], v[136:139], v[80:83]\n\t"
        "v_mul_f32 v174, v88, v174\n\t"
        "v_mfma_f32_16x16x32_bf16 v[84:87], a[216:219], v[136:139], v[84:87]\n\t"
        "buffer_load_dwordx4 v[108:111], %[vl16], %[rws], %[t1] offen offset:3072 sc1\n\t"
        "s_waitcnt vmcnt(19)\n\t"
        "v_mfma_f32_16x16x32_bf16 v[68:71], a[220:223], v[140:143], v[68:71]\n\t"
        "v_mul_f32 v175, v88, v175\n\t"
        "v_mfma_f32_16x16x32_bf16 v[72:75], a[224:227], v[140:143], v[72:75]\n\t"
        "v_mul_f32 v176, v88, v176\n\t"
        "v_mul_f32 v177, v88, v177\n\t"
        "v_mfma_f32_16x16x32_bf16 v[76:79], a[228:231], v[140:143], v[76:79]\n\t"
        "v_mfma_f32_16x16x32_bf16 v[80:83], a[232:235], v[140:143], v[80:83]\n\t"
        "v_mul_f32 v178, v88, v178\n\t"
        "v_mfma_f32_16x16x32_bf16 v[84:87], a[236:239], v[140:143], v[84:87]\n\t"
        "s_add_u32 %[t1], %[hoff3], 4096\n\t"
        "buffer_load_dwordx4 v[112:115], %[vl16], %[rws], %[t1] offen offset:0 sc1\n\t"
        "s_waitcnt vmcnt(19)\n\t"
        "v_mfma_f32_16x16x32_bf16 v[48:51], a[0:3], v[0:3], 0\n\t"
        "v_mul_f32 v179, v88, v179\n\t"
        "v_mfma_f32_16x16x32_bf16 v[52:55], a[4:7], v[0:3], 0\n\t"
        "v_mul_f32 v180, v88, v180\n\t"
        "v_mul_f32 v181, v88, v181\n\t"
        "v_mfma_f32_16x16x32_bf16 v[56:59], a[8:11], v[0:3], 0\n\t"
        "v_mfma_f32_16x16x32_bf16 v[60:63], a[12:15], v[0:3], 0\n\t"
        "v_mul_f32 v182, v88, v182\n\t"
        "v_mfma_f32_16x16x32_bf16 v[64:67], a[16:19], v[0:3], 0\n\t"
        "buffer_load_dwordx4 v[116:119], %[vl16], %[rws], %[t1] offen offset:1024 sc1\n\t"
        "s_waitcnt vmcnt(12)\n\t"
        "v_xor_b32 v90, %[elo], v90\n\t"
        "v_xor_b32 v91, %[ehi], v91\n\t"
        "v_xor_b32 v92, %[elo], v92\n\t"
        "v_xor_b32 v93, %[ehi], v93\n\t"
        "v_xor_b32 v204, %[elo], v204\n\t"
        "v_xor_b32 v205, %[ehi], v205\n\t"
        "v_xor_b32 v206, %[elo], v206\n\t"
        "v_xor_b32 v207, %[ehi], v207\n\t"
        "v_or3_b32 v90, v90, v91, v92\n\t"
        "v_or3_b32 v204, v204, v205, v206\n\t"
        "v_or3_b32 v90, v90, v93, v207\n\t"
        "v_or_b32 v90, v90, v204\n\t"
        "v_mov_b32 v91, 0x80000000\n\t"
        "v_cmp_eq_u32 vcc, 0, v90\n\t"
        "s_cmp_eq_u64 vcc, -1\n\t"
        "s_cselect_b32 %[okE], 1, 0\n\t"
        "s_cselect_b64 vcc, -1, 0\n\t"
        "v_cndmask_b32 v91, v91, %[vE], vcc\n\t"
        "buffer_store_dwordx2 %[evv], v91, %[rws], 0 offen sc1\n\t"
        "v_mfma_f32_16x16x32_bf16 v[48:51], a[20:23], v[4:7], v[48:51]\n\t"
        "v_mul_f32 v183, v88, v183\n\t"
        "v_mfma_f32_16x16x32_bf16 v[52:55], a[24:27], v[4:7], v[52:55]\n\t"
        "v_mfma_f32_16x16x32_bf16 v[56:59], a[28:31], v[4:7], v[56:59]\n\t"
        "ds_write_b128 %[gxw1], v[68:71] offset:5120\n\t"
        "v_mfma_f32_16x16x32_bf16 v[60:63], a[32:35], v[4:7], v[60:63]\n\t"
        "v_mfma_f32_16x16x32_bf16 v[64:67], a[36:39], v[4:7], v[64:67]\n\t"
        "buffer_load_dwordx4 v[120:123], %[vl16], %[rws], %[t1] offen offset:2048 sc1\n\t"
        "v_mfma_f32_16x16x32_bf16 v[48:51], a[40:43], v[8:11], v[48:51]\n\t"
        "v_mfma_f32_16x16x32_bf16 v[52:55], a[44:47], v[8:11], v[52:55]\n\t"
        "v_mfma_f32_16x16x32_bf16 v[56:59], a[48:51], v[8:11], v[56:59]\n\t"
        "ds_write_b128 %[gxw1], v[72:75] offset:6144\n\t"
        "v_mfma_f32_16x16x32_bf16 v[60:63], a[52:55], v[8:11], v[60:63]\n\t"
        "v_mfma_f32_16x16x32_bf16 v[64:67], a[56:59], v[8:11], v[64:67]\n\t"
        "buffer_load_dwordx4 v[124:127], %[vl16], %[rws], %[t1] offen offset:3072 sc1\n\t"
        "v_mfma_f32_16x16x32_bf16 v[48:51], a[60:63], v[12:15], v[48:51]\n\t"
        "v_mfma_f32_16x16x32_bf16 v[52:55], a[64:67], v[12:15], v[52:55]\n\t"
        "v_mfma_f32_16x16x32_bf16 v[56:59], a[68:71], v[12:15], v[56:59]\n\t"
        "ds_write_b128 %[gxw1], v[76:79] offset:7168\n\t"
        "v_mfma_f32_16x16x32_bf16 v[60:63], a[72:75], v[12:15], v[60:63]\n\t"
        "v_mfma_f32_16x16x32_bf16 v[64:67], a[76:79], v[12:15], v[64:67]\n\t"
        "s_add_u32 %[t1], %[hoff3], 8192\n\t"
        "buffer_load_dwordx4 v[128:131], %[vl16], %[rws], %[t1] offen offset:0 sc1\n\t"
        "v_mfma_f32_16x16x32_bf16 v[48:51], a[80:83], v[16:19], v[48:51]\n\t"
        "v_mfma_f32_16x16x32_bf16 v[52:55], a[84:87], v[16:19], v[52:55]\n\t"
        "v_mfma_f32_16x16x32_bf16 v[56:59], a[88:91], v[16:19], v[56:59]\n\t"
        "ds_write_b128 %[gxw1], v[80:83] offset:8192\n\t"
        "v_mfma_f32_16x16x32_bf16 v[60:63], a[92:95], v[16:19], v[60:63]\n\t"
        "v_mfma_f32_16x16x32_bf16 v[64:67], a[96:99], v[16:19], v[64:67]\n\t"
        "buffer_load_dwordx4 v[132:135], %[vl16], %[rws], %[t1] offen offset:1024 sc1\n\t"
        "v_mfma_f32_16x16x32_bf16 v[48:51], a[100:103], v[20:23], v[48:51]\n\t"
        "v_mfma_f32_16x16x32_bf16 v[52:55], a[104:107], v[20:23], v[52:55]\n\t"
        "v_mfma_f32_16x16x32_bf16 v[56:59], a[108:111], v[20:23], v[56:59]\n\t"
        "ds_write_b128 %[gxw1], v[84:87] offset:9216\n\t"
        "v_mfma_f32_16x16x32_bf16 v[60:63], a[112:115], v[20:23], v[60:63]\n\t"
        "v_mfma_f32_16x16x32_bf16 v[64:67], a[116:119], v[20:23], v[64:67]\n\t"
        "buffer_load_dwordx4 v[136:139], %[vl16], %[rws], %[t1] offen offset:2048 sc1\n\t"
        "s_waitcnt vmcnt(17)\n\t"
        "v_mfma_f32_16x16x32_bf16 v[48:51], a[120:123], v[24:27], v[48:51]\n\t"
        "v_mfma_f32_16x16x32_bf16 v[52:55], a[124:127], v[24:27], v[52:55]\n\t"
        "v_mfma_f32_16x16x32_bf16 v[56:59], a[128:131], v[24:27], v[56:59]\n\t"
        "v_mfma_f32_16x16x32_bf16 v[60:63], a[132:135], v[24:27], v[60:63]\n\t"
        "v_mfma_f32_16x16x32_bf16 v[64:67], a[136:139], v[24:27], v[64:67]\n\t"
        "buffer_load_dwordx4 v[140:143], %[vl16], %[rws], %[t1] offen offset:3072 sc1\n\t"
        "s_waitcnt vmcnt(17)\n\t"
        "v_mfma_f32_16x16x32_bf16 v[48:51], a[140:143], v[28:31], v[48:51]\n\t"
        "v_mfma_f32_16x16x32_bf16 v[52:55], a[144:147], v[28:31], v[52:55]\n\t"
        "v_mfma_f32_16x16x32_bf16 v[56:59], a[148:151], v[28:31], v[56:59]\n\t"
        "v_mfma_f32_16x16x32_bf16 v[60:63], a[152:155], v[28:31], v[60:63]\n\t"
        "v_mfma_f32_16x16x32_bf16 v[64:67], a[156:159], v[28:31], v[64:67]\n\t"
        "buffer_load_dwordx2 v[204:205], %[polly], %[rws], 0 offen sc1\n\t"
        "s_waitcnt vmcnt(17)\n\t"
        "v_mfma_f32_16x16x32_bf16 v[48:51], a[160:163], v[32:35], v[48:51]\n\t"
        "v_mfma_f32_16x16x32_bf16 v[52:55], a[164:167], v[32:35], v[52:55]\n\t"
        "v_mfma_f32_16x16x32_bf16 v[56:59], a[168:171], v[32:35], v[56:59]\n\t"
        "v_mfma_f32_16x16x32_bf16 v[60:63], a[172:175], v[32:35], v[60:63]\n\t"
        "v_mfma_f32_16x16x32_bf16 v[64:67], a[176:179], v[32:35], v[64:67]\n\t"
        "s_waitcnt vmcnt(16)\n\t"
        "v_mfma_f32_16x16x32_bf16 v[48:51], a[180:183], v[36:39], v[48:51]\n\t"
        "v_mfma_f32_16x16x32_bf16 v[52:55], a[184:187], v[36:39], v[52:55]\n\t"
        "v_mfma_f32_16x16x32_bf16 v[56:59], a[188:191], v[36:39], v[56:59]\n\t"
        "v_mfma_f32_16x16x32_bf16 v[60:63], a[192:195], v[36:39], v[60:63]\n\t"
        "v_mfma_f32_16x16x32_bf16 v[64:67], a[196:199], v[36:39], v[64:67]\n\t"
        "s_waitcnt vmcnt(15)\n\t"
        "v_mfma_f32_16x16x32_bf16 v[48:51], a[200:203], v[40:43], v[48:51]\n\t"
        "v_mfma_f32_16x16x32_bf16 v[52:55], a[204:207], v[40:43], v[52:55]\n\t"
        "v_mfma_f32_16x16x32_bf16 v[56:59], a[208:211], v[40:43], v[56:59]\n\t"
        "v_mfma_f32_16x16x32_bf16 v[60:63], a[212:215], v[40:43], v[60:63]\n\t"
        "v_mfma_f32_16x16x32_bf16 v[64:67], a[216:219], v[40:43], v[64:67]\n\t"
        "s_waitcnt vmcnt(14)\n\t"
        "v_mfma_f32_16x16x32_bf16 v[48:51], a[220:223], v[44:47], v[48:51]\n\t"
        "v_mfma_f32_16x16x32_bf16 v[52:55], a[224:227], v[44:47], v[52:55]\n\t"
        "v_mfma_f32_16x16x32_bf16 v[56:59], a[228:231], v[44:47], v[56:59]\n\t"
        "v_mfma_f32_16x16x32_bf16 v[60:63], a[232:235], v[44:47], v[60:63]\n\t"
        "v_mfma_f32_16x16x32_bf16 v[64:67], a[236:239], v[44:47], v[64:67]\n\t"
        "s_waitcnt vmcnt(13)\n\t"
        "v_mfma_f32_16x16x32_bf16 v[68:71], a[0:3], v[96:99], 0\n\t"
        "s_waitcnt vmcnt(12)\n\t"
        "v_mfma_f32_16x16x32_bf16 v[68:71], a[20:23], v[100:103], v[68:71]\n\t"
        "s_waitcnt vmcnt(11)\n\t"
        "v_mfma_f32_16x16x32_bf16 v[68:71], a[40:43], v[104:107], v[68:71]\n\t"
        "s_waitcnt vmcnt(10)\n\t"
        "v_mfma_f32_16x16x32_bf16 v[68:71], a[60:63], v[108:111], v[68:71]\n\t"
        "ds_write_b128 %[gxw2], v[48:51] offset:10240\n\t"
        "s_waitcnt vmcnt(9)\n\t"
        "v_mfma_f32_16x16x32_bf16 v[68:71], a[80:83], v[112:115], v[68:71]\n\t"
        "s_waitcnt vmcnt(8)\n\t"
        "v_mfma_f32_16x16x32_bf16 v[68:71], a[100:103], v[116:119], v[68:71]\n\t"
        "ds_write_b128 %[gxw2], v[52:55] offset:11264\n\t"
        "s_waitcnt vmcnt(6)\n\t"
        "v_mfma_f32_16x16x32_bf16 v[68:71], a[120:123], v[120:123], v[68:71]\n\t"
        "s_waitcnt vmcnt(5)\n\t"
        "v_mfma_f32_16x16x32_bf16 v[68:71], a[140:143], v[124:127], v[68:71]\n\t"
        "ds_write_b128 %[gxw2], v[56:59] offset:12288\n\t"
        "s_waitcnt vmcnt(4)\n\t"
        "v_mfma_f32_16x16x32_bf16 v[68:71], a[160:163], v[128:131], v[68:71]\n\t"
        "s_waitcnt vmcnt(3)\n\t"
        "v_mfma_f32_16x16x32_bf16 v[68:71], a[180:183], v[132:135], v[68:71]\n\t"
        "s_waitcnt vmcnt(0)\n\t"
        "v_cmp_ne_u32 vcc, %[elo], v204\n\t"
        "v_cmp_ne_u32 %[tm], %[ehi], v205\n\t"
        "s_or_b64 vcc, vcc, %[tm]\n\t"
        "s_cbranch_vccz Lok4_%=\n\t"
        "s_mov_b32 %[t1], 0\n\t"
        "Lsp4_%=:\n\t"
        "buffer_load_dwordx2 v[204:205], %[polly], %[rws], 0 offen sc1\n\t"
        "s_waitcnt vmcnt(0)\n\t"
        "v_cmp_ne_u32 vcc, %[elo], v204\n\t"
        "v_cmp_ne_u32 %[tm], %[ehi], v205\n\t"
        "s_or_b64 vcc, vcc, %[tm]\n\t"
        "s_cbranch_vccz Lok4_%=\n\t"
        "s_sleep 2\n\t"
        "s_add_u32 %[t1], %[t1], 1\n\t"
        "s_cmp_lt_u32 %[t1], 0x40000\n\t"
        "s_cbranch_scc1 Lsp4_%=\n\t"
        "Lok4_%=:\n\t"
        "s_mov_b32 m0, %[ym0]\n\t"
        "s_nop 0\n\t"
        "buffer_load_dwordx4 %[vyt], %[rws], %[ys0] offen sc1 lds\n\t"
        "s_mov_b32 m0, %[ym1]\n\t"
        "s_nop 0\n\t"
        "buffer_load_dwordx4 %[vyt], %[rws], %[ys1] offen sc1 lds\n\t"
        "s_mov_b32 m0, %[ym2]\n\t"
        "s_nop 0\n\t"
        "buffer_load_dwordx4 %[vyt], %[rws], %[ys2] offen sc1 lds\n\t"
        "ds_write_b128 %[gxw2], v[60:63] offset:13312\n\t"
        "v_mfma_f32_16x16x32_bf16 v[68:71], a[200:203], v[136:139], v[68:71]\n\t"
        "v_mfma_f32_16x16x32_bf16 v[68:71], a[220:223], v[140:143], v[68:71]\n\t"
        "ds_write_b128 %[gxw2], v[64:67] offset:14336\n\t"
        "v_mfma_f32_16x16x32_bf16 v[72:75], a[4:7], v[96:99], 0\n\t"
        "v_mfma_f32_16x16x32_bf16 v[72:75], a[24:27], v[100:103], v[72:75]\n\t"
        "v_mfma_f32_16x16x32_bf16 v[72:75], a[44:47], v[104:107], v[72:75]\n\t"
        "s_waitcnt lgkmcnt(0)\n\t"
        "s_barrier\n\t"
        "v_mfma_f32_16x16x32_bf16 v[72:75], a[64:67], v[108:111], v[72:75]\n\t"
        "ds_read_b128 v[48:51], %[aGr] offset:0\n\t"
        "ds_read_b128 v[0:3], %[aGr] offset:5120\n\t"
        "v_mfma_f32_16x16x32_bf16 v[72:75], a[84:87], v[112:115], v[72:75]\n\t"
        "ds_read_b128 v[20:23], %[aGr] offset:10240\n\t"
        "ds_read_b128 v[52:55], %[aGr] offset:1024\n\t"
        "v_mfma_f32_16x16x32_bf16 v[72:75], a[104:107], v[116:119], v[72:75]\n\t"
        "ds_read_b128 v[4:7], %[aGr] offset:6144\n\t"
        "ds_read_b128 v[24:27], %[aGr] offset:11264\n\t"
        "v_mfma_f32_16x16x32_bf16 v[72:75], a[124:127], v[120:123], v[72:75]\n\t"
        "ds_read_b128 v[56:59], %[aGr] offset:2048\n\t"
        "s_waitcnt lgkmcnt(5)\n\t"
        "v_add_f32 v48, v48, v0\n\t"
        "v_add_f32 v49, v49, v1\n\t"
        "v_mfma_f32_16x16x32_bf16 v[72:75], a[144:147], v[124:127], v[72:75]\n\t"
        "ds_read_b128 v[8:11], %[aGr] offset:7168\n\t"
        "v_add_f32 v50, v50, v2\n\t"
        "v_add_f32 v51, v51, v3\n\t"
        "v_mfma_f32_16x16x32_bf16 v[72:75], a[164:167], v[128:131], v[72:75]\n\t"
        "ds_read_b128 v[28:31], %[aGr] offset:12288\n\t"
        "s_waitcnt lgkmcnt(6)\n\t"
        "v_add_f32 v48, v48, v20\n\t"
        "v_add_f32 v49, v49, v21\n\t"
        "v_mfma_f32_16x16x32_bf16 v[72:75], a[184:187], v[132:135], v[72:75]\n\t"
        "ds_read_b128 v[60:63], %[aGr] offset:3072\n\t"
        "v_add_f32 v50, v50, v22\n\t"
        "v_add_f32 v51, v51, v23\n\t"
        "v_mfma_f32_16x16x32_bf16 v[72:75], a[204:207], v[136:139], v[72:75]\n\t"
        "ds_read_b128 v[12:15], %[aGr] offset:8192\n\t"
        "s_waitcnt lgkmcnt(6)\n\t"
        "v_add_f32 v52, v52, v4\n\t"
        "v_add_f32 v53, v53, v5\n\t"
        "v_mfma_f32_16x16x32_bf16 v[72:75], a[224:227], v[140:143], v[72:75]\n\t"
        "ds_read_b128 v[32:35], %[aGr] offset:13312\n\t"
        "v_add_f32 v54, v54, v6\n\t"
        "v_add_f32 v55, v55, v7\n\t"
        "v_mfma_f32_16x16x32_bf16 v[76:79], a[8:11], v[96:99], 0\n\t"
        "ds_read_b128 v[64:67], %[aGr] offset:4096\n\t"
        "s_waitcnt lgkmcnt(7)\n\t"
        "v_add_f32 v52, v52, v24\n\t"
        "v_add_f32 v53, v53, v25\n\t"
        "v_mfma_f32_16x16x32_bf16 v[76:79], a[28:31], v[100:103], v[76:79]\n\t"
        "ds_read_b128 v[16:19], %[aGr] offset:9216\n\t"
        "v_add_f32 v54, v54, v26\n\t"
        "v_add_f32 v55, v55, v27\n\t"
        "v_mfma_f32_16x16x32_bf16 v[76:79], a[48:51], v[104:107], v[76:79]\n\t"
        "ds_read_b128 v[36:39], %[aGr] offset:14336\n\t"
        "s_waitcnt lgkmcnt(7)\n\t"
        "v_add_f32 v56, v56, v8\n\t"
        "v_add_f32 v57, v57, v9\n\t"
        "v_mfma_f32_16x16x32_bf16 v[76:79], a[68:71], v[108:111], v[76:79]\n\t"
        "v_add_f32 v58, v58, v10\n\t"
        "v_add_f32 v59, v59, v11\n\t"
        "s_waitcnt lgkmcnt(6)\n\t"
        "v_add_f32 v56, v56, v28\n\t"
        "v_add_f32 v57, v57, v29\n\t"
        "v_mfma_f32_16x16x32_bf16 v[76:79], a[88:91], v[112:115], v[76:79]\n\t"
        "v_add_f32 v58, v58, v30\n\t"
        "v_add_f32 v59, v59, v31\n\t"
        "s_waitcnt lgkmcnt(4)\n\t"
        "v_add_f32 v60, v60, v12\n\t"
        "v_add_f32 v61, v61, v13\n\t"
        "v_mfma_f32_16x16x32_bf16 v[76:79], a[108:111], v[116:119], v[76:79]\n\t"
        "v_add_f32 v62, v62, v14\n\t"
        "v_add_f32 v63, v63, v15\n\t"
        "s_waitcnt lgkmcnt(3)\n\t"
        "v_add_f32 v60, v60, v32\n\t"
        "v_add_f32 v61, v61, v33\n\t"
        "v_mfma_f32_16x16x32_bf16 v[76:79], a[128:131], v[120:123], v[76:79]\n\t"
        "v_add_f32 v62, v62, v34\n\t"
        "v_add_f32 v63, v63, v35\n\t"
        "s_waitcnt lgkmcnt(1)\n\t"
        "v_add_f32 v64, v64, v16\n\t"
        "v_add_f32 v65, v65, v17\n\t"
        "v_mfma_f32_16x16x32_bf16 v[76:79], a[148:151], v[124:127], v[76:79]\n\t"
        "v_add_f32 v66, v66, v18\n\t"
        "v_add_f32 v67, v67, v19\n\t"
        "s_waitcnt lgkmcnt(0)\n\t"
        "v_add_f32 v64, v64, v36\n\t"
        "v_add_f32 v65, v65, v37\n\t"
        "v_mfma_f32_16x16x32_bf16 v[76:79], a[168:171], v[128:131], v[76:79]\n\t"
        "v_add_f32 v66, v66, v38\n\t"
        "v_add_f32 v67, v67, v39\n\t"
        "v_mfma_f32_16x16x32_bf16 v[76:79], a[188:191], v[132:135], v[76:79]\n\t"
        "v_add_f32 v68, v68, v48\n\t"
        "v_add_f32 v69, v69, v49\n\t"
        "v_add_f32 v70, v70, v50\n\t"
        "v_add_f32 v71, v71, v51\n\t"
        "v_mfma_f32_16x16x32_bf16 v[76:79], a[208:211], v[136:139], v[76:79]\n\t"
        "v_fma_f32 v68, v68, v88, v164\n\t"
        "v_fma_f32 v69, v69, v88, v165\n\t"
        "v_fma_f32 v70, v70, v88, v166\n\t"
        "v_fma_f32 v71, v71, v88, v167\n\t"
        "v_mfma_f32_16x16x32_bf16 v[76:79], a[228:231], v[140:143], v[76:79]\n\t"
        "v_add_f32 v72, v72, v52\n\t"
        "v_add_f32 v73, v73, v53\n\t"
        "v_add_f32 v74, v74, v54\n\t"
        "v_add_f32 v75, v75, v55\n\t"
        "v_mfma_f32_16x16x32_bf16 v[80:83], a[12:15], v[96:99], 0\n\t"
        "v_fma_f32 v72, v72, v88, v168\n\t"
        "v_fma_f32 v73, v73, v88, v169\n\t"
        "v_fma_f32 v74, v74, v88, v170\n\t"
        "v_fma_f32 v75, v75, v88, v171\n\t"
        "v_mfma_f32_16x16x32_bf16 v[80:83], a[32:35], v[100:103], v[80:83]\n\t"
        "v_fma_f32 v60, v60, v88, v176\n\t"
        "v_fma_f32 v61, v61, v88, v177\n\t"
        "v_fma_f32 v62, v62, v88, v178\n\t"
        "v_fma_f32 v63, v63, v88, v179\n\t"
        "v_mfma_f32_16x16x32_bf16 v[80:83], a[52:55], v[104:107], v[80:83]\n\t"
        "v_fma_f32 v64, v64, v88, v180\n\t"
        "v_fma_f32 v65, v65, v88, v181\n\t"
        "v_fma_f32 v66, v66, v88, v182\n\t"
        "v_fma_f32 v67, v67, v88, v183\n\t"
        "v_mfma_f32_16x16x32_bf16 v[80:83], a[72:75], v[108:111], v[80:83]\n\t"
        "v_add_f32 v76, v76, v56\n\t"
        "v_add_f32 v77, v77, v57\n\t"
        "v_add_f32 v78, v78, v58\n\t"
        "v_add_f32 v79, v79, v59\n\t"
        "v_mfma_f32_16x16x32_bf16 v[80:83], a[92:95], v[112:115], v[80:83]\n\t"
        "v_fma_f32 v76, v76, v88, v172\n\t"
        "v_fma_f32 v77, v77, v88, v173\n\t"
        "v_fma_f32 v78, v78, v88, v174\n\t"
        "v_fma_f32 v79, v79, v88, v175\n\t"
        "v_mfma_f32_16x16x32_bf16 v[80:83], a[112:115], v[116:119], v[80:83]\n\t"
        "v_mfma_f32_16x16x32_bf16 v[80:83], a[132:135], v[120:123], v[80:83]\n\t"
        "v_mfma_f32_16x16x32_bf16 v[80:83], a[152:155], v[124:127], v[80:83]\n\t"
        "v_mfma_f32_16x16x32_bf16 v[80:83], a[172:175], v[128:131], v[80:83]\n\t"
        "v_mfma_f32_16x16x32_bf16 v[80:83], a[192:195], v[132:135], v[80:83]\n\t"
        "s_waitcnt vmcnt(0)\n\t"
        "s_waitcnt lgkmcnt(0)\n\t"
        "s_barrier\n\t"
        "ds_read_b64_tr_b16 v[0:1], %[aYT] offset:0\n\t"
        "ds_read_b64_tr_b16 v[2:3], %[aYT] offset:2048\n\t"
        "ds_read_b64_tr_b16 v[4:5], %[aYT] offset:4096\n\t"
        "ds_read_b64_tr_b16 v[6:7], %[aYT] offset:6144\n\t"
        "ds_read_b64_tr_b16 v[8:9], %[aYT] offset:8192\n\t"
        "v_bfe_u32 v46, %[vl16], 8, 1\n\t"
        "v_mad_u32_u24 v46, v46, 24, %[vq]\n\t"
        "s_bitcmp1_b32 %[n2], 0\n\t"
        "v_mfma_f32_16x16x32_bf16 v[80:83], a[212:215], v[136:139], v[80:83]\n\t"
        "s_waitcnt lgkmcnt(4)\n\t"
        "v_lshlrev_b32 v12, 16, v0\n\t"
        "v_and_b32 v13, 0xffff0000, v0\n\t"
        "v_lshlrev_b32 v14, 16, v1\n\t"
        "v_and_b32 v15, 0xffff0000, v1\n\t"
        "v_mfma_f32_16x16x32_bf16 v[80:83], a[232:235], v[140:143], v[80:83]\n\t"
        "v_fma_f32 v68, v144, v12, v68\n\t"
        "v_fma_f32 v69, v145, v13, v69\n\t"
        "v_fma_f32 v70, v146, v14, v70\n\t"
        "v_fma_f32 v71, v147, v15, v71\n\t"
        "v_mfma_f32_16x16x32_bf16 v[84:87], a[16:19], v[96:99], 0\n\t"
        "v_cvt_pk_bf16_f32 v36, v68, v69\n\t"
        "v_cvt_pk_bf16_f32 v37, v70, v71\n\t"
        "s_cbranch_scc0 Lop0_%=\n\t"
        "global_store_dwordx2 %[vq], v[36:37], %[out] offset:0 sc1\n\t"
        "Lop0_%=:\n\t"
        "s_waitcnt lgkmcnt(3)\n\t"
        "v_lshlrev_b32 v16, 16, v2\n\t"
        "v_mfma_f32_16x16x32_bf16 v[84:87], a[36:39], v[100:103], v[84:87]\n\t"
        "v_and_b32 v17, 0xffff0000, v2\n\t"
        "v_lshlrev_b32 v18, 16, v3\n\t"
        "v_and_b32 v19, 0xffff0000, v3\n\t"
        "v_fma_f32 v72, v148, v16, v72\n\t"
        "v_mfma_f32_16x16x32_bf16 v[84:87], a[56:59], v[104:107], v[84:87]\n\t"
        "v_fma_f32 v73, v149, v17, v73\n\t"
        "v_fma_f32 v74, v150, v18, v74\n\t"
        "v_fma_f32 v75, v151, v19, v75\n\t"
        "v_cvt_pk_bf16_f32 v38, v72, v73\n\t"
        "v_mfma_f32_16x16x32_bf16 v[84:87], a[76:79], v[108:111], v[84:87]\n\t"
        "v_cvt_pk_bf16_f32 v39, v74, v75\n\t"
        "s_cbranch_scc1 Lop1_%=\n\t"
        "s_nop 1\n\t"
        "v_permlane16_swap_b32 v36, v38\n\t"
        "v_permlane16_swap_b32 v37, v39\n\t"
        "global_store_dwordx4 v46, v[36:39], %[out] offset:0 sc1\n\t"
        "Lop1_%=:\n\t"
        "s_waitcnt lgkmcnt(2)\n\t"
        "v_lshlrev_b32 v12, 16, v4\n\t"
        "v_and_b32 v13, 0xffff0000, v4\n\t"
        "v_mfma_f32_16x16x32_bf16 v[84:87], a[96:99], v[112:115], v[84:87]\n\t"
        "v_lshlrev_b32 v14, 16, v5\n\t"
        "v_and_b32 v15, 0xffff0000, v5\n\t"
        "v_fma_f32 v76, v152, v12, v76\n\t"
        "v_fma_f32 v77, v153, v13, v77\n\t"
        "v_mfma_f32_16x16x32_bf16 v[84:87], a[116:119], v[116:119], v[84:87]\n\t"
        "v_fma_f32 v78, v154, v14, v78\n\t"
        "v_fma_f32 v79, v155, v15, v79\n\t"
        "v_cvt_pk_bf16_f32 v40, v76, v77\n\t"
        "v_cvt_pk_bf16_f32 v41, v78, v79\n\t"
        "v_mfma_f32_16x16x32_bf16 v[84:87], a[136:139], v[120:123], v[84:87]\n\t"
        "s_cbranch_scc0 Lop2_%=\n\t"
        "s_nop 1\n\t"
        "v_permlane16_swap_b32 v38, v40\n\t"
        "v_permlane16_swap_b32 v39, v41\n\t"
        "global_store_dwordx4 v46, v[38:41], %[out] offset:32 sc1\n\t"
        "Lop2_%=:\n\t"
        "s_waitcnt lgkmcnt(1)\n\t"
        "v_lshlrev_b32 v16, 16, v6\n\t"
        "v_and_b32 v17, 0xffff0000, v6\n\t"
        "v_lshlrev_b32 v18, 16, v7\n\t"
        "v_mfma_f32_16x16x32_bf16 v[84:87], a[156:159], v[124:127], v[84:87]\n\t"
        "v_and_b32 v19, 0xffff0000, v7\n\t"
        "v_fma_f32 v60, v156, v16, v60\n\t"
        "v_fma_f32 v61, v157, v17, v61\n\t"
        "v_fma_f32 v62, v158, v18, v62\n\t"
        "v_mfma_f32_16x16x32_bf16 v[84:87], a[176:179], v[128:131], v[84:87]\n\t"
        "v_fma_f32 v63, v159, v19, v63\n\t"
        "s_waitcnt lgkmcnt(0)\n\t"
        "v_lshlrev_b32 v12, 16, v8\n\t"
        "v_and_b32 v13, 0xffff0000, v8\n\t"
        "v_lshlrev_b32 v14, 16, v9\n\t"
        "v_mfma_f32_16x16x32_bf16 v[84:87], a[196:199], v[132:135], v[84:87]\n\t"
        "v_and_b32 v15, 0xffff0000, v9\n\t"
        "v_fma_f32 v64, v160, v12, v64\n\t"
        "v_fma_f32 v65, v161, v13, v65\n\t"
        "v_fma_f32 v66, v162, v14, v66\n\t"
        "v_mfma_f32_16x16x32_bf16 v[84:87], a[216:219], v[136:139], v[84:87]\n\t"
        "v_fma_f32 v67, v163, v15, v67\n\t"
        "v_fma_f32 v80, v80, v88, v60\n\t"
        "v_fma_f32 v81, v81, v88, v61\n\t"
        "v_fma_f32 v82, v82, v88, v62\n\t"
        "v_mfma_f32_16x16x32_bf16 v[84:87], a[236:239], v[140:143], v[84:87]\n\t"
        "v_fma_f32 v83, v83, v88, v63\n\t"
        "v_cvt_pk_bf16_f32 v42, v80, v81\n\t"
        "v_cvt_pk_bf16_f32 v43, v82, v83\n\t"
        "s_cbranch_scc1 Lop3_%=\n\t"
        "s_nop 1\n\t"
        "v_permlane16_swap_b32 v40, v42\n\t"
        "v_permlane16_swap_b32 v41, v43\n\t"
        "global_store_dwordx4 v46, v[40:43], %[out] offset:64 sc1\n\t"
        "Lop3_%=:\n\t"
        "s_nop 7\n\t"
        "v_pk_fma_f32 v[84:85], v[84:85], v[88:89], v[64:65] op_sel:[0,0,0] op_sel_hi:[1,0,1]\n\t"
        "v_pk_fma_f32 v[86:87], v[86:87], v[88:89], v[66:67] op_sel:[0,0,0] op_sel_hi:[1,0,1]\n\t"
        "v_cvt_pk_bf16_f32 v44, v84, v85\n\t"
        "v_cvt_pk_bf16_f32 v45, v86, v87\n\t"
        "s_cbranch_scc0 Lop4_%=\n\t"
        "s_nop 1\n\t"
        "v_permlane16_swap_b32 v42, v44\n\t"
        "v_permlane16_swap_b32 v43, v45\n\t"
        "global_store_dwordx4 v46, v[42:45], %[out] offset:96 sc1\n\t"
        "s_branch Lop5_%=\n\t"
        "Lop4_%=:\n\t"
        "global_store_dwordx2 %[vq], v[44:45], %[out] offset:128 sc1\n\t"
        "Lop5_%=:\n\t"
        : "+{v[0:3]}"(acc4[0]), "+{v[4:7]}"(acc4[1]), "+{v[8:11]}"(acc4[2]), "+{v[12:15]}"(acc4[3]), "+{v[16:19]}"(acc4[4]), "+{v[20:23]}"(acc4[5]), "+{v[24:27]}"(acc4[6]), "+{v[28:31]}"(acc4[7]), "+{v[32:35]}"(acc4[8]), "+{v[36:39]}"(acc4[9]), "+{v[40:43]}"(acc4[10]), "+{v[44:47]}"(acc4[11]), [t0] "=&s"(bt0), [t1] "=&s"(bt1), [tm] "=&s"(btm), [okE] "=&s"(okE), "+{v[96:127]}"(FLR), "+{v[164:179]}"(CBV)
        : [aC0] "v"(aC0), [aC1] "v"(aC1), [aC2] "v"(aC2), [st0] "v"(st0), [paF] "v"(paF), [pbF] "v"(pbF), [paH] "v"(paH), [pbH] "v"(pbH), [vl16] "v"(vl16), [evv] "v"(evv), [vfH] "v"(vfH), [pollh] "v"(pollh), [vfY] "v"(vfY), [polly] "v"(polly), [aEx] "v"(aEx), [oY] "v"(oY), [ddF] "v"(ddF), [gxw0] "v"(gxw0), [gxw1] "v"(gxw1), [gxw2] "v"(gxw2), [vyt] "v"(vyt), [aGr] "v"(aGr), [aYT] "v"(aYT), [vq] "v"(vq), [aB2] "v"(aB2), [vPE] "v"(vPE), [vE] "v"(vE), [aQ0] "v"(aQ0), [aQ1] "v"(aQ1), [aQ2] "v"(aQ2), [aQ3] "v"(aQ3), [aQ4] "v"(aQ4), [st1] "v"(st1), [gate] "v"(gate_tok), [rws] "s"(rws), [w2o] "s"(w2o), [hoff0] "s"(hoff0), [hoff1] "s"(hoff1), [hoff2] "s"(hoff2), [hoff3] "s"(hoff3), [elo] "s"(elo), [ehi] "s"(ehi), [cc] "s"(cc), [out] "s"(out), [par] "s"(par), [w] "s"(w), [dY2] "s"(dY2), [ys0] "s"(ys0), [ys1] "s"(ys1), [ys2] "s"(ys2), [ym0] "s"(ym0), [ym1] "s"(ym1), [ym2] "s"(ym2), [n2] "s"(n2), [hs0] "s"(hs0), [hs1] "s"(hs1), [hs2] "s"(hs2), [aFc0] "v"(aFc[0]), [aFc1] "v"(aFc[1]), [aFc2] "v"(aFc[2]), [aPre] "v"(aPre), [aZ] "v"(aZ), [pref] "v"(pref)
        : "v48", "v49", "v50", "v51", "v52", "v53", "v54", "v55", "v56", "v57", "v58", "v59", "v60", "v61", "v62", "v63", "v64", "v65", "v66", "v67", "v68", "v69", "v70", "v71", "v72", "v73", "v74", "v75", "v76", "v77", "v78", "v79", "v80", "v81", "v82", "v83", "v84", "v85", "v86", "v87", "v88", "v89", "v90", "v91", "v92", "v93", "v94", "v95", "v128", "v129", "v130", "v131", "v132", "v133", "v134", "v135", "v136", "v137", "v138", "v139", "v140", "v141", "v142", "v143", "v144", "v145", "v146", "v147", "v148", "v149", "v150", "v151", "v152", "v153", "v154", "v155", "v156", "v157", "v158", "v159", "v160", "v161", "v162", "v163", "v180", "v181", "v182", "v183", "v184", "v185", "v186", "v187", "v188", "v189", "v190", "v191", "v192", "v193", "v194", "v195", "v196", "v197", "v198", "v199", "v200", "v201", "v202", "v203", "v204", "v205", "v206", "v207", "vcc", "scc", "memory", "a240", "a246", "a247", "a248", "a249", "a250", "a251", "a252", "a253", "a254", "a255");

    }
    const u32 lane2 = __builtin_amdgcn_mbcnt_hi(~0u, __builtin_amdgcn_mbcnt_lo(~0u, 0u));
    if (blk == 0 && w == 0 && okE == 0) {
        sfor<4>([&](auto J) { spin_flags(rws, WS_FP + (lane2 * 4 + J) * 8, elo, ehi, 4); });
        const u32x2 ev2 = {elo, ehi};
        ST64(ev2, lane2 == 0 ? WS_E : OOB);
    }
}

extern "C" void kernel_launch(void* const* d_in, const int* in_sizes, int n_in, void* d_out, int out_size, void* d_ws, size_t ws_size, hipStream_t stream) {
    auto desc = [](const void* p, u32 bytes) { const u64 a = (u64)(size_t)p; u32x4 d; d[0] = (u32)a; d[1] = (u32)(a >> 32); d[2] = bytes; d[3] = 0x00020000u; return d; };
    Args a;
    a.dq = desc(d_in[0], S * D * 2); a.dk = desc(d_in[1], S * D * 2); a.dv = desc(d_in[2], S * D * 2);
    a.dw1 = desc(d_in[7], D * F * 2); a.dw2 = desc(d_in[9], F * D * 2); a.dsk = desc(d_in[4], D * S * 2); a.dws = desc(d_ws, WS_END);
    a.tap = (const u16*)d_in[3]; a.gw = (const u16*)d_in[11]; a.b1 = (const u16*)d_in[8]; a.b2 = (const u16*)d_in[10]; a.ssmD = (const u16*)d_in[5];
    a.out = (u16*)d_out;
    fused_kernel<<<256, 256, 0, stream>>>(a);
}
```
